```python
import torch

def _softmax(x, axis=-1):
    return torch.softmax(x, dim=axis)
def _gelu(x):
    return torch.nn.functional.gelu(x, approximate="tanh")
def _silu(x):
    return torch.nn.functional.silu(x)
def _sigmoid(x):
    return torch.sigmoid(x)
def _layernorm(x, axis=-1, eps=1e-5):
    m = x.mean(dim=axis, keepdim=True)
    v = x.var(dim=axis, keepdim=True, unbiased=False)
    return (x - m) / torch.sqrt(v + eps)
def _scatter_add(tgt, idx, src, axis=0):
    return tgt.clone().index_add_(axis, idx, src)
def _segment_sum(src, seg, n):
    out = torch.zeros((n,) + src.shape[1:], dtype=src.dtype)
    return out.index_add_(0, seg, src)
def _pad(x, pw):
    flat = tuple(v for pair in reversed(pw) for v in pair)
    return torch.nn.functional.pad(x, flat)

kernel_name = 'parallel_sum__sliding_local_dilated__mixer_token_mlp__prompt'

def setup_inputs():

    _w = (lambda a: (lambda t: t.to(torch.bfloat16) if t.is_floating_point() else t)(torch.as_tensor(a)))
    def _A_setup():
        B, S, D = 1, 2048, 448
        return {
            "x": _w((lambda *s: torch.randn(*s, dtype=torch.bfloat16) * 0.05)(B, S, D)),
            "wqkv": _w((lambda *s: torch.randn(*s, dtype=torch.bfloat16) * 0.05)(D, 3 * D)),
            "wo": _w((lambda *s: torch.randn(*s, dtype=torch.bfloat16) * 0.05)(D, D)),
        }
    _A_in = _A_setup()

    def _B_setup():
        B, S, D, F = 1, 2048, 448, 1792
        return {
            "x": _w((lambda *s: torch.randn(*s, dtype=torch.bfloat16) * 0.05)(B, S, D)),
            "w1": _w((lambda *s: torch.randn(*s, dtype=torch.bfloat16) * 1.6)(S, F) * (S ** -0.5)),
            "b1": _w((lambda *s: torch.randn(*s, dtype=torch.bfloat16) * 12.8)(F) * 0.02),
            "w2": _w((lambda *s: torch.randn(*s, dtype=torch.bfloat16) * 0.4)(F, S) * (F ** -0.5)),
            "b2": _w((lambda *s: torch.randn(*s, dtype=torch.bfloat16) * 6.4)(S) * 0.02),
        }
    _B_in = _B_setup()

    return {
        "A_x": _A_in["x"],
        "A_wqkv": _A_in["wqkv"],
        "A_wo": _A_in["wo"],
        "B_x": _B_in["x"],
        "B_w1": _B_in["w1"],
        "B_b1": _B_in["b1"],
        "B_w2": _B_in["w2"],
        "B_b2": _B_in["b2"],
    }

def reference(A_x, A_wqkv, A_wo, B_x, B_w1, B_b1, B_w2, B_b2):
    def _A_ref(x, wqkv, wo):
        B, S, D = x.shape
        H = 16; Dh = D // H
        W, dil = 8, 2
        qkv = (lambda x, *d: x.permute(*d))((x @ wqkv).reshape(B, S, 3, H, Dh), 2, 0, 3, 1, 4)
        q, k, v = qkv[0], qkv[1], qkv[2]
        scale = Dh ** -0.5
        pos = torch.arange(S)
        off = torch.arange(W) * dil
        idx = pos[:, None] - off[None, :]
        mask = idx >= 0
        idx_c = torch.where(mask, idx, 0).reshape(-1)
        kg = k[:, :, idx_c, :].reshape(B, H, S, W, Dh)
        vg = v[:, :, idx_c, :].reshape(B, H, S, W, Dh)
        logits = torch.einsum("bhsd,bhswd->bhsw", q, kg) * scale
        logits = torch.where(mask[None, None, :, :], logits, -1e9)
        attn = _softmax(logits, axis=-1)
        o = torch.einsum("bhsw,bhswd->bhsd", attn, vg)
        return (lambda x, *d: x.permute(*d))(o, 0, 2, 1, 3).reshape(B, S, D) @ wo

    def _B_ref(x, w1, b1, w2, b2):
        B, S, D = x.shape
        h = _layernorm(x, axis=-1)
        u = torch.einsum("bsd,sf->bdf", h, w1) + b1
        u = _gelu(u)
        y = torch.einsum("bdf,fs->bds", u, w2) + b2
        return (lambda x, *d: x.permute(*d))(y, 0, 2, 1)

    x = A_x
    xn = _layernorm(x, axis=-1)
    return x + _A_ref(xn, A_wqkv, A_wo) + _B_ref(xn, B_w1, B_b1, B_w2, B_b2)
```

```cpp
#define ADLY 16
#define GDLY 4
#define HSTAMP 0
#define HCOND xcd==0&&w==0
#include <hip/hip_runtime.h>
#include <stdint.h>
#include <type_traits>
#include <utility>

#define __float_as_uint(x) __builtin_bit_cast(unsigned, (float)(x))
#define __uint_as_float(x) __builtin_bit_cast(float, (unsigned)(x))
#define __all(p) (__builtin_amdgcn_ballot_w64(!(p)) == 0)
typedef unsigned short u16;
typedef unsigned long long u64;
using bfv8 = __attribute__((ext_vector_type(8))) __bf16;
using bfv2 = __attribute__((ext_vector_type(2))) __bf16;
using f32x4 = __attribute__((ext_vector_type(4))) float;
using f32x2 = __attribute__((ext_vector_type(2))) float;
using u32x2 = __attribute__((ext_vector_type(2))) unsigned int;
using u32x4 = __attribute__((ext_vector_type(4))) unsigned int;
using u32x16 = __attribute__((ext_vector_type(16))) unsigned int;
using s16x4 = __attribute__((ext_vector_type(4))) short;

#define LAS __attribute__((address_space(3)))
#define GAS __attribute__((address_space(1)))
#define AI __attribute__((always_inline))
#define SB() __builtin_amdgcn_sched_barrier(0)
#define SGB(mask, n) __builtin_amdgcn_sched_group_barrier(mask, n, 0)
#define DRAIN() asm volatile("s_waitcnt vmcnt(0)" ::: "memory")
#define WAITVM(n) asm volatile("s_waitcnt vmcnt(%0)" ::"n"(n) : "memory")
#define WAITLG() asm volatile("s_waitcnt lgkmcnt(0)" ::: "memory")
#define RLX_AGENT __ATOMIC_RELAXED, __HIP_MEMORY_SCOPE_AGENT
#define PRIO(n) __builtin_amdgcn_s_setprio(n)

#define RQ1 2
#define RQ2 5
#define PLP 0
#define PEP 1
#define PE2 2
#define PL2 1
#define PP2 0
#define SX 1
#define WST 7
#define WPI 1
#define WN 7
#define WORD 0
#define WRM 1
#define NB2L 3
#define IL 1
#define LA0 6
#define LA1 6
#define W0 0
#define HK1 1
#define PE1 1
#define PL1 2
#define NB1 8
#define NF0 2
#define PKE 1
#define NSG 2
#define NE 2
#define WSL 1
#define TP 20
#define SG2 0
#define SLP 4
#define OV 0
constexpr int S_ = 2048, D_ = 448, F_ = 1792, N3_ = 1344;
constexpr int AUX_SC1 = 16;
constexpr int RSRC_W3 = 0x00020000;

constexpr int FS = 128;
constexpr int WS_E = 0, WS_HF = 4096, WS_UF = WS_HF + 128 * FS, WS_OF = WS_UF + 168 * FS;
constexpr int WS_ST = 65536;
constexpr int KS_ = 28 * 1024;
constexpr int WS_H = 131072;
constexpr int WS_U = WS_H + 64 * KS_;
constexpr int WS_O = WS_U + 56 * KS_;
constexpr int WS_END = WS_O + 132 * 16384;

constexpr int L_LATE = 65536, SS = 16384;
constexpr int L_HB = 131072;
constexpr int L_CNT = 162176;
constexpr int L_SA = 162432;
constexpr int L_DUMMY = 162560;
constexpr int LDS_BYTES = 163840;
constexpr int HB_PITCH = 928, AT_PITCH = 144;

__device__ __forceinline__ unsigned pack2(float a, float b) {
  bfv2 v; v[0] = (__bf16)a; v[1] = (__bf16)b;
  return __builtin_bit_cast(unsigned, v);
}
__device__ __forceinline__ float bfr(float f) { return __uint_as_float(pack2(f, 0.f) << 16); }
__device__ __forceinline__ float lo_f(unsigned u) { return __uint_as_float(u << 16); }
__device__ __forceinline__ float hi_f(unsigned u) { return __uint_as_float(u & 0xffff0000u); }
__device__ __forceinline__ float dot2(unsigned a, unsigned b, float c) {
  return __builtin_amdgcn_fdot2_f32_bf16(__builtin_bit_cast(bfv2, a), __builtin_bit_cast(bfv2, b), c, false);
}
__device__ __forceinline__ f32x4 mfma16(u32x4 a, u32x4 b, f32x4 c) {
  return __builtin_amdgcn_mfma_f32_16x16x32_bf16(__builtin_bit_cast(bfv8, a), __builtin_bit_cast(bfv8, b), c, 0, 0, 0);
}
__device__ __forceinline__ float gsum(float v) {
  unsigned u = __float_as_uint(v);
  auto a = __builtin_amdgcn_permlane32_swap(u, u, false, false);
  float s = __uint_as_float(a[0]) + __uint_as_float(a[1]);
  u = __float_as_uint(s);
  auto b = __builtin_amdgcn_permlane16_swap(u, u, false, false);
  return __uint_as_float(b[0]) + __uint_as_float(b[1]);
}
__device__ __forceinline__ float max2(float a, float b) { return __builtin_elementwise_maximum(a, b); }
template <bool FIRST> __device__ __forceinline__ void mfma_ip(f32x4& d, u32x4 a, u32x4 b) {
  if constexpr (FIRST) asm volatile("v_mfma_f32_16x16x32_bf16 %0, %1, %2, 0" : "=&v"(d) : "v"(a), "v"(b));
  else asm volatile("v_mfma_f32_16x16x32_bf16 %0, %1, %2, %0" : "+v"(d) : "v"(a), "v"(b));
}
__device__ __forceinline__ float gmax(float v) {
  unsigned u = __float_as_uint(v);
  auto a = __builtin_amdgcn_permlane32_swap(u, u, false, false);
  float s = max2(__uint_as_float(a[0]), __uint_as_float(a[1]));
  u = __float_as_uint(s);
  auto b = __builtin_amdgcn_permlane16_swap(u, u, false, false);
  return max2(__uint_as_float(b[0]), __uint_as_float(b[1]));
}
template <int CTRL>
__device__ __forceinline__ float dpp_add(float v) {
  return v + __uint_as_float(__builtin_amdgcn_update_dpp(0u, __float_as_uint(v), CTRL, 0xf, 0xf, true));
}
__device__ __forceinline__ void dma16(__amdgpu_buffer_rsrc_t rs, int voff, int soff, int dst) {
  asm volatile("s_mov_b32 m0, %3\n\ts_nop 0\n\tbuffer_load_dwordx4 %0, %1, %2 offen lds" ::"v"(voff), "s"(rs), "s"(soff), "s"(dst) : "memory");
}
__device__ __forceinline__ void touch(__amdgpu_buffer_rsrc_t rs, int voff, int soff, int dst) {
  asm volatile("s_mov_b32 m0, %3\n\ts_nop 0\n\tbuffer_load_dword %0, %1, %2 offen lds" ::"v"(voff), "s"(rs), "s"(soff), "s"(dst) : "memory");
}
__device__ __forceinline__ void ldx4(u32x4 (&a)[4], __amdgpu_buffer_rsrc_t rs, int voff, int soff) {
  asm volatile("buffer_load_dwordx4 %0, %4, %5, %6 offen\n\tbuffer_load_dwordx4 %1, %4, %5, %6 offen offset:224\n\t"
               "buffer_load_dwordx4 %2, %4, %5, %6 offen offset:448\n\tbuffer_load_dwordx4 %3, %4, %5, %6 offen offset:672"
               : "=&v"(a[0]), "=&v"(a[1]), "=&v"(a[2]), "=&v"(a[3]) : "v"(voff), "s"(rs), "s"(soff) : "memory");
}
__device__ __forceinline__ void dma4(__amdgpu_buffer_rsrc_t rs, int v, int so, int step, int dst) {
  const int s1 = so + step - 1024, s2 = so + 2 * step - 2048, s3 = so + 3 * step - 3072;
  asm volatile("s_mov_b32 m0, %6\n\ts_nop 0\n\t"
               "buffer_load_dwordx4 %0, %1, %2 offen lds\n\t"
               "buffer_load_dwordx4 %0, %1, %3 offen offset:1024 lds\n\t"
               "buffer_load_dwordx4 %0, %1, %4 offen offset:2048 lds\n\t"
               "buffer_load_dwordx4 %0, %1, %5 offen offset:3072 lds"
               ::"v"(v), "s"(rs), "s"(so), "s"(s1), "s"(s2), "s"(s3), "s"(dst) : "memory");
}
__device__ __forceinline__ void ld4(u32x4 (&a)[4], __amdgpu_buffer_rsrc_t rs, int v, int so, int step) {
  asm volatile("buffer_load_dwordx4 %0, %4, %5, %6 offen\n\tbuffer_load_dwordx4 %1, %4, %5, %7 offen\n\t"
               "buffer_load_dwordx4 %2, %4, %5, %8 offen\n\tbuffer_load_dwordx4 %3, %4, %5, %9 offen"
               : "=&v"(a[0]), "=&v"(a[1]), "=&v"(a[2]), "=&v"(a[3])
               : "v"(v), "s"(rs), "s"(so), "s"(so + step), "s"(so + 2 * step), "s"(so + 3 * step) : "memory");
}
template <int N> __device__ __forceinline__ void wait4(u32x4 (&a)[4]) {
  asm volatile("s_waitcnt vmcnt(%4)" : "+v"(a[0]), "+v"(a[1]), "+v"(a[2]), "+v"(a[3]) : "n"(N) : "memory");
}
__device__ __forceinline__ void turn_pair(u32x4 (&a)[4], u32x4 (&b)[4]) {
  unsigned xa[8][2], xb[8][2];
#pragma unroll
  for (int r = 0; r < 8; ++r)
#pragma unroll
    for (int e = 0; e < 2; ++e) {
      const u32x4 v = r < 4 ? a[r] : b[r - 4];
      xa[r][e] = __builtin_amdgcn_update_dpp(v[e], v[2 + e], 0x128, 0xf, 0xc, false);
      xb[r][e] = __builtin_amdgcn_update_dpp(v[2 + e], v[e], 0x128, 0xf, 0x3, false);
    }
#pragma unroll
  for (int j = 0; j < 4; ++j)
#pragma unroll
    for (int q = 0; q < 4; ++q) {
      a[j][q] = __builtin_amdgcn_perm(xa[2 * q + 1][j >> 1], xa[2 * q][j >> 1], (j & 1) ? 0x07060302u : 0x05040100u);
      b[j][q] = __builtin_amdgcn_perm(xb[2 * q + 1][j >> 1], xb[2 * q][j >> 1], (j & 1) ? 0x07060302u : 0x05040100u);
    }
#pragma unroll
  for (int j = 0; j < 4; ++j) asm volatile("" : "+v"(a[j]), "+v"(b[j]));
}
__device__ __forceinline__ u32x2 tr_read(const LAS char* p) {
  return __builtin_bit_cast(u32x2, __builtin_amdgcn_ds_read_tr16_b64_v4i16((LAS s16x4*)p));
}
__device__ __forceinline__ float ln_sd(float var) { return bfr(__builtin_amdgcn_sqrtf(bfr(bfr(var) + 1e-5f))); }

__device__ __forceinline__ float row_sum(float v) {
  v = dpp_add<0xB1>(v);
  v = dpp_add<0x4E>(v);
  v = dpp_add<0x141>(v);
  return dpp_add<0x140>(v);
}
__device__ __forceinline__ unsigned ln2_rows(u32x4 (&f)[4], bool act) {
  f32x2 d[16];
#pragma unroll
  for (int e = 0; e < 16; ++e) d[e] = f32x2{lo_f(f[e >> 2][e & 3]), hi_f(f[e >> 2][e & 3])};
  f32x2 t[4];
#pragma unroll
  for (int e = 0; e < 4; ++e) t[e] = (d[e] + d[4 + e]) + (d[8 + e] + d[12 + e]);
  t[0] = (t[0] + t[1]) + (t[2] + t[3]);
  const float mean = row_sum(t[0][0] + t[0][1]) * (1.0f / D_);
  const float mr = bfr(mean);
  const float mb = act ? mr : 0.f;
#pragma unroll
  for (int e = 0; e < 16; ++e) d[e] -= f32x2{mb, mb};
#pragma unroll
  for (int e = 0; e < 4; ++e) t[e] = d[e] * d[e];
#pragma unroll
  for (int e = 4; e < 16; ++e) t[e & 3] = __builtin_elementwise_fma(d[e], d[e], t[e & 3]);
  t[0] = (t[0] + t[1]) + (t[2] + t[3]);
  const float dm = mean - mr;
  const float var = fmaxf(row_sum(t[0][0] + t[0][1]) * (1.0f / D_) - dm * dm, 0.f);
  const float s1 = ln_sd(var), r1 = __builtin_amdgcn_rcpf(s1);
  const float r2 = __builtin_amdgcn_rcpf(ln_sd(var * r1 * r1));
  const float a = r1 * r2, cc = -bfr(dm * r1) * r2;
#pragma unroll
  for (int e = 0; e < 16; ++e) {
    const f32x2 v = __builtin_elementwise_fma(d[e], f32x2{a, a}, f32x2{cc, cc});
    f[e >> 2][e & 3] = pack2(v[0], v[1]);
  }
  return (__float_as_uint(mr) >> 16) | __float_as_uint(s1);
}
__device__ __forceinline__ float solo(float v) { asm("" : "+v"(v)); return v; }
__device__ __forceinline__ float vadd(float a, float b) { return solo(a + b); }
__device__ __forceinline__ float vmul(float a, float b) { return solo(a * b); }
__device__ __forceinline__ f32x4 add4(f32x4 a, f32x4 b) { return f32x4{vadd(a[0], b[0]), vadd(a[1], b[1]), vadd(a[2], b[2]), vadd(a[3], b[3])}; }

constexpr unsigned SPIN_CAP = 1u << 18;
template <int SL>
__device__ __forceinline__ void poll_flags(const GAS u64* p, u64 tag) {
#pragma unroll 1
  for (unsigned spins = 0; spins < SPIN_CAP; ++spins) {
    const u64 v = __hip_atomic_load(p, RLX_AGENT);
    if (__all(v == tag)) break;
    __builtin_amdgcn_s_sleep(SL);
  }
  __builtin_amdgcn_fence(__ATOMIC_ACQUIRE, "wavefront");
}

__device__ __forceinline__ unsigned arrive(int lb, int i) {
  unsigned r;
  asm volatile("s_waitcnt lgkmcnt(0)\n\ts_mov_b64 exec, 1\n\tds_add_rtn_u32 %0, %1, %2\n\ts_mov_b64 exec, -1\n\ts_waitcnt lgkmcnt(0)"
               : "=&v"(r) : "v"(lb + L_CNT + 4 * i), "v"(1u) : "memory");
  return __builtin_amdgcn_readfirstlane(r);
}
__device__ __forceinline__ void sig(int lb, int i) {
  asm volatile("s_waitcnt lgkmcnt(0)\n\ts_mov_b64 exec, 1\n\tds_add_u32 %0, %1\n\ts_mov_b64 exec, -1" ::"v"(lb + L_CNT + 4 * i), "v"(1u) : "memory");
}
template <int I> __device__ __forceinline__ void post(int sa, unsigned one) {
  asm volatile("ds_add_u32 %0, %1 offset:%2" ::"v"(sa), "v"(one), "n"(4 * I) : "memory");
}
__device__ __forceinline__ unsigned look(LAS char* L, int i) {
  return __hip_atomic_load((LAS unsigned*)(L + L_CNT) + i, __ATOMIC_RELAXED, __HIP_MEMORY_SCOPE_WORKGROUP);
}
__device__ __forceinline__ void check(LAS char* L, unsigned v, int i, unsigned target) {
  unsigned t, n, a;
  asm volatile("s_nop 0\n\tv_readfirstlane_b32 %0, %3\n\ts_cmp_lt_u32 %0, %5\n\ts_cbranch_scc1 .Lcw%=\n.Lcb%=:\n\t.subsection 1\n"
               ".Lcw%=:\n\ts_mov_b32 %1, 0x400000\n\tv_mov_b32 %2, %4\n"
               ".Lcl%=:\n\tds_read_b32 %3, %2\n\ts_waitcnt lgkmcnt(0)\n\tv_readfirstlane_b32 %0, %3\n\ts_cmp_ge_u32 %0, %5\n\t"
               "s_cbranch_scc1 .Lcb%=\n\ts_sleep 0\n\ts_sub_u32 %1, %1, 1\n\ts_cbranch_scc0 .Lcl%=\n\ts_branch .Lcb%=\n\t.subsection 0"
               : "=&s"(t), "=&s"(n), "=&v"(a), "+v"(v) : "s"((int)(unsigned)(size_t)L + L_CNT + 4 * i), "n"(target) : "memory", "scc");
}
__device__ __forceinline__ void await(LAS char* L, int i, unsigned target) {
  unsigned t, n, v;
  asm volatile("s_mov_b32 %1, 0x400000\n.Lal%=:\n\tds_read_b32 %2, %3\n\ts_waitcnt lgkmcnt(0)\n\tv_readfirstlane_b32 %0, %2\n\ts_cmp_ge_u32 %0, %4\n\t"
               "s_cbranch_scc1 .Lab%=\n\ts_sleep 0\n\ts_sub_u32 %1, %1, 1\n\ts_cbranch_scc0 .Lal%=\n.Lab%=:"
               : "=&s"(t), "=&s"(n), "=&v"(v) : "v"((int)(unsigned)(size_t)L + L_CNT + 4 * i), "n"(target) : "memory", "scc");
}
__device__ __forceinline__ void gbar4(LAS char* L, int lb, int i) { sig(lb, i); await(L, i, 4u); }

#define STAMP(i) do { if ((i) == HSTAMP) { SB(); hs_t = __builtin_amdgcn_s_memrealtime(); SB(); } } while (0)
#define STAMPC(i) do {} while (0)
#define STAMP_DECL u64 hs_t = 0
#define STAMP_OUT() do { if (hs_t && (HCOND)) { while (__builtin_amdgcn_s_memrealtime() < hs_t + 3000) __builtin_amdgcn_s_sleep(1); } } while (0)
#define BAR() __builtin_amdgcn_s_barrier()
#define LANEVARS                                                                                         \
  int lane;                                                                                              \
  asm volatile("v_mbcnt_lo_u32_b32 %0, -1, 0\n\tv_mbcnt_hi_u32_b32 %0, -1, %0" : "=v"(lane));             \
  const int c = lane & 15, g = lane >> 4, q4 = c >> 2, p4 = c & 3, rr = lane >> 3;                       \
  (void)c; (void)g; (void)q4; (void)p4; (void)rr
constexpr f32x4 Z4 = {0.f, 0.f, 0.f, 0.f};
constexpr int OOB = 0x40000000;
template <int A, int B> __device__ __forceinline__ void pin(f32x4 (&a)[A][B]) {
#pragma unroll
  for (int i = 0; i < A; ++i)
#pragma unroll
    for (int t = 0; t < B; ++t) asm volatile("" : "+v"(a[i][t]));
}
template <class T> __device__ __forceinline__ T sel(bool c, T a, T b) { return __builtin_unpredictable(c) ? a : b; }
__device__ __forceinline__ int trbase(int i, int g, int q4, int p4) {
  return (4 * g + q4) * 128 + (((2 * i + (p4 >> 1)) ^ (2 * ((q4 >> 1) | ((g & 1) << 1)))) << 4) + 8 * (p4 & 1);
}
__device__ __forceinline__ void slot_frags(const LAS char* L, const int (&tb)[4], int so, u32x4 (&a)[4]) {
#pragma unroll
  for (int i = 0; i < 4; ++i) {
    const u32x2 lo = tr_read(L + tb[i] + so), hi = tr_read(L + tb[i] + so + 2048);
    a[i] = u32x4{lo.x, lo.y, hi.x, hi.y};
  }
}
__device__ __forceinline__ u64 peek(__amdgpu_buffer_rsrc_t rs, int off) {
  u64 v;
  asm volatile("buffer_load_dwordx2 %0, %1, %2, 0 offen sc1" : "=v"(v) : "v"(off), "s"(rs) : "memory");
  return v;
}
__device__ __forceinline__ void settle(__amdgpu_buffer_rsrc_t rs, int off, u64 first, u64 tag) {
  unsigned n;
  asm volatile("v_cmp_ne_u64 vcc, %4, %1\n\ts_cbranch_vccz .Lsb%=\n\ts_mov_b32 %0, 0x40000\n"
               ".Lsl%=:\n\tbuffer_load_dwordx2 %1, %2, %3, 0 offen sc1\n\ts_waitcnt vmcnt(0)\n\tv_cmp_ne_u64 vcc, %4, %1\n\t"
               "s_cbranch_vccz .Lsb%=\n\ts_sleep %5\n\ts_sub_u32 %0, %0, 1\n\ts_cbranch_scc0 .Lsl%=\n.Lsb%=:"
               : "=&s"(n), "+v"(first) : "v"(off), "s"(rs), "s"(tag), "n"(SLP) : "memory", "scc", "vcc");
}
template <class F, int... I> __device__ __forceinline__ void static_for(F f, std::integer_sequence<int, I...>) { (f(std::integral_constant<int, I>{}), ...); }
#define SFOR(n, var, ...) static_for([&](auto var##_c) AI { constexpr int var = decltype(var##_c)::value; __VA_ARGS__ }, std::make_integer_sequence<int, n>{})
template <int OFF> __device__ __forceinline__ void ld1_a(u32x4& v, __amdgpu_buffer_rsrc_t rs, int vo, int so) {
  asm volatile("buffer_load_dwordx4 %0, %1, %2, %3 offen offset:%4 sc1" : "=v"(v) : "v"(vo), "s"(rs), "s"(so), "n"(OFF) : "memory");
}
template <int N> __device__ __forceinline__ void wait1(u32x4& a) { asm volatile("s_waitcnt vmcnt(%1)" : "+v"(a) : "n"(N) : "memory"); }
template <int N> __device__ __forceinline__ void wait2(u32x4& a, u32x4& b) { asm volatile("s_waitcnt vmcnt(%2)" : "+v"(a), "+v"(b) : "n"(N) : "memory"); }

template <int NK, int NR, int NT, bool G2, int KX, int NB, class LD, class PUT, class HK>
__device__ __forceinline__ void kloop(LAS char* L, const int (&tb)[4], int jit0, u32x4 (&fr)[NR][4], u32x4 (&hv)[NB][NT],
                                      f32x4 (&acc)[4][NT], LD ld, PUT put, HK hook) {
  constexpr int NL = NK * NT;
  auto mm = [&](auto kc, auto ic, auto tc) AI {
    constexpr int k = decltype(kc)::value, i = decltype(ic)::value, t = decltype(tc)::value;
    f32x4& a = k >= KX ? acc[t & 3][i] : acc[i][t];
    const f32x4 cz = k == 0 ? Z4 : a;
    a = (G2 && k < KX) ? mfma16(hv[k % NB][t], fr[k % NR][i], cz) : mfma16(fr[k % NR][i], hv[k % NB][t], cz);
    asm volatile("" : "+v"(a));
  };
  SFOR(NK - 2, k,
    u32x2 lo;
    SFOR(NT, t,
      constexpr int n = k * NT + t, is = NB * NT + n < NL ? NB * NT + n : NL;
      wait1<is - n - 1>(hv[k % NB][t]);
      if constexpr (t == 0) hook(std::integral_constant<int, k>{});
      SFOR(4, i,
        mm(k_c, i_c, t_c);
        constexpr int e = t * 4 + i;
        if constexpr (k >= 1 && k - 1 + NR < NK && e < 8) {
          const LAS char* q = L + tb[e >> 1] + ((jit0 + k - 1) & 3) * SS;
          if constexpr (e & 1) { const u32x2 hi = tr_read(q + 2048); fr[k - 1][e >> 1] = u32x4{lo.x, lo.y, hi.x, hi.y}; }
          else lo = tr_read(q);
        }
        SB();
      );
      if constexpr (k + NB < NK) ld(std::integral_constant<int, k + NB>{}, std::integral_constant<int, t>{});
      SB();
    );
  );
  SFOR(NT, t,
    wait2<NT - 1 - t>(hv[(NK - 2) % NB][t], hv[(NK - 1) % NB][t]);
    if constexpr (t == 0) hook(std::integral_constant<int, NK - 2>{});
    SFOR(8, e,
      mm(std::integral_constant<int, NK - 2 + (e & 1)>{}, std::integral_constant<int, (e >> 1)>{}, t_c);
      constexpr int d = t * 4 + (e >> 1) - 2;
      if constexpr ((e & 1) && d >= 0) put(std::integral_constant<int, d & 3>{}, std::integral_constant<int, d / 4>{});
      SB();
    );
  );
  put(std::integral_constant<int, 2>{}, std::integral_constant<int, NT - 1>{});
  put(std::integral_constant<int, 3>{}, std::integral_constant<int, NT - 1>{});
}
template <int N> __device__ __forceinline__ void gelu_pack(const f32x4 (&v)[N], unsigned (&pr)[2 * N]) {
  f32x4 z[N];
#pragma unroll
  for (int e = 0; e < N; ++e) z[e] = v[e] * (v[e] * v[e] * -0.1029432395800235f - 2.302208198144325f);
  SB();
#pragma unroll
  for (int e = 0; e < 4 * N; ++e) z[e >> 2][e & 3] = __builtin_amdgcn_exp2f(z[e >> 2][e & 3]);
  SB();
#pragma unroll
  for (int e = 0; e < N; ++e) z[e] += 1.0f;
#pragma unroll
  for (int e = 0; e < 4 * N; ++e) z[e >> 2][e & 3] = __builtin_amdgcn_rcpf(z[e >> 2][e & 3]);
  SB();
#pragma unroll
  for (int e = 0; e < N; ++e) { z[e] *= v[e]; pr[2 * e] = pack2(z[e][0], z[e][1]); pr[2 * e + 1] = pack2(z[e][2], z[e][3]); }
}
__device__ __forceinline__ float fold32(float a, float b) {
  const auto s = __builtin_amdgcn_permlane32_swap(__float_as_uint(a), __float_as_uint(b), false, false);
  return vadd(__uint_as_float(s[0]), __uint_as_float(s[1]));
}
#define PINV(v) asm volatile("" : "+v"(v))
#define IC(n) std::integral_constant<int, (n)>{}
constexpr int g1buf(int nt, int t) { return ((t + 8 - nt) & 3) * SS; }
constexpr int L_B1 = 162816;
constexpr int A_W = 26624, A_WS = 24576, A_X1 = 100352, A_XS = 26624;
constexpr int A_MEAN = 153600, A_RSTD = A_MEAN + 1024, A_CS = A_RSTD + 1024;
#define NW0 12
#define FINISH() do { STAMP(8); { LANEVARS; char* ws = ws_; (void)ws; STAMP_OUT(); } __builtin_amdgcn_endpgm(); } while (0)
__global__ __launch_bounds__(512, 2) void fused_block(const u16*, const u16*, const u16*, const u16*, const u16*, const u16*,
                                                      const u16*, u16*, char* ws_) {
  extern __shared__ __attribute__((aligned(16))) char smem[];
  LAS char* L = (LAS char*)smem;
  const int lb = (int)(unsigned)(size_t)L;
  const auto kp = __builtin_amdgcn_kernarg_segment_ptr();
  u32x16 ka; u32x2 kb;
  asm volatile("s_load_dwordx16 %0, %2, 0x0\n\ts_load_dwordx2 %1, %2, 0x40" : "=&s"(ka), "=&s"(kb) : "s"(kp));
  SB();
  const int w = __builtin_amdgcn_readfirstlane((int)threadIdx.x >> 6);
  const int bid = blockIdx.x, xcd = bid & 7, idx = bid >> 3;
  const int m = w & 3;
  STAMP_DECL;
  STAMP(0);
#define ARGP(i) ((const u16*)(((u64)ka[2 * (i) + 1] << 32) | ka[2 * (i)]))
#define ARGWAIT() asm volatile("s_waitcnt lgkmcnt(0)" : "+s"(ka), "+s"(kb) : "s"(kp) : "memory")
  u64 tag;
  const int un = xcd * 16 + idx, row0 = 32 * (un >> 1) + 8 * (un & 1);
  const bool isln = idx < 16;
  u32x4 hx[4];
  u64 e1;
  if (isln && w >= 4) {
    int lv = sel(((int)threadIdx.x & 15) < 14, (((int)threadIdx.x >> 4) & 3) * (D_ * 2) + ((int)threadIdx.x & 15) * 16, OOB);
    int ls = (row0 + 4 * (m & 1) + 8 * (m & 2)) * (D_ * 2), z = 0;
    asm volatile("" : "+v"(lv), "+v"(z));
    asm volatile("" : "+s"(ls));
    ARGWAIT();
    ldx4(hx, __builtin_amdgcn_make_buffer_rsrc((void*)ARGP(0), 0, S_ * D_ * 2, RSRC_W3), lv, ls);
    e1 = peek(__builtin_amdgcn_make_buffer_rsrc((void*)(((u64)kb[1] << 32) | kb[0]), 0, WS_END, RSRC_W3), z);
    BAR();
  } else {
    u64 pc;
    unsigned co = sel(isln, 0, (((idx - 16) * 8 + w) * 8 + ((int)threadIdx.x & 7)) * 128);
    asm volatile(".Lfb_start:\n\ts_getpc_b64 %0\n\ts_mov_b32 m0, %2\n\tv_min_u32 %1, .Lfb_end-.Lfb_start-8, %1\n\t"
                 "global_load_lds_dword %1, %0\n\t.subsection 2\n.Lfb_end:\n\ts_endpgm\n\t.subsection 0" : "=&s"(pc), "+v"(co) : "s"(lb + L_DUMMY) : "memory");
    *((LAS unsigned*)(L + L_CNT) + ((int)threadIdx.x & 31)) = 0u;
  }
  SB();
  auto gettag = [&]() AI {
    const u32x2 tv = *(const LAS u32x2*)(L + L_CNT + 128);
    tag = ((u64)(unsigned)__builtin_amdgcn_readfirstlane((int)tv.y) << 32) | (unsigned)__builtin_amdgcn_readfirstlane((int)tv.x);
  };

  const int ng = 24 - 6 * (xcd & 1);
  const bool isg = idx < ng;
  const int lp = (idx * 43) >> 8;
  int ft = 7 * (xcd >> 1) + 4 * (xcd & 1) + lp, dg = idx - 6 * lp;
  int off1 = (dg * 43) >> 4;
  const int q = 22 * (xcd >> 2) + 8 * (xcd & 1) + idx - ng;
  int hp = 2 * (xcd & 2) + (q & 3), rg = q >> 2;
  int rotq = RQ1 * (q & 3) + RQ2 * rg + 70;
  rotq -= 7 * ((rotq * 147) >> 10);
  constexpr int EPB = 248;
  auto k1 = [&](int j) AI { return (j + off1) & 15; };
  int reg = (w >> 2) * L_LATE + m * 4096;
  int fv, fs, fs1, role = (isg ? 0 : 2) + (w >> 2);
  {
    const int l = (int)threadIdx.x & 63, rr = l >> 3, ch = l & 7;
    const int v1 = ((m * 512 + rr) * F_ + ft * 64) * 2 + (ch ^ (2 * ((rr >> 1) & 3))) * 16;
    const int cq = ch ^ (2 * ((rr >> 1) & 1)) ^ (4 * (m & 1));
    const int vq = sel(cq < 7, (8 * m + rr) * (N3_ * 2) + hp * 112 + cq * 16, OOB);
    const int va = (192 * rg + 16 + 8 * m + rr) * (D_ * 2) + (ch ^ (4 * (m & 1) + (rr >> 1))) * 16;
    fv = sel(isg, v1, sel(w >= 4, va, vq));
    fs = sel(isg, k1(0) * 32 * (F_ * 2), rotq * sel(w >= 4, 128, 64 * N3_ * 2));
    fs1 = k1(1) * 32 * (F_ * 2);
  }
  asm volatile("" : "+v"(fv));
  asm volatile("" : "+s"(fs), "+s"(fs1), "+s"(ft), "+s"(dg), "+s"(reg), "+s"(off1), "+s"(rotq), "+s"(role), "+s"(hp), "+s"(rg));
  const int r0 = 192 * rg;
  STAMP(26);
  ARGWAIT();
  STAMP(27);
  const u16 *x = ARGP(0), *wqkv = ARGP(1), *wo = ARGP(2), *w1 = ARGP(3), *b1 = ARGP(4), *w2 = ARGP(5), *b2 = ARGP(6);
  u16* out = (u16*)ARGP(7);
  char* ws = (char*)(((u64)kb[1] << 32) | kb[0]);
  const auto rs_x = __builtin_amdgcn_make_buffer_rsrc((void*)x, 0, S_ * D_ * 2, RSRC_W3);
  const auto rs_ws = __builtin_amdgcn_make_buffer_rsrc((void*)ws, 0, WS_END, RSRC_W3);
  const auto rs_w1 = __builtin_amdgcn_make_buffer_rsrc((void*)w1, 0, S_ * F_ * 2, RSRC_W3);
  const auto rs_w2 = __builtin_amdgcn_make_buffer_rsrc((void*)w2, 0, F_ * S_ * 2, RSRC_W3);
  auto putflag = [&](int off, int lane) AI {
    __builtin_amdgcn_raw_buffer_store_b128(u32x4{(unsigned)tag, (unsigned)(tag >> 32), (unsigned)tag, (unsigned)(tag >> 32)}, rs_ws,
                                           sel(lane < 8, off + lane * 16, OOB), 0, AUX_SC1);
  };
  auto mktag = [&](u64 e0) AI {
    e0 = e0 * 0x9E3779B97F4A7C15ull + 0x632BE59BD9B4E019ull;
    *(LAS u64*)(L + L_CNT + 128) = e0;
    tag = ((u64)(unsigned)__builtin_amdgcn_readfirstlane((int)(e0 >> 32)) << 32) | (unsigned)__builtin_amdgcn_readfirstlane((int)e0);
  };
  const auto rs_wq = __builtin_amdgcn_make_buffer_rsrc((void*)wqkv, 0, D_ * N3_ * 2, RSRC_W3);
  auto piece = [&](bool late, int fv, int sg, int so, int e) AI {
    if (late) {
      if (e < 6 || m < 2) dma16(rs_x, e ? fv : sel(fv >= 32 * D_ * 2, fv - 32 * D_ * 2, OOB), so + (e ? e - 1 : 0) * 32 * (D_ * 2), lb + (sg ? A_X1 + (sg - 1) * A_XS : 0) + (m + 4 * e) * 1024);
    } else {
      if (e < 6) dma16(rs_wq, fv, so + (e / 3) * 32 * (N3_ * 2) + (e % 3) * 896, lb + A_W + sg * A_WS + e * 4096 + m * 1024);
    }
  };
  if (role == 0) {
    BAR();
    if (GDLY) __builtin_amdgcn_s_sleep(GDLY);
    dma4(rs_w1, fv, fs, 8 * F_ * 2, lb + reg);
    dma4(rs_w1, fv, fs1, 8 * F_ * 2, lb + reg + SS);
    STAMP(25);
  } else if (role == 2) {
    if (ADLY) __builtin_amdgcn_s_sleep(ADLY);
#pragma unroll
    for (int e = 0; e < 6; ++e) piece(false, fv, 2, fs, e);
    STAMP(25);
  } else if (role == 3) {
    if (ADLY) __builtin_amdgcn_s_sleep(ADLY);
#pragma unroll
    for (int e = 0; e < 7; ++e) piece(true, fv, 2, fs, e);
  }
  asm volatile("" : "+s"(role));
  if (role != 1) {
  } else if (isln) {
    PRIO(3);
    LANEVARS;
    const bool act = c < 14;
    int hb = sel(act, L_HB + (4 * m + g) * HB_PITCH + c * 16, L_DUMMY);
    int tp = L_HB + (4 * (g & 1) + q4) * HB_PITCH + (16 * (g >> 1) + 4 * p4) * 2 + m * 64;
    int ho = WS_H + (un >> 1) * KS_ + (2 * m + (g >> 1)) * 1024 + (2 * (un & 1) + (g & 1)) * 256 + c * 16;
    int h3 = sel(m < 2, ho + 3 * 8192, sel((lane < 16) & (m == 3), WS_ST + (row0 + (c & 7) + 2 * (c & 8)) * 16, OOB));
    asm volatile("" : "+v"(hb), "+v"(tp), "+v"(ho), "+v"(h3));
    STAMP(11);
    asm volatile("s_waitcnt vmcnt(1)" : "+v"(hx[0]), "+v"(hx[1]), "+v"(hx[2]), "+v"(hx[3])::"memory");
    STAMP(12);
    const unsigned sa = ln2_rows(hx, act);
#pragma unroll
    for (int e = 0; e < 4; ++e) *(LAS u32x4*)(L + hb + e * 224) = hx[e];
    *(LAS unsigned*)(L + L_SA + (4 * m + g) * 4) = sa;
    asm volatile("s_waitcnt vmcnt(0)" : "+v"(e1)::"memory");
    mktag(e1);
    STAMP(13);
    gbar4(L, lb, 0);
    u32x4 hs[4];
#pragma unroll
    for (int u = 0; u < 4; ++u) {
      const u32x2 a0 = tr_read(L + tp + u * 256), a1 = tr_read(L + tp + u * 256 + 8 * HB_PITCH);
      hs[u] = u32x4{a0.x, a0.y, a1.x, a1.y};
    }
    const unsigned sb = *(const LAS unsigned*)(L + L_SA + c * 4);
#pragma unroll
    for (int u = 0; u < 3; ++u) __builtin_amdgcn_raw_buffer_store_b128(hs[u], rs_ws, ho + u * 8192, 0, AUX_SC1);
    if (m == 3) hs[3] = u32x4{sb, (unsigned)tag, sb, (unsigned)(tag >> 32)};
    __builtin_amdgcn_raw_buffer_store_b128(hs[3], rs_ws, h3, 0, AUX_SC1);
    SB();
    sig(lb, 7);
    STAMP(14);
    DRAIN();
    if (arrive(lb, 1) == 3u) putflag(WS_HF + un * FS, lane);
    STAMP(1);
  } else {
    u64 e0 = peek(rs_ws, WS_E);
    BAR();
    asm volatile("s_waitcnt vmcnt(0)" : "+v"(e0)::"memory");
    mktag(e0);
    sig(lb, 0);
    sig(lb, 7);
  }
  asm volatile("" : "+s"(role));
  int idy = idx;
  asm volatile("" : "+s"(idy));
  const u64 tm = idy < 12 ? 0x59d28518a320820ull : idy < 24 ? 0xbdab49ca2f839acull : 0x18820ffbbcdeb38ull;
  const int tile = sel(xcd & 1, (int)(tm >> (5 * (idy - 12 * ((idy * 43) >> 9)))) & 31, idy);
  const int st = 4 * (xcd ^ SX) + (tile & 3), dt = tile >> 2;
  const int rotl = dt % 6;
  auto pos2 = [&](bool late, int p) AI { const int v = p + rotl; return 14 * m + (late ? 8 + (v >= 6 ? v - 6 : v) : (p + 2 * dt) & 7); };
  auto ldw2 = [&](u32x4 (&a)[4], int vr, int p) AI { ld4(a, rs_w2, vr, (pos2(false, p & 6) * 32 + 16 * (p & 1)) * (S_ * 2), S_ * 2); };
  auto w2lane = [&](int lane) AI { return ((32 * ((lane >> 3) & 1) + 4 * (lane >> 4)) * S_ + st * 64 + 8 * (lane & 7)) * 2; };
  auto gemm2 = [&](auto latec, auto nec, auto& f2, auto idc) AI {
    constexpr bool LATE = decltype(latec)::value;
    constexpr int NR2 = LATE ? 6 : 8, NK2 = LATE ? 10 : 8, N0 = decltype(nec)::value;
    {
      constexpr int NB2 = LATE ? NB2L : 3;
      u32x4 uv[NB2][4];
      f32x4 acc[4][4];
      {
        LANEVARS;
        PRIO(PP2);
        const int w2E = (rr * S_ + st * 64) * 2 + ((lane & 7) ^ (2 * ((rr >> 1) & 3))) * 16;
        int tb[4];
#pragma unroll
        for (int i = 0; i < 4; ++i) tb[i] = reg + trbase(i, g, q4, p4);
        const int v0 = lane * 16;
        const int ta = 4 * dt + 3 * ((lane >> 3) & 1), dgp = sel(ta < 20, (ta * 13) >> 6, (ta >> 2) - 1);
        const int fo = WS_UF + ((7 * m + (LATE ? 4 : 0) + min(lane & 7, LATE ? 2 : 3)) * 6 + dgp) * FS;
        const int oo = WS_OF + (((st * 43) >> 7) * 8 + 2 * m + (lane & 1)) * FS;
        auto fill = [&](int p, int dst) AI { dma4(rs_w2, w2E, pos2(true, p) * 32 * (S_ * 2), 8 * S_ * 2, lb + dst); };
        constexpr int XS = L_HB - L_LATE;
        u64 f0;
        const auto rs_wo = __builtin_amdgcn_make_buffer_rsrc((void*)wo, 0, D_ * D_ * 2, RSRC_W3);
        const int woE = (rr * D_ + dt * 64) * 2 + ((lane & 7) ^ (2 * ((rr >> 1) & 3))) * 16;
        auto fillo = [&](int e) AI { dma4(rs_wo, woE, (4 * m + e) * 28 * (D_ * 2), 8 * D_ * 2, lb + L_LATE + m * 4096 + e * SS); };
        const int ub = WS_U + 4 * dt * 1024, ob = WS_O + (4 * st * 16 + 4 * m) * 1024;
        u64 o0 = 0;
        auto ldu = [&](auto pc, auto tc) AI {
          constexpr int p = decltype(pc)::value, t = decltype(tc)::value;
          if constexpr (LATE && p == 6 && t == 0) { asm volatile("" : "+v"(o0)); STAMP(20); settle(rs_ws, oo, o0, tag); STAMP(17); }
          if constexpr (LATE && p == 6 - NB2 && t == 0) o0 = peek(rs_ws, oo);
          if constexpr (LATE && p >= 6) ld1_a<(p - 6) * 1024>(uv[p % NB2][t], rs_ws, v0, ob + t * 16384);
          else ld1_a<t * 1024>(uv[p % NB2][t], rs_ws, v0, ub + pos2(LATE, p) * KS_);
        };
        auto first = [&]() AI {
          STAMP(16);
          settle(rs_ws, fo, f0, tag);
          PRIO(LATE ? PL2 : PE2);
          STAMP(5);
          SFOR(NB2 * 4, n, ldu(IC(n / 4), IC(n % 4)););
          SB();
        };
        if constexpr (!LATE) {
          const int vr = w2lane(lane);
#pragma unroll
          for (int p = N0; p < 8; ++p) {
            ldw2(f2[p], vr, p);
            if (!SG2 && p == N0 + NSG - 1) { WAITVM(4 * NSG); sig(lb, 2); }
          }
          STAMP(18);
          await(L, 3, 4u);
          await(L, 4, 4u);
          auto turn = [&](auto pc, auto nc) AI {
            constexpr int p = decltype(pc)::value, n = decltype(nc)::value;
            wait4<n>(f2[2 * p]);
            wait4<n>(f2[2 * p + 1]);
            turn_pair(f2[2 * p], f2[2 * p + 1]);
            SB();
          };
#define TURN(p, n) turn(std::integral_constant<int, p>{}, std::integral_constant<int, n>{})
          fill(0, reg); fill(1, reg + SS);
          if (SG2) { WAITVM(4 * (8 - N0) + 8); sig(lb, 2); }
          if (IL) TURN(0, 32);
          await(L, 5, 4u);
          fill(2, reg + 2 * SS); fill(3, reg + 3 * SS);
          if (IL) TURN(1, 32);
          fill(4, L_HB + m * 5120); fill(5, L_LATE + reg + 3 * SS);
          if (IL) TURN(2, 32);
          if (OV == 2) sig(lb, 31);
          if (OV) f0 = peek(rs_ws, fo);
          fillo(0); fillo(1); fillo(2);
          WAITVM(12 + (OV ? 1 : 0));
          sig(lb, 16 + m);
          if (OV) {
            asm volatile("s_waitcnt vmcnt(12)" : "+v"(f0)::"memory");
            first();
            TURN(3, 24);
          } else {
            f0 = peek(rs_ws, fo);
            TURN(3, 13);
            WAITVM(1);
            sig(lb, 16 + m);
            asm volatile("s_waitcnt vmcnt(0)" : "+v"(f0)::"memory");
            first();
          }
        } else {
          auto take = [&](int r, int so) AI {
            slot_frags(L, tb, so, f2[r]);
            asm volatile("s_waitcnt lgkmcnt(0)" : "+v"(f2[r][0]), "+v"(f2[r][1]), "+v"(f2[r][2]), "+v"(f2[r][3])::"memory");
          };
          if (OV == 2) { await(L, 31, 4u); f0 = peek(rs_ws, fo); asm volatile("s_waitcnt vmcnt(0)" : "+v"(f0)::"memory"); first(); }
          await(L, 16 + m, 1u);
          if (OV == 1) f0 = peek(rs_ws, fo);
          take(5, 3 * SS);
          fillo(3);
          if (OV == 1) { asm volatile("s_waitcnt vmcnt(4)" : "+v"(f0)::"memory"); first(); }
          if (!OV) f0 = peek(rs_ws, fo);
#pragma unroll
          for (int r = 0; r < 4; ++r) slot_frags(L, tb, r * SS - L_LATE, f2[r]);
          slot_frags(L, tb, XS + m * 1024, f2[4]);
          sig(lb, 20 + m);
          if (!OV) {
            await(L, 16 + m, 2u);
            asm volatile("s_waitcnt vmcnt(0)" : "+v"(f0)::"memory");
            first();
          }
        }
        int wr[4];
#pragma unroll
        for (int e = 0; e < 4; ++e) wr[e] = reg + g * 256 + (LATE ? c ^ (2 * e + (g >> 1)) : (c + 8 * e + 4 * (g >> 1)) & 15) * 16;
        kloop<NK2, NR2, 4, true, LATE ? 6 : 99, NB2>(L, tb, 0, f2, uv, acc, ldu,
            [&](auto ic, auto tc) AI {
              constexpr int i = LATE ? decltype(tc)::value : decltype(ic)::value, t = LATE ? decltype(ic)::value : decltype(tc)::value;
              *(LAS f32x4*)(L + wr[(t & 1) + (LATE ? 2 * (i & 1) : 0)] + i * SS + t * 1024) = acc[i][t];
            }, [&](auto kc) AI {
              constexpr int k = decltype(kc)::value;
              if constexpr (!LATE && k == NK2 - 2) await(L, 20 + m, 1u);
              if constexpr (OV && !LATE && k == 0) sig(lb, 16 + m);
              if constexpr (OV && LATE && k == 1) await(L, 16 + m, 2u);
              if constexpr (!HK1 && !(k & 1)) STAMP(21 + k / 2);
            });
        STAMP(6);
      }
      if constexpr (LATE) {
        sig(lb, 10);
      } else {
        LANEVARS;
        const int eo = ((st * 64 + 8 * rr + m) * D_ + dt * 64 + (lane & 7) * 8) * 2;
        u32x4 xr[2];
        float bs[2];
#pragma unroll
        for (int e = 0; e < 2; ++e) {
          xr[e] = __builtin_amdgcn_raw_buffer_load_b128(rs_x, eo + e * 4 * D_ * 2, 0, 0);
          bs[e] = lo_f(*(const GAS u16*)(b2 + st * 64 + 8 * rr + 4 * e + m));
        }
        int ra[2];
#pragma unroll
        for (int e = 0; e < 2; ++e) ra[e] = (m * 16 + ((lane & 7) >> 1)) * 1024 + (lane & 1) * 512 + ((rr + 8 * e + 4 * (lane & 3)) & 15) * 16;
        gbar4(L, lb, 9);
        f32x4 tm[4][4], base[4];
#pragma unroll
        for (int j = 0; j < 4; ++j)
#pragma unroll
          for (int e = 0; e < 4; ++e) tm[j][e] = *(const LAS f32x4*)(L + ra[e >> 1] + j * 4096 + (e & 1) * 256);
        SB();
#pragma unroll
        for (int e = 0; e < 4; ++e) {
          const unsigned a = xr[e >> 1][2 * (e & 1)], b = xr[e >> 1][2 * (e & 1) + 1];
          const float s = bs[e >> 1];
          base[e] = add4(add4(add4(f32x4{lo_f(a), hi_f(a), lo_f(b), hi_f(b)}, f32x4{s, s, s, s}), add4(tm[0][e], tm[1][e])), add4(tm[2][e], tm[3][e]));
          asm volatile("" : "+v"(base[e]));
        }
#pragma unroll
        for (int e = 0; e < 2; ++e) {
          ra[e] = L_LATE + (rr >> 1) * SS + ((lane & 7) >> 1) * 1024 + (lane & 1) * 512 + ((8 * (rr & 1) + 4 * e + m) ^ ((lane & 3) + 2 * (rr & 2))) * 16;
          asm volatile("" : "+v"(ra[e]));
        }
        PRIO(3);
        STAMP(20);
        await(L, 10, 4u);
        STAMP(7);
#pragma unroll
        for (int j = 0; j < 4; ++j)
#pragma unroll
          for (int e = 0; e < 4; ++e) tm[j][e] = *(const LAS f32x4*)(L + ra[e >> 1] + j * 4096 + (e & 1) * 256);
        SB();
        const auto rs_out = __builtin_amdgcn_make_buffer_rsrc((void*)out, 0, S_ * D_ * 2, RSRC_W3);
#pragma unroll
        for (int e = 0; e < 2; ++e) {
          const f32x4 s0 = (base[2 * e] + tm[0][2 * e]) + tm[1][2 * e] + (tm[2][2 * e] + tm[3][2 * e]);
          const f32x4 s1 = (base[2 * e + 1] + tm[0][2 * e + 1]) + tm[1][2 * e + 1] + (tm[2][2 * e + 1] + tm[3][2 * e + 1]);
          __builtin_amdgcn_raw_buffer_store_b128(u32x4{pack2(s0[0], s0[1]), pack2(s0[2], s0[3]), pack2(s1[0], s1[1]), pack2(s1[2], s1[3])},
                                                 rs_out, eo + e * 4 * D_ * 2, 0, AUX_SC1);
        }
      }
    }
    asm volatile("s_nop %0" ::"n"(decltype(idc)::value));
    FINISH();
  };
  auto utile = [&](auto latec) AI {
    constexpr bool LATE = decltype(latec)::value;
      auto gemm1 = [&](auto ntc) AI {
        constexpr int NT = decltype(ntc)::value, NL = 8 * NT, NB = NB1, TPN = TP * 4;
        const int t0 = NT == 5 ? 5 * dg : 4 * dg + 4;
        u32x4 fr[8][4], hv[NB], f2[LATE ? 6 : 8][4];
        LANEVARS;
        PRIO(LATE ? PLP : PEP);
        const int v0 = lane * 16, v4 = v0 + 4096, wr = reg + v0;
        int ho[8];
        auto ld = [&](auto nc) AI {
          constexpr int n = decltype(nc)::value, t = n >> 3;
          ld1_a<(t & 3) * 1024>(hv[n % NB], rs_ws, t < 4 ? v0 : v4, ho[n & 7]);
        };
        auto start = [&](int fo, u64 f0) AI {
          settle(rs_ws, fo, f0, tag);
          PRIO(LATE ? PL1 : PE1);
          STAMP(2);
#pragma unroll
          for (int p = 0; p < 8; ++p) ho[p] = WS_H + (m * 16 + k1((LATE ? 8 : 0) + p)) * KS_ + t0 * 1024;
          SFOR(NB, n, ld(n_c););
          SB();
        };
        {
          const int w1E = ((m * 512 + rr) * F_ + ft * 64) * 2 + ((lane & 7) ^ (2 * ((rr >> 1) & 3))) * 16;
          auto fill = [&](int j) AI { dma4(rs_w1, w1E, k1(j) * 32 * (F_ * 2), 8 * F_ * 2, lb + reg + ((j >> 2) & 1) * L_LATE + (j & 3) * SS); };
          int tb[2][4];
#pragma unroll
          for (int i = 0; i < 8; ++i) tb[i >> 2][i & 3] = (i >> 2) * L_LATE + m * 4096 + trbase(i & 3, g, q4, p4);
          auto take = [&](int j) AI { slot_frags(L, tb[j >> 2], (j & 3) * SS, fr[j]); };
          auto taken = [&](int j) AI { asm volatile("s_waitcnt lgkmcnt(0)" : "+v"(fr[j][0]), "+v"(fr[j][1]), "+v"(fr[j][2]), "+v"(fr[j][3])::"memory"); };
          const int fo = WS_HF + (m * 32 + 2 * (((LATE ? 8 : 0) + ((lane >> 1) & 7) + off1) & 15) + (lane & 1)) * FS;
          u64 f0;
          if constexpr (!LATE) {
            STAMP(11);
            WAITVM(W0);
            STAMP(12);
#pragma unroll
            for (int j = 2; j < 8; ++j) fill(j);
            STAMP(13);
            await(L, 0, 4u);
            gettag();
            SFOR(8, j, WAITVM(28 + (j > 8 - PKE)); take(j); taken(j); if constexpr (j == 8 - PKE) f0 = peek(rs_ws, fo); fill(8 + j););
            STAMP(14);
            WAITVM(17); sig(lb, 12 + m);
            asm volatile("s_waitcnt vmcnt(%1)" : "+v"(f0) : "n"(4 * PKE) : "memory");
            STAMP(9);
            start(fo, f0);
            WAITVM(NB); sig(lb, 12 + m);
          } else {
            if (m == 0) {
              const auto rs_b1 = __builtin_amdgcn_make_buffer_rsrc((void*)b1, 0, F_ * 2, RSRC_W3);
              await(L, 0, 4u);
              touch(rs_b1, sel(lane < 32, lane * 4, OOB), ft * 128, lb + L_B1);
            }
            await(L, 12 + m, 1u);
            f0 = peek(rs_ws, fo);
#pragma unroll
            for (int j = 0; j < 4; ++j) take(j);
            asm volatile("s_waitcnt vmcnt(0)" : "+v"(f0)::"memory");
            if (m == 0) sig(lb, 11);
            start(fo, f0);
            await(L, 12 + m, 2u);
#pragma unroll
            for (int j = 4; j < 8; ++j) take(j);
#pragma unroll
            for (int j = 0; j < 8; ++j) taken(j);
            sig(lb, 30);
            STAMP(9);
          }
        }
        const int ra = (lane >> 5) * L_LATE + ((LATE ? 2 : 0) + (g & 1)) * 1024 + m * 256 + c * 16;
        const int ba = L_B1 + (16 * ((LATE ? 2 : 0) + (g & 1)) + 4 * m + 2 * (lane >> 5)) * 2;
        const int uo = WS_U + (2 * ft + (LATE ? 1 : 0)) * KS_ + (t0 + g) * 1024 + m * 256 + c * 16;
        const int vr = w2lane(lane);
        int sa = sel(lane == 0, lb + L_CNT, OOB);
        unsigned one = 1u;
        PINV(sa); PINV(one);
        f32x4 acc[2][4], pt[4];
        float bq[2], v[2], z[2];
        unsigned pr[6], cv, cr;
        auto rd = [&](int t, int o) AI {
#pragma unroll
          for (int e = 0; e < 4; ++e) pt[e] = *(const LAS f32x4*)(L + ra + o + g1buf(NT, t) + e * 4096);
        };
        auto step = [&](auto sc, int t) AI {
          constexpr int s = decltype(sc)::value;
          if constexpr (s == 0) { pt[0] = add4(pt[0], pt[1]); PINV(pt[0]); }
          if constexpr (s == 1) { pt[2] = add4(pt[2], pt[3]); PINV(pt[2]); }
          if constexpr (s == 2) { pt[0] = add4(pt[0], pt[2]); PINV(pt[0]); }
          if constexpr (s == 3) {
#pragma unroll
            for (int e = 0; e < 2; ++e) { v[e] = vadd(fold32(pt[0][e], pt[0][2 + e]), bq[e]); PINV(v[e]); }
          }
          if constexpr (s == 4) {
#pragma unroll
            for (int e = 0; e < 2; ++e) {
              const float p = solo(__builtin_fmaf(vmul(v[e], v[e]), -0.1029432395800235f, -2.302208198144325f));
              z[e] = vmul(v[e], p); PINV(z[e]);
            }
          }
          if constexpr (s == 5) {
#pragma unroll
            for (int e = 0; e < 2; ++e) { z[e] = vadd(__builtin_amdgcn_exp2f(z[e]), 1.0f); PINV(z[e]); }
          }
          if constexpr (s == 6) {
            pr[t] = pack2(vmul(__builtin_amdgcn_rcpf(z[0]), v[0]), vmul(__builtin_amdgcn_rcpf(z[1]), v[1]));
            PINV(pr[t]);
          }
        };
        auto put = [&](unsigned a, unsigned b, unsigned cc, unsigned d, int off) AI {
          const auto s0 = __builtin_amdgcn_permlane32_swap(a, cc, false, false), s1 = __builtin_amdgcn_permlane32_swap(b, d, false, false);
          const auto r0 = __builtin_amdgcn_permlane16_swap(s0[0], s1[0], false, false), r1 = __builtin_amdgcn_permlane16_swap(s0[1], s1[1], false, false);
          __builtin_amdgcn_raw_buffer_store_b128(u32x4{r0[0], r1[0], r0[1], r1[1]}, rs_ws, off, 0, AUX_SC1);
          SB();
        };
        auto bias = [&](int o) AI { const unsigned b = *(const LAS unsigned*)(L + ba + o); bq[0] = lo_f(b); bq[1] = hi_f(b); };
        SFOR(NL, n,
          constexpr int t = n >> 3, p = n & 7, nw = !LATE && t == NT - 1 ? 4 * (p < NE ? p : NE) : 0;
          if constexpr (n == 9 && !LATE) check(L, cr, 30, 4u);
          if constexpr (n == 13) { check(L, cv, 11, 1u); bias(0); }
          if constexpr (t >= 2 && p == 4) { check(L, cv, 24 + t - 2, 8u); rd(t - 2, 0); if constexpr (NT == 5 && t == 2) post<29>(sa, one); }
          if constexpr (!LATE && n == NL - TPN) PRIO(3);
          if constexpr (t >= 2 && p == 3) cv = look(L, 24 + t - 2);
          if constexpr (n == 7 && !LATE) cr = look(L, 30);
          if constexpr (n == 11) cv = look(L, 11);
          if constexpr (n == NL - 2) { cr = look(L, 29); cv = look(L, 24 + NT - 2); }
          SB();
          wait1<(n + NB < NL ? n + NB : NL) - 1 - n + nw>(hv[n % NB]);
          if constexpr (HK1 == 1 && p == 0) STAMP(21 + t);
          SFOR(4, i,
            mfma_ip<p == 0>(acc[t & 1][i], fr[p][i], hv[n % NB]);
            if constexpr (t >= 1 && (p == 1 || p == 2) && (i & 1)) *(LAS f32x4*)(L + wr + g1buf(NT, t - 1) + (2 * p - 2 + (i >> 1)) * 1024) = acc[(t - 1) & 1][2 * p - 2 + (i >> 1)];
            if constexpr (t >= 2 && p >= 5 && 4 * (p - 5) + i >= 1 && 4 * (p - 5) + i < 8) step(IC(4 * (p - 5) + i - 1), t - 2);
            SB();
          );
          if constexpr (t >= 1 && p == 2) post<24 + t - 1>(sa, one);
          if constexpr (n + NB < NL) ld(IC(n + NB));
          if constexpr (!LATE && t == NT - 1 && p < NE) ldw2(f2[p], vr, p);
          SB();
        );
        STAMP(3);
        PRIO(3);
        if constexpr (NT == 5) check(L, cr, 29, 8u);
        asm volatile("s_nop 7" ::: "memory");
#pragma unroll
        for (int i = 0; i < 4; ++i) *(LAS f32x4*)(L + wr + g1buf(NT, NT - 1) + i * 1024) = acc[(NT - 1) & 1][i];
        post<24 + NT - 1>(sa, one);
        if constexpr (LATE) post<4>(sa, one);
        check(L, cv, 24 + NT - 2, 8u);
        rd(NT - 2, 0);
        if constexpr (!LATE) post<3>(sa, one);
        SFOR(7, s, step(s_c, NT - 2););
        if constexpr (NT == 5) put(pr[0], pr[1], pr[2], pr[3], uo);
        else if constexpr (!LATE) put(pr[0], pr[1], pr[2], pr[2], sel(lane < 48, uo, OOB));
        if constexpr (LATE) {
          const unsigned b2q = *(const LAS unsigned*)(L + ba - 64);
          await(L, 24 + NT - 1, 8u);
          STAMP(19);
          {
            f32x4 qa[8];
#pragma unroll
            for (int e = 0; e < 8; ++e) qa[e] = *(const LAS f32x4*)(L + ra - (e >> 2) * 2048 + g1buf(NT, NT - 1) + (e & 3) * 4096);
            post<5>(sa, one);
            const f32x4 s0 = (qa[0] + qa[1]) + (qa[2] + qa[3]), s1 = (qa[4] + qa[5]) + (qa[6] + qa[7]);
            f32x4 y[1] = {f32x4{fold32(s0[0], s0[2]), fold32(s0[1], s0[3]), fold32(s1[0], s1[2]), fold32(s1[1], s1[3])} + f32x4{bq[0], bq[1], lo_f(b2q), hi_f(b2q)}};
            unsigned o2[2];
            gelu_pack<1>(y, o2);
            pr[NT - 1] = o2[0]; pr[5] = o2[1];
          }
          if constexpr (NT == 4) put(pr[0], pr[1], pr[2], pr[3], uo);
          if constexpr (NT == 5) put(pr[4], pr[5], pr[4], pr[5], sel(lane < 32, uo + 4096 - g * (1024 + KS_), OOB));
          else put(pr[5], pr[5], pr[5], pr[5], sel(lane < 16, uo + 3072 - KS_, OOB));
          STAMP(4);
          DRAIN();
          if (arrive(lb, 6) == 3u) { await(L, 2, 4u); putflag(WS_UF + (ft * 6 + dg) * FS, lane); }
          STAMP(15);
        }
        gemm2(latec, IC(LATE ? 0 : NE), f2, IC(2 * NT - 6 + LATE));
      };
      if (dg < 4) gemm1(IC(5));
      gemm1(IC(4));
  };
  if (role == 0) utile(std::false_type{});
  if (role == 1) utile(std::true_type{});
  if (role == 2) {
    {
      LANEVARS;
        const int cq = (lane & 7) ^ (2 * ((rr >> 1) & 1)) ^ (4 * (m & 1));
        const int vq = sel(cq < 7, (8 * m + rr) * (N3_ * 2) + hp * 112 + cq * 16, OOB);
        const int va = (r0 + 16 + 8 * m + rr) * (D_ * 2) + ((lane & 7) ^ (4 * (m & 1) + (rr >> 1))) * 16;
        const int so_ = WS_ST + min(max(r0 - 16 + min(64 * m + lane, 207), 0), S_ - 1) * 16;
        auto pcs = [&](int pp, int from, int to) AI {
          const bool wr = rotq >= 7 - pp;
#pragma unroll
          for (int n = from; n < to; ++n) {
            if (n < 6) piece(false, vq, (pp + 2) % 3, fs + sel(wr, (pp - 7) * 64 * N3_ * 2, pp * 64 * N3_ * 2), n);
            else piece(true, va, (pp + 2) % 3, rotq * 128 + sel(wr, (pp - 7) * 128, pp * 128), n - 6);
          }
        };
        u64 e0 = peek(rs_ws, WS_E);
        pcs(1, 0, LA0);
        asm volatile("s_waitcnt vmcnt(%1)" : "+v"(e0) : "n"(LA0) : "memory");
        mktag(e0);
        WAITLG();
        STAMP(11);
        BAR();
        auto warm = [&](int e) AI {
          const auto rs_wo = __builtin_amdgcn_make_buffer_rsrc((void*)wo, 0, D_ * D_ * 2, RSRC_W3);
          const int j = idx - 28, o = WORD ? (e + 7) % 11 : e;
          if (o < 3) touch(rs_w2, (lane >> 2) * (S_ * 2) + (lane & 3) * 128, (448 * m + 256 + 48 * j + 16 * o) * (S_ * 2) + (xcd ^ SX) * 512, lb + L_DUMMY);
          else if (o < 7) touch(rs_wo, lane * 128, ((o - 3) * 16 + j * 4 + m) * 8192, lb + L_DUMMY);
          else touch(rs_w2, (lane >> 2) * (S_ * 2) + (lane & 3) * 128, (448 * m + 64 * j + 16 * (o - 7)) * (S_ * 2) + (xcd ^ SX) * 512, lb + L_DUMMY);
        };
        u32x4 sg;
        SFOR(6, n0,
          constexpr int n = n0 + 1, la = n < 3 ? LA0 : LA1, lp_ = n < 4 ? LA0 : LA1;
          pcs(n, lp_, 13);
          if constexpr (n == 3) asm volatile("buffer_load_dwordx4 %0, %1, %2, 0 offen sc1" : "=v"(sg) : "v"(so_), "s"(rs_ws) : "memory");
          if constexpr (n < 6) pcs(n + 1, 0, la);
          if constexpr (n == 4) {
            asm volatile("" : "+v"(sg));
            _Pragma("unroll 1")
            for (unsigned spins = 0; spins < SPIN_CAP; ++spins) {
              if (__all((sg.y == (unsigned)tag) & (sg.w == (unsigned)(tag >> 32)) & (sg.x == sg.z))) break;
              __builtin_amdgcn_s_sleep(4);
              asm volatile("buffer_load_dwordx4 %0, %1, %2, 0 offen sc1\n\ts_waitcnt vmcnt(0)" : "=v"(sg) : "v"(so_), "s"(rs_ws) : "memory");
            }
            *(LAS float*)(L + A_MEAN + (64 * m + lane) * 4) = -lo_f(sg.x);
            *(LAS float*)(L + A_RSTD + (64 * m + lane) * 4) = __builtin_amdgcn_rcpf(hi_f(sg.x));
            WAITLG();
          }
          if (n >= WST && idx >= 28) {
            SFOR(WPI, e, warm((n - WST) * WPI + e););
            WAITVM((n < 6 ? la : 0) + WPI);
          } else {
            WAITVM(n < 6 ? la : 0);
          }
          if constexpr (HK1 == 2) STAMP(20 + n);
          BAR();
        );
        sig(lb, 3);
        if (WRM && idx >= 28) {
          if (WSL) touch(rs_w2, lane * (S_ * 2), (448 * m + 64 * (idx - 28)) * (S_ * 2) + (xcd ^ SX) * 512 + 384, lb + L_DUMMY);
#pragma unroll
          for (int e = (7 - WST) * WPI; e < WN; ++e) warm(e);
          DRAIN();
        }
    }
    if (idx < 28) { u32x4 f2[8][4]; gemm2(std::false_type{}, IC(0), f2, IC(0)); }
    FINISH();
  } else {
        auto att = [&](auto jc) AI {
          constexpr int J = decltype(jc)::value, NH = J ? 2 : 4, H0 = J ? 0 : 2;
          const int H = m & 1;
          LANEVARS;
          f32x4 qa[6][6], qh[NH];
          int wa[2], xa[2][2];
#pragma unroll
          for (int i = 0; i < 2; ++i) {
            const int by = sel((i == 1) & (p4 == 3), 112, 56 * H + 32 * i + 8 * p4);
            wa[i] = A_W + (8 * g + q4) * 128 + (((by >> 4) ^ (2 * ((q4 >> 1) | ((g & 1) << 1)))) << 4) + (by & 8);
          }
          int wb[2] = {wa[0] + A_WS + 12288, wa[1] + A_WS + 12288};
#pragma unroll
          for (int i = 0; i < 4; ++i) xa[i >> 1][i & 1] = (i >> 1) * A_X1 + c * 128 + (((4 * (i & 1) + g) ^ ((c >> 1) & 7)) << 4);
          asm volatile("" : "+v"(wa[0]), "+v"(wa[1]), "+v"(wb[0]), "+v"(wb[1]), "+v"(xa[0][0]), "+v"(xa[0][1]), "+v"(xa[1][0]), "+v"(xa[1][1]));
          u32x4 wf[6], xf[7];
          u32x2 lo;
          auto rdx = [&](int k, int t) AI {
            const int sg = ((k >> 1) + 2) % 3;
            xf[t] = *(const LAS u32x4*)(L + xa[sg ? 1 : 0][k & 1] + (sg == 2 ? A_XS : 0) + (t == 6 ? 0 : (1 + 6 * J + t) * 2048));
          };
          auto rdw = [&](int k, int i, int hf) AI {
            const int o = (((k >> 1) + 2) % 3) * 2 + (k & 1);
            const LAS char* p = L + (o < 3 ? wa[i & 1] + o * 12288 : wb[i & 1] + (o - 3) * 12288) + (i >> 1) * 4096;
            if (hf) { const u32x2 hi = tr_read(p + 512); wf[i] = u32x4{lo.x, lo.y, hi.x, hi.y}; }
            else lo = tr_read(p);
          };
          const bool c0 = c == 0;
          const int exa = A_X1 + H * 4096 + lane * 16;
          DRAIN();
          STAMP(11);
          BAR();
          STAMP(12);
          PRIO(2);
#pragma unroll
          for (int t = 0; t < 7; ++t) rdx(0, t);
#pragma unroll
          for (int i = 0; i < 10; ++i) rdw(0, i >> 1, i & 1);
          gettag();
          SB();
          SFOR(14, k,
            if constexpr (HK1 == 2 && (k & 1)) STAMP(21 + (k >> 1));
            SFOR(6, ci,
              constexpr int i = (k == 13 && J == 0) ? (ci + 2) % 6 : ci;
              SFOR(7, t,
                if constexpr (t < 6) {
                  if constexpr (i < 4) mfma_ip<k == 0>(qa[t][i], wf[i], xf[t]); else mfma_ip<k == 0>(qa[t][i], xf[t], wf[i]);
                } else if constexpr (i >= H0 && i < H0 + NH) {
                  if constexpr (i < 4) mfma_ip<k == 0>(qh[i - H0], wf[i], xf[6]); else mfma_ip<k == 0>(qh[i - H0], xf[6], wf[i]);
                  if constexpr (k < 13 && i == H0 + NH - 1 && J == 0) rdx(k + 1, 6);
                }
                if constexpr (ci == 0 && t < 2) rdw(k, 5, t);
                if constexpr (ci == (k == 13 ? 0 : H0) && t == 2) { SFOR(4, j, xf[6][j] = sel(c0, 0x3f803f80u, xf[6][j]);); asm volatile("" : "+v"(xf[6])); }
                if constexpr ((k & 1) && k < 13 && ci == 1 && t == 1) { WAITLG(); BAR(); }
                if constexpr (k < 13 && ci >= 1 && (t == 2 || t == 3)) rdw(k + 1, ci - 1, t - 2);
                if constexpr (k < 13 && ci == 5 && t < 6) rdx(k + 1, t);
                if constexpr (k < 13 && J == 1 && ci == 2 && t == 0) rdx(k + 1, 6);
                if constexpr (k == 13 && t == 2 && ci >= 2 && ci < NH + 2) {
                  constexpr int ip = H0 + ci - 2;
                  if constexpr (ip < 4) *(LAS f32x4*)(L + sel(c0, A_CS + (H * 96 + 16 * ip + 4 * g) * 4, L_DUMMY)) = qh[ci - 2];
                  else *(LAS float*)(L + sel(g == 0, A_CS + (H * 96 + 16 * ip + c) * 4, L_DUMMY)) = qh[ci - 2][0];
                }
                if constexpr (k == 13 && J == 0 && t == 4 && ci >= 2) *(LAS f32x4*)(L + exa + (ci - 2) * 1024) = qa[5][ci];
                SB();
              );
            );
          );
          STAMP(2);
          const bool odd = c & 1;
          float mc[7], rc[6];
          f32x2 rk[7];
#pragma unroll
          for (int t = 0; t < 7; ++t) {
            mc[t] = *(const LAS float*)(L + A_MEAN + (96 * J + 16 * t + c) * 4);
            if (t) rc[t - 1] = *(const LAS float*)(L + A_RSTD + (96 * J + 16 * t + c) * 4);
            const LAS float* p = (const LAS float*)(L + A_RSTD + (96 * J + 16 * t + 4 * g + (c & 1)) * 4);
            rk[t] = f32x2{p[0], p[2]};
          }
          f32x2 mk[2][2];
          {
            const int d0 = 16 + (c & 14) - 4 * g, hi = min(r0 + 96 * J + c, 14);
#pragma unroll
            for (int e = 0; e < 4; ++e) {
              const int dk = d0 - 16 * (e >> 1) - 2 * (e & 1);
              mk[0][e >> 1][e & 1] = sel((dk >= 0) & (dk <= hi), 0.f, -3.0e38f);
              mk[1][e >> 1][e & 1] = sel((dk >= 0) & (dk <= 14), 0.f, -3.0e38f);
            }
          }
          gbar4(L, lb, 4);
          STAMP(13);
          f32x4 c4[4], ex[4];
          float cv[2];
#pragma unroll
          for (int i = 0; i < 4; ++i) c4[i] = *(const LAS f32x4*)(L + A_CS + (H * 96 + 16 * i + 4 * g) * 4);
#pragma unroll
          for (int i = 0; i < 2; ++i) cv[i] = *(const LAS float*)(L + A_CS + (H * 96 + 64 + 16 * i + c) * 4);
#pragma unroll
          for (int i = 0; i < 4; ++i) { if constexpr (J) ex[i] = *(const LAS f32x4*)(L + exa + i * 1024); else ex[i] = qh[i & (NH - 1)]; }
          sig(lb, 5);
          auto pk4 = [&](f32x4 v) AI { return u32x2{pack2(v[0], v[1]), pack2(v[2], v[3])}; };
          auto frag = [&](f32x4 a, f32x4 b, float mu, int i) AI {
            const u32x2 l = pk4(c4[i] * mu + a), h = pk4(c4[i + 1] * mu + b);
            return u32x4{l.x, l.y, h.x, h.y};
          };
          f32x4 s0[6], s1[6];
          u32x4 kp = frag(ex[0], ex[1], mc[0], 2);
#pragma unroll
          for (int u = 0; u < 6; ++u) {
            const u32x4 qf = frag(qa[u][0], qa[u][1], mc[u + 1], 0);
            s0[u] = mfma16(kp, qf, Z4);
            kp = frag(qa[u][2], qa[u][3], mc[u + 1], 2);
            s1[u] = mfma16(kp, qf, Z4);
          }
          SB();
          f32x4 m4[7];
#pragma unroll
          for (int t = 0; t < 7; ++t) m4[t] = *(const LAS f32x4*)(L + A_MEAN + (96 * J + 16 * t + 4 * g) * 4);
          STAMP(14);
          f32x2 lg[6][2];
          float mx[6], inv[6];
#pragma unroll
          for (int u = 0; u < 6; ++u) {
            const float sc = rc[u] * (0.18898223650461362f * 1.4426950408889634f);
#pragma unroll
            for (int e = 0; e < 2; ++e) {
              const f32x4 s = e ? s1[u] : s0[u];
              lg[u][e] = __builtin_elementwise_fma(f32x2{sel(odd, s[1], s[0]), sel(odd, s[3], s[2])}, rk[u + e] * sc, mk[u ? 1 : 0][e]);
            }
          }
#pragma unroll
          for (int u = 0; u < 6; ++u) mx[u] = gmax(max2(max2(lg[u][0][0], lg[u][0][1]), max2(lg[u][1][0], lg[u][1][1])));
          SB();
          u32x2 vv[7][2];
#pragma unroll
          for (int t = 0; t < 7; ++t)
#pragma unroll
            for (int i = 0; i < 2; ++i) vv[t][i] = pk4(m4[t] * cv[i] + (t ? qa[t - 1][4 + i] : ex[2 + i]));
          SB();
#pragma unroll
          for (int u = 0; u < 6; ++u)
#pragma unroll
            for (int e = 0; e < 2; ++e) {
              lg[u][e] -= mx[u];
              lg[u][e] = f32x2{__builtin_amdgcn_exp2f(lg[u][e][0]), __builtin_amdgcn_exp2f(lg[u][e][1])};
            }
#pragma unroll
          for (int u = 0; u < 6; ++u) { const f32x2 t = lg[u][0] + lg[u][1]; inv[u] = __builtin_amdgcn_rcpf(gsum(t[0] + t[1])); }
          const f32x2 par = {sel(odd, 0.f, 1.f), sel(odd, 1.f, 0.f)};
          f32x4 o0[6], o1[6];
#pragma unroll
          for (int u = 0; u < 6; ++u) {
            const f32x2 iv = par * inv[u];
            u32x4 pp;
#pragma unroll
            for (int e = 0; e < 4; ++e) { const f32x2 p = lg[u][e >> 1] * rk[u + (e >> 1)], t = iv * p[e & 1]; pp[e] = pack2(t[0], t[1]); }
            o0[u] = mfma16(u32x4{vv[u][0].x, vv[u][0].y, vv[u + 1][0].x, vv[u + 1][0].y}, pp, Z4);
            o1[u] = mfma16(u32x4{vv[u][1].x, vv[u][1].y, vv[u + 1][1].x, vv[u + 1][1].y}, pp, Z4);
          }
          PRIO(3);
#pragma unroll
          for (int u = 0; u < 6; ++u) {
            const u32x2 l = pk4(o0[u]), h = pk4(o1[u]);
            __builtin_amdgcn_raw_buffer_store_b128(u32x4{l.x, l.y, h.x, h.y}, rs_ws, WS_O + ((12 * rg + 6 * J + u) * 16 + 2 * hp + H) * 1024 + lane * 16, 0, AUX_SC1);
          }
          SB();
          };
        if (m < 2) att(std::integral_constant<int, 0>{}); else att(std::integral_constant<int, 1>{});
        STAMP(4);
    LANEVARS;
    DRAIN();
    if (arrive(lb, 6) == 3u) putflag(WS_OF + (rg * 8 + hp) * FS, lane);
    STAMP(15);
    if (idx < 28) { u32x4 f2[6][4]; gemm2(std::true_type{}, IC(0), f2, IC(1)); }
    if (bid == EPB) {
      __builtin_amdgcn_s_sleep(64);
      poll_flags<16>((const GAS u64*)(ws + WS_UF + (m * 64 + lane) * FS), tag);
      if (arrive(lb, 8) == 3u) putflag(WS_E, lane);
    }
  }
  FINISH();
}

extern "C" void kernel_launch(void* const* d_in, const int* in_sizes, int n_in,
                              void* d_out, int out_size, void* d_ws, size_t ws_size,
                              hipStream_t stream) {
  (void)in_sizes; (void)n_in; (void)out_size; (void)ws_size;
  (void)hipFuncSetAttribute((const void*)fused_block, hipFuncAttributeMaxDynamicSharedMemorySize, LDS_BYTES);
  fused_block<<<256, 512, LDS_BYTES, stream>>>((const u16*)d_in[0], (const u16*)d_in[1], (const u16*)d_in[2],
                                               (const u16*)d_in[4], (const u16*)d_in[5], (const u16*)d_in[6],
                                               (const u16*)d_in[7], (u16*)d_out, (char*)d_ws);
}
```

```cpp
#include <hip/hip_runtime.h>
#include <stdint.h>
#include <type_traits>
#include <utility>

#define __float_as_uint(x) __builtin_bit_cast(unsigned, (float)(x))
#define __uint_as_float(x) __builtin_bit_cast(float, (unsigned)(x))
#define __all(p) (__builtin_amdgcn_ballot_w64(!(p)) == 0)
typedef unsigned short u16;
typedef unsigned long long u64;
using bfv8 = __attribute__((ext_vector_type(8))) __bf16;
using bfv2 = __attribute__((ext_vector_type(2))) __bf16;
using f32x4 = __attribute__((ext_vector_type(4))) float;
using f32x2 = __attribute__((ext_vector_type(2))) float;
using u32x2 = __attribute__((ext_vector_type(2))) unsigned int;
using u32x4 = __attribute__((ext_vector_type(4))) unsigned int;
using u32x16 = __attribute__((ext_vector_type(16))) unsigned int;
using s16x4 = __attribute__((ext_vector_type(4))) short;

#define LAS __attribute__((address_space(3)))
#define GAS __attribute__((address_space(1)))
#define AI __attribute__((always_inline))
#define SB() __builtin_amdgcn_sched_barrier(0)
#define SGB(mask, n) __builtin_amdgcn_sched_group_barrier(mask, n, 0)
#define DRAIN() asm volatile("s_waitcnt vmcnt(0)" ::: "memory")
#define WAITVM(n) asm volatile("s_waitcnt vmcnt(%0)" ::"n"(n) : "memory")
#define WAITLG() asm volatile("s_waitcnt lgkmcnt(0)" ::: "memory")
#define RLX_AGENT __ATOMIC_RELAXED, __HIP_MEMORY_SCOPE_AGENT
#define PRIO(n) __builtin_amdgcn_s_setprio(n)

constexpr int RQ1 = 2, RQ2 = 5, PLP = 0, PEP = 1, PE2 = 2, PL2 = 1, PP2 = 0, SX = 1, WST = 7, WPI = 1, WN = 7, WORD = 0, WRM = 1, NB2L = 3, IL = 1, ADLY = 4, LA0 = 6, LA1 = 6, W0 = 0, PE1 = 1, PL1 = 2, NB1 = 8, PKE = 1, NSG = 2, NE = 2, WSL = 1, TP = 20, SG2 = 0, SLP = 4, OV = 0, GDLY = 0, GDL2 = 4, AW = 0, W5 = 8, W6 = 12, EW = 7, EG = 2, TW = 9, ND = 1, LA6 = 6, AV = 14, HW = 0, HO = 0, RO = 0, DF = 0;
constexpr int S_ = 2048, D_ = 448, F_ = 1792, N3_ = 1344;
constexpr int AUX_SC1 = 16;
constexpr int RSRC_W3 = 0x00020000;

constexpr int FS = 128;
constexpr int WS_E = 0, WS_HF = 4096, WS_UF = WS_HF + 128 * FS, WS_OF = WS_UF + 168 * FS;
constexpr int WS_ST = 65536;
constexpr int KS_ = 28 * 1024;
constexpr int WS_H = 131072;
constexpr int WS_U = WS_H + 64 * KS_;
constexpr int WS_O = WS_U + 56 * KS_;
constexpr int WS_END = WS_O + 132 * 16384;

constexpr int L_LATE = 65536, SS = 16384;
constexpr int L_HB = 131072;
constexpr int L_CNT = 162176;
constexpr int L_SA = 162432;
constexpr int L_DUMMY = 162560;
constexpr int LDS_BYTES = 163840;
constexpr int HB_PITCH = 928, AT_PITCH = 144;

__device__ __forceinline__ unsigned pack2(float a, float b) {
  bfv2 v; v[0] = (__bf16)a; v[1] = (__bf16)b;
  return __builtin_bit_cast(unsigned, v);
}
__device__ __forceinline__ float bfr(float f) { return __uint_as_float(pack2(f, 0.f) << 16); }
__device__ __forceinline__ float lo_f(unsigned u) { return __uint_as_float(u << 16); }
__device__ __forceinline__ float hi_f(unsigned u) { return __uint_as_float(u & 0xffff0000u); }
__device__ __forceinline__ float dot2(unsigned a, unsigned b, float c) {
  return __builtin_amdgcn_fdot2_f32_bf16(__builtin_bit_cast(bfv2, a), __builtin_bit_cast(bfv2, b), c, false);
}
__device__ __forceinline__ f32x4 mfma16(u32x4 a, u32x4 b, f32x4 c) {
  return __builtin_amdgcn_mfma_f32_16x16x32_bf16(__builtin_bit_cast(bfv8, a), __builtin_bit_cast(bfv8, b), c, 0, 0, 0);
}
__device__ __forceinline__ float gsum(float v) {
  unsigned u = __float_as_uint(v);
  auto a = __builtin_amdgcn_permlane32_swap(u, u, false, false);
  float s = __uint_as_float(a[0]) + __uint_as_float(a[1]);
  u = __float_as_uint(s);
  auto b = __builtin_amdgcn_permlane16_swap(u, u, false, false);
  return __uint_as_float(b[0]) + __uint_as_float(b[1]);
}
__device__ __forceinline__ float max2(float a, float b) { return __builtin_elementwise_maximum(a, b); }
template <bool FIRST> __device__ __forceinline__ void mfma_ip(f32x4& d, u32x4 a, u32x4 b) {
  if constexpr (FIRST) asm volatile("v_mfma_f32_16x16x32_bf16 %0, %1, %2, 0" : "=&v"(d) : "v"(a), "v"(b));
  else asm volatile("v_mfma_f32_16x16x32_bf16 %0, %1, %2, %0" : "+v"(d) : "v"(a), "v"(b));
}
__device__ __forceinline__ float gmax(float v) {
  unsigned u = __float_as_uint(v);
  auto a = __builtin_amdgcn_permlane32_swap(u, u, false, false);
  float s = max2(__uint_as_float(a[0]), __uint_as_float(a[1]));
  u = __float_as_uint(s);
  auto b = __builtin_amdgcn_permlane16_swap(u, u, false, false);
  return max2(__uint_as_float(b[0]), __uint_as_float(b[1]));
}
template <int CTRL>
__device__ __forceinline__ float dpp_add(float v) {
  return v + __uint_as_float(__builtin_amdgcn_update_dpp(0u, __float_as_uint(v), CTRL, 0xf, 0xf, true));
}
__device__ __forceinline__ void dma16(__amdgpu_buffer_rsrc_t rs, int voff, int soff, int dst) {
  asm volatile("s_mov_b32 m0, %3\n\ts_nop 0\n\tbuffer_load_dwordx4 %0, %1, %2 offen lds" ::"v"(voff), "s"(rs), "s"(soff), "s"(dst) : "memory");
}
__device__ __forceinline__ void touch(__amdgpu_buffer_rsrc_t rs, int voff, int soff, int dst) {
  asm volatile("s_mov_b32 m0, %3\n\ts_nop 0\n\tbuffer_load_dword %0, %1, %2 offen lds" ::"v"(voff), "s"(rs), "s"(soff), "s"(dst) : "memory");
}
__device__ __forceinline__ void ldx4(u32x4 (&a)[4], __amdgpu_buffer_rsrc_t rs, int voff, int soff) {
  asm volatile("buffer_load_dwordx4 %0, %4, %5, %6 offen\n\tbuffer_load_dwordx4 %1, %4, %5, %6 offen offset:224\n\t"
               "buffer_load_dwordx4 %2, %4, %5, %6 offen offset:448\n\tbuffer_load_dwordx4 %3, %4, %5, %6 offen offset:672"
               : "=&v"(a[0]), "=&v"(a[1]), "=&v"(a[2]), "=&v"(a[3]) : "v"(voff), "s"(rs), "s"(soff) : "memory");
}
__device__ __forceinline__ void dma4(__amdgpu_buffer_rsrc_t rs, int v, int so, int step, int dst) {
  const int s1 = so + step - 1024, s2 = so + 2 * step - 2048, s3 = so + 3 * step - 3072;
  asm volatile("s_mov_b32 m0, %6\n\ts_nop 0\n\t"
               "buffer_load_dwordx4 %0, %1, %2 offen lds\n\t"
               "buffer_load_dwordx4 %0, %1, %3 offen offset:1024 lds\n\t"
               "buffer_load_dwordx4 %0, %1, %4 offen offset:2048 lds\n\t"
               "buffer_load_dwordx4 %0, %1, %5 offen offset:3072 lds"
               ::"v"(v), "s"(rs), "s"(so), "s"(s1), "s"(s2), "s"(s3), "s"(dst) : "memory");
}
__device__ __forceinline__ void ld4(u32x4 (&a)[4], __amdgpu_buffer_rsrc_t rs, int v, int so, int step) {
  asm volatile("buffer_load_dwordx4 %0, %4, %5, %6 offen\n\tbuffer_load_dwordx4 %1, %4, %5, %7 offen\n\t"
               "buffer_load_dwordx4 %2, %4, %5, %8 offen\n\tbuffer_load_dwordx4 %3, %4, %5, %9 offen"
               : "=&v"(a[0]), "=&v"(a[1]), "=&v"(a[2]), "=&v"(a[3])
               : "v"(v), "s"(rs), "s"(so), "s"(so + step), "s"(so + 2 * step), "s"(so + 3 * step) : "memory");
}
template <int N> __device__ __forceinline__ void wait4(u32x4 (&a)[4]) {
  asm volatile("s_waitcnt vmcnt(%4)" : "+v"(a[0]), "+v"(a[1]), "+v"(a[2]), "+v"(a[3]) : "n"(N) : "memory");
}
__device__ __forceinline__ void turn_pair(u32x4 (&a)[4], u32x4 (&b)[4]) {
  unsigned xa[8][2], xb[8][2];
#pragma unroll
  for (int r = 0; r < 8; ++r)
#pragma unroll
    for (int e = 0; e < 2; ++e) {
      const u32x4 v = r < 4 ? a[r] : b[r - 4];
      xa[r][e] = __builtin_amdgcn_update_dpp(v[e], v[2 + e], 0x128, 0xf, 0xc, false);
      xb[r][e] = __builtin_amdgcn_update_dpp(v[2 + e], v[e], 0x128, 0xf, 0x3, false);
    }
#pragma unroll
  for (int j = 0; j < 4; ++j)
#pragma unroll
    for (int q = 0; q < 4; ++q) {
      a[j][q] = __builtin_amdgcn_perm(xa[2 * q + 1][j >> 1], xa[2 * q][j >> 1], (j & 1) ? 0x07060302u : 0x05040100u);
      b[j][q] = __builtin_amdgcn_perm(xb[2 * q + 1][j >> 1], xb[2 * q][j >> 1], (j & 1) ? 0x07060302u : 0x05040100u);
    }
#pragma unroll
  for (int j = 0; j < 4; ++j) asm volatile("" : "+v"(a[j]), "+v"(b[j]));
}
__device__ __forceinline__ u32x2 tr_read(const LAS char* p) {
  return __builtin_bit_cast(u32x2, __builtin_amdgcn_ds_read_tr16_b64_v4i16((LAS s16x4*)p));
}
__device__ __forceinline__ float ln_sd(float var) { return bfr(__builtin_amdgcn_sqrtf(bfr(bfr(var) + 1e-5f))); }

__device__ __forceinline__ float row_sum(float v) {
  v = dpp_add<0xB1>(v);
  v = dpp_add<0x4E>(v);
  v = dpp_add<0x141>(v);
  return dpp_add<0x140>(v);
}
__device__ __forceinline__ unsigned ln2_rows(u32x4 (&f)[4], bool act) {
  f32x2 d[16];
#pragma unroll
  for (int e = 0; e < 16; ++e) d[e] = f32x2{lo_f(f[e >> 2][e & 3]), hi_f(f[e >> 2][e & 3])};
  f32x2 t[4];
#pragma unroll
  for (int e = 0; e < 4; ++e) t[e] = (d[e] + d[4 + e]) + (d[8 + e] + d[12 + e]);
  t[0] = (t[0] + t[1]) + (t[2] + t[3]);
  const float mean = row_sum(t[0][0] + t[0][1]) * (1.0f / D_);
  const float mr = bfr(mean);
  const float mb = act ? mr : 0.f;
#pragma unroll
  for (int e = 0; e < 16; ++e) d[e] -= f32x2{mb, mb};
#pragma unroll
  for (int e = 0; e < 4; ++e) t[e] = d[e] * d[e];
#pragma unroll
  for (int e = 4; e < 16; ++e) t[e & 3] = __builtin_elementwise_fma(d[e], d[e], t[e & 3]);
  t[0] = (t[0] + t[1]) + (t[2] + t[3]);
  const float dm = mean - mr;
  const float var = fmaxf(row_sum(t[0][0] + t[0][1]) * (1.0f / D_) - dm * dm, 0.f);
  const float s1 = ln_sd(var), r1 = __builtin_amdgcn_rcpf(s1);
  const float r2 = __builtin_amdgcn_rcpf(ln_sd(var * r1 * r1));
  const float a = r1 * r2, cc = -bfr(dm * r1) * r2;
#pragma unroll
  for (int e = 0; e < 16; ++e) {
    const f32x2 v = __builtin_elementwise_fma(d[e], f32x2{a, a}, f32x2{cc, cc});
    f[e >> 2][e & 3] = pack2(v[0], v[1]);
  }
  return (__float_as_uint(mr) >> 16) | __float_as_uint(s1);
}
__device__ __forceinline__ float solo(float v) { asm("" : "+v"(v)); return v; }
__device__ __forceinline__ float vadd(float a, float b) { return solo(a + b); }
__device__ __forceinline__ float vmul(float a, float b) { return solo(a * b); }
__device__ __forceinline__ f32x4 add4(f32x4 a, f32x4 b) { return f32x4{vadd(a[0], b[0]), vadd(a[1], b[1]), vadd(a[2], b[2]), vadd(a[3], b[3])}; }

constexpr unsigned SPIN_CAP = 1u << 18;
template <int SL>
__device__ __forceinline__ void poll_flags(const GAS u64* p, u64 tag) {
#pragma unroll 1
  for (unsigned spins = 0; spins < SPIN_CAP; ++spins) {
    const u64 v = __hip_atomic_load(p, RLX_AGENT);
    if (__all(v == tag)) break;
    __builtin_amdgcn_s_sleep(SL);
  }
  __builtin_amdgcn_fence(__ATOMIC_ACQUIRE, "wavefront");
}

__device__ __forceinline__ unsigned arrive(int lb, int i) {
  unsigned r;
  asm volatile("s_waitcnt lgkmcnt(0)\n\ts_mov_b64 exec, 1\n\tds_add_rtn_u32 %0, %1, %2\n\ts_mov_b64 exec, -1\n\ts_waitcnt lgkmcnt(0)"
               : "=&v"(r) : "v"(lb + L_CNT + 4 * i), "v"(1u) : "memory");
  return __builtin_amdgcn_readfirstlane(r);
}
__device__ __forceinline__ void sig(int lb, int i) {
  asm volatile("s_waitcnt lgkmcnt(0)\n\ts_mov_b64 exec, 1\n\tds_add_u32 %0, %1\n\ts_mov_b64 exec, -1" ::"v"(lb + L_CNT + 4 * i), "v"(1u) : "memory");
}
template <int I> __device__ __forceinline__ void post(int sa, unsigned one) {
  asm volatile("ds_add_u32 %0, %1 offset:%2" ::"v"(sa), "v"(one), "n"(4 * I) : "memory");
}
__device__ __forceinline__ unsigned look(LAS char* L, int i) {
  return __hip_atomic_load((LAS unsigned*)(L + L_CNT) + i, __ATOMIC_RELAXED, __HIP_MEMORY_SCOPE_WORKGROUP);
}
__device__ __forceinline__ void check(LAS char* L, unsigned v, int i, unsigned target) {
  unsigned t, n, a;
  asm volatile("s_nop 0\n\tv_readfirstlane_b32 %0, %3\n\ts_cmp_lt_u32 %0, %5\n\ts_cbranch_scc1 .Lcw%=\n.Lcb%=:\n\t.subsection 1\n"
               ".Lcw%=:\n\ts_mov_b32 %1, 0x400000\n\tv_mov_b32 %2, %4\n"
               ".Lcl%=:\n\tds_read_b32 %3, %2\n\ts_waitcnt lgkmcnt(0)\n\tv_readfirstlane_b32 %0, %3\n\ts_cmp_ge_u32 %0, %5\n\t"
               "s_cbranch_scc1 .Lcb%=\n\ts_sleep 0\n\ts_sub_u32 %1, %1, 1\n\ts_cbranch_scc0 .Lcl%=\n\ts_branch .Lcb%=\n\t.subsection 0"
               : "=&s"(t), "=&s"(n), "=&v"(a), "+v"(v) : "s"((int)(unsigned)(size_t)L + L_CNT + 4 * i), "n"(target) : "memory", "scc");
}
__device__ __forceinline__ void await(LAS char* L, int i, unsigned target) {
  unsigned t, n, v;
  asm volatile("s_mov_b32 %1, 0x400000\n.Lal%=:\n\tds_read_b32 %2, %3\n\ts_waitcnt lgkmcnt(0)\n\tv_readfirstlane_b32 %0, %2\n\ts_cmp_ge_u32 %0, %4\n\t"
               "s_cbranch_scc1 .Lab%=\n\ts_sleep 0\n\ts_sub_u32 %1, %1, 1\n\ts_cbranch_scc0 .Lal%=\n.Lab%=:"
               : "=&s"(t), "=&s"(n), "=&v"(v) : "v"((int)(unsigned)(size_t)L + L_CNT + 4 * i), "n"(target) : "memory", "scc");
}
__device__ __forceinline__ void gbar4(LAS char* L, int lb, int i) { sig(lb, i); await(L, i, 4u); }

#define BAR() __builtin_amdgcn_s_barrier()
#define LANEVARS                                                                                         \
  int lane;                                                                                              \
  asm volatile("v_mbcnt_lo_u32_b32 %0, -1, 0\n\tv_mbcnt_hi_u32_b32 %0, -1, %0" : "=v"(lane));             \
  const int c = lane & 15, g = lane >> 4, q4 = c >> 2, p4 = c & 3, rr = lane >> 3;                       \
  (void)c; (void)g; (void)q4; (void)p4; (void)rr
constexpr f32x4 Z4 = {0.f, 0.f, 0.f, 0.f};
constexpr int OOB = 0x40000000;
template <int A, int B> __device__ __forceinline__ void pin(f32x4 (&a)[A][B]) {
#pragma unroll
  for (int i = 0; i < A; ++i)
#pragma unroll
    for (int t = 0; t < B; ++t) asm volatile("" : "+v"(a[i][t]));
}
template <class T> __device__ __forceinline__ T sel(bool c, T a, T b) { return __builtin_unpredictable(c) ? a : b; }
__device__ __forceinline__ int trbase(int i, int g, int q4, int p4) {
  return (4 * g + q4) * 128 + (((2 * i + (p4 >> 1)) ^ (2 * ((q4 >> 1) | ((g & 1) << 1)))) << 4) + 8 * (p4 & 1);
}
__device__ __forceinline__ void slot_frags(const LAS char* L, const int (&tb)[4], int so, u32x4 (&a)[4]) {
#pragma unroll
  for (int i = 0; i < 4; ++i) {
    const u32x2 lo = tr_read(L + tb[i] + so), hi = tr_read(L + tb[i] + so + 2048);
    a[i] = u32x4{lo.x, lo.y, hi.x, hi.y};
  }
}
__device__ __forceinline__ u64 peek(__amdgpu_buffer_rsrc_t rs, int off) {
  u64 v;
  asm volatile("buffer_load_dwordx2 %0, %1, %2, 0 offen sc1" : "=v"(v) : "v"(off), "s"(rs) : "memory");
  return v;
}
__device__ __forceinline__ void settle(__amdgpu_buffer_rsrc_t rs, int off, u64 first, u64 tag) {
  unsigned n;
  asm volatile("v_cmp_ne_u64 vcc, %4, %1\n\ts_cbranch_vccz .Lsb%=\n\ts_mov_b32 %0, 0x40000\n"
               ".Lsl%=:\n\tbuffer_load_dwordx2 %1, %2, %3, 0 offen sc1\n\ts_waitcnt vmcnt(0)\n\tv_cmp_ne_u64 vcc, %4, %1\n\t"
               "s_cbranch_vccz .Lsb%=\n\ts_sleep %5\n\ts_sub_u32 %0, %0, 1\n\ts_cbranch_scc0 .Lsl%=\n.Lsb%=:"
               : "=&s"(n), "+v"(first) : "v"(off), "s"(rs), "s"(tag), "n"(SLP) : "memory", "scc", "vcc");
}
template <class F, int... I> __device__ __forceinline__ void static_for(F f, std::integer_sequence<int, I...>) { (f(std::integral_constant<int, I>{}), ...); }
#define SFOR(n, var, ...) static_for([&](auto var##_c) AI { constexpr int var = decltype(var##_c)::value; __VA_ARGS__ }, std::make_integer_sequence<int, n>{})
template <int OFF> __device__ __forceinline__ void ld1_a(u32x4& v, __amdgpu_buffer_rsrc_t rs, int vo, int so) {
  asm volatile("buffer_load_dwordx4 %0, %1, %2, %3 offen offset:%4 sc1" : "=v"(v) : "v"(vo), "s"(rs), "s"(so), "n"(OFF) : "memory");
}
template <int N> __device__ __forceinline__ void wait1(u32x4& a) { asm volatile("s_waitcnt vmcnt(%1)" : "+v"(a) : "n"(N) : "memory"); }
template <int N> __device__ __forceinline__ void wait2(u32x4& a, u32x4& b) { asm volatile("s_waitcnt vmcnt(%2)" : "+v"(a), "+v"(b) : "n"(N) : "memory"); }

template <int NK, int NR, int NT, bool G2, int KX, int NB, class LD, class PUT, class HK>
__device__ __forceinline__ void kloop(LAS char* L, const int (&tb)[4], int jit0, u32x4 (&fr)[NR][4], u32x4 (&hv)[NB][NT],
                                      f32x4 (&acc)[4][NT], LD ld, PUT put, HK hook) {
  constexpr int NL = NK * NT;
  auto mm = [&](auto kc, auto ic, auto tc) AI {
    constexpr int k = decltype(kc)::value, i = decltype(ic)::value, t = decltype(tc)::value;
    f32x4& a = k >= KX ? acc[t & 3][i] : acc[i][t];
    const f32x4 cz = k == 0 ? Z4 : a;
    a = (G2 && k < KX) ? mfma16(hv[k % NB][t], fr[k % NR][i], cz) : mfma16(fr[k % NR][i], hv[k % NB][t], cz);
    asm volatile("" : "+v"(a));
  };
  SFOR(NK - 2, k,
    u32x2 lo;
    SFOR(NT, t,
      constexpr int n = k * NT + t, is = NB * NT + n < NL ? NB * NT + n : NL;
      wait1<is - n - 1>(hv[k % NB][t]);
      if constexpr (t == 0) hook(std::integral_constant<int, k>{});
      SFOR(4, i,
        mm(k_c, i_c, t_c);
        constexpr int e = t * 4 + i;
        if constexpr (k >= 1 && k - 1 + NR < NK && e < 8) {
          const LAS char* q = L + tb[e >> 1] + ((jit0 + k - 1) & 3) * SS;
          if constexpr (e & 1) { const u32x2 hi = tr_read(q + 2048); fr[k - 1][e >> 1] = u32x4{lo.x, lo.y, hi.x, hi.y}; }
          else lo = tr_read(q);
        }
        SB();
      );
      if constexpr (k + NB < NK) ld(std::integral_constant<int, k + NB>{}, std::integral_constant<int, t>{});
      SB();
    );
  );
  SFOR(NT, t,
    wait2<NT - 1 - t>(hv[(NK - 2) % NB][t], hv[(NK - 1) % NB][t]);
    if constexpr (t == 0) hook(std::integral_constant<int, NK - 2>{});
    SFOR(8, e,
      mm(std::integral_constant<int, NK - 2 + (e & 1)>{}, std::integral_constant<int, (e >> 1)>{}, t_c);
      constexpr int d = t * 4 + (e >> 1) - 2;
      if constexpr ((e & 1) && d >= 0) put(std::integral_constant<int, d & 3>{}, std::integral_constant<int, d / 4>{});
      SB();
    );
  );
  put(std::integral_constant<int, 2>{}, std::integral_constant<int, NT - 1>{});
  put(std::integral_constant<int, 3>{}, std::integral_constant<int, NT - 1>{});
}
template <int N> __device__ __forceinline__ void gelu_pack(const f32x4 (&v)[N], unsigned (&pr)[2 * N]) {
  f32x4 z[N];
#pragma unroll
  for (int e = 0; e < N; ++e) z[e] = v[e] * (v[e] * v[e] * -0.1029432395800235f - 2.302208198144325f);
  SB();
#pragma unroll
  for (int e = 0; e < 4 * N; ++e) z[e >> 2][e & 3] = __builtin_amdgcn_exp2f(z[e >> 2][e & 3]);
  SB();
#pragma unroll
  for (int e = 0; e < N; ++e) z[e] += 1.0f;
#pragma unroll
  for (int e = 0; e < 4 * N; ++e) z[e >> 2][e & 3] = __builtin_amdgcn_rcpf(z[e >> 2][e & 3]);
  SB();
#pragma unroll
  for (int e = 0; e < N; ++e) { z[e] *= v[e]; pr[2 * e] = pack2(z[e][0], z[e][1]); pr[2 * e + 1] = pack2(z[e][2], z[e][3]); }
}
__device__ __forceinline__ float fold32(float a, float b) {
  const auto s = __builtin_amdgcn_permlane32_swap(__float_as_uint(a), __float_as_uint(b), false, false);
  return vadd(__uint_as_float(s[0]), __uint_as_float(s[1]));
}
#define PINV(v) asm volatile("" : "+v"(v))
#define IC(n) std::integral_constant<int, (n)>{}
constexpr int g1buf(int nt, int t) { return ((t + 8 - nt) & 3) * SS; }
constexpr int L_B1 = 162816;
constexpr int A_W = 26624, A_WS = 24576, A_X1 = 100352, A_XS = 26624;
constexpr int A_MEAN = 153600, A_RSTD = A_MEAN + 1024, A_CS = A_RSTD + 1024;
#define FINISH() __builtin_amdgcn_endpgm()
__global__ __launch_bounds__(512, 2) void fused_block(const u16*, const u16*, const u16*, const u16*, const u16*, const u16*,
                                                      const u16*, u16*, char*) {
  extern __shared__ __attribute__((aligned(16))) char smem[];
  LAS char* L = (LAS char*)smem;
  const int lb = (int)(unsigned)(size_t)L;
  const auto kp = __builtin_amdgcn_kernarg_segment_ptr();
  u32x16 ka; u32x2 kb;
  asm volatile("s_load_dwordx16 %0, %2, 0x0\n\ts_load_dwordx2 %1, %2, 0x40" : "=&s"(ka), "=&s"(kb) : "s"(kp));
  SB();
  const int w = __builtin_amdgcn_readfirstlane((int)threadIdx.x >> 6);
  const int bid = blockIdx.x, xcd = bid & 7, idz = bid >> 3;
  const int m = w & 3;
#define ARGP(i) ((const u16*)(((u64)ka[2 * (i) + 1] << 32) | ka[2 * (i)]))
#define ARGWAIT() asm volatile("s_waitcnt lgkmcnt(0)" : "+s"(ka), "+s"(kb) : "s"(kp) : "memory")
  u64 tag;
  const int un = xcd * 16 + idz, row0 = 32 * (un >> 1) + 8 * (un & 1);
  const bool isln = idz < 16;
  u32x4 hx[4];
  u64 e1;
  if (isln && w >= 4) {
    int lv = sel(((int)threadIdx.x & 15) < 14, (((int)threadIdx.x >> 4) & 3) * (D_ * 2) + ((int)threadIdx.x & 15) * 16, OOB);
    int ls = (row0 + 4 * (m & 1) + 8 * (m & 2)) * (D_ * 2), z = 0;
    asm volatile("" : "+v"(lv), "+v"(z));
    asm volatile("" : "+s"(ls));
    ARGWAIT();
    ldx4(hx, __builtin_amdgcn_make_buffer_rsrc((void*)ARGP(0), 0, S_ * D_ * 2, RSRC_W3), lv, ls);
    e1 = peek(__builtin_amdgcn_make_buffer_rsrc((void*)(((u64)kb[1] << 32) | kb[0]), 0, WS_END, RSRC_W3), z);
    BAR();
  } else {
    u64 pc;
    unsigned co = sel(isln, 0, (((idz - 16) * 8 + w) * 8 + ((int)threadIdx.x & 7)) * 128);
    asm volatile(".Lfb_start:\n\ts_getpc_b64 %0\n\ts_mov_b32 m0, %2\n\tv_min_u32 %1, .Lfb_end-.Lfb_start-8, %1\n\t"
                 "global_load_lds_dword %1, %0\n\t.subsection 2\n.Lfb_end:\n\ts_endpgm\n\t.subsection 0" : "=&s"(pc), "+v"(co) : "s"(lb + L_DUMMY) : "memory");
    *((LAS unsigned*)(L + L_CNT) + ((int)threadIdx.x & 31)) = 0u;
  }
  SB();
  auto gettag = [&]() AI {
    const u32x2 tv = *(const LAS u32x2*)(L + L_CNT + 128);
    tag = ((u64)(unsigned)__builtin_amdgcn_readfirstlane((int)tv.y) << 32) | (unsigned)__builtin_amdgcn_readfirstlane((int)tv.x);
  };

  const int ng = 24 - 6 * (xcd & 1);
  const int k4 = idz + ng - 32, k5 = idz - 16 - 4 * (xcd & 1);
  const bool isg = k4 >= 0;
  const int lp = sel(k5 < 0, ((0x24 - 3 * ((8 >> xcd) & 1) + 0xc0) >> (k4 & 6)) & 3, k5 >> 2);
  int ft = 7 * (xcd >> 1) + 4 * (xcd & 1) + lp, dg = sel(k5 < 0, 4 + (idz & 1), idz & 3);
  int idx = sel(isg, 6 * lp + dg, ng + idz);
  asm volatile("" : "+s"(idx));
  int off1 = (dg * 43) >> 4;
  const int q = 22 * (xcd >> 2) + 8 * (xcd & 1) + idx - ng;
  int hp = 2 * (xcd & 2) + (q & 3), rg = q >> 2;
  int rotq = RQ1 * (q & 3) + RQ2 * rg + 70;
  rotq -= 7 * ((rotq * 147) >> 10);
  constexpr int EPB = 56;
  auto k1 = [&](int j) AI { return (j + off1) & 15; };
  int reg = (w >> 2) * L_LATE + m * 4096;
  int fv, fw, fs, fs1, role = (isg ? 0 : 2) + (w >> 2);
  {
    const int l = (int)threadIdx.x & 63, rr = l >> 3, ch = l & 7;
    const int v1 = ((m * 512 + rr) * F_ + ft * 64) * 2 + (ch ^ (2 * ((rr >> 1) & 3))) * 16;
    const int cq = ch ^ (2 * ((rr >> 1) & 1)) ^ (4 * (m & 1));
    const int vq = sel(cq < 7, (8 * m + rr) * (N3_ * 2) + hp * 112 + cq * 16, OOB);
    const int va = (192 * rg + 16 + 8 * m + rr) * (D_ * 2) + (ch ^ (4 * (m & 1) + (rr >> 1))) * 16;
    fv = sel(isg, v1, vq);
    fw = va;
    fs = sel(isg, k1(0) * 32 * (F_ * 2), rotq * (64 * N3_ * 2));
    fs1 = sel(isg, k1(1) * 32 * (F_ * 2), rotq * 128);
  }
  asm volatile("" : "+v"(fv), "+v"(fw));
  asm volatile("" : "+s"(fs), "+s"(fs1), "+s"(ft), "+s"(dg), "+s"(reg), "+s"(off1), "+s"(rotq), "+s"(role), "+s"(hp), "+s"(rg));
  const int r0 = 192 * rg;
  ARGWAIT();
  if (!(isln && w >= 4)) BAR();
  const u16 *x = ARGP(0), *wqkv = ARGP(1), *wo = ARGP(2), *w1 = ARGP(3), *b1 = ARGP(4), *w2 = ARGP(5), *b2 = ARGP(6);
  u16* out = (u16*)ARGP(7);
  char* ws = (char*)(((u64)kb[1] << 32) | kb[0]);
  const auto rs_x = __builtin_amdgcn_make_buffer_rsrc((void*)x, 0, S_ * D_ * 2, RSRC_W3);
  const auto rs_ws = __builtin_amdgcn_make_buffer_rsrc((void*)ws, 0, WS_END, RSRC_W3);
  const auto rs_w1 = __builtin_amdgcn_make_buffer_rsrc((void*)w1, 0, S_ * F_ * 2, RSRC_W3);
  const auto rs_w2 = __builtin_amdgcn_make_buffer_rsrc((void*)w2, 0, F_ * S_ * 2, RSRC_W3);
  auto putflag = [&](int off, int lane) AI {
    __builtin_amdgcn_raw_buffer_store_b128(u32x4{(unsigned)tag, (unsigned)(tag >> 32), (unsigned)tag, (unsigned)(tag >> 32)}, rs_ws,
                                           sel(lane < 8, off + lane * 16, OOB), 0, AUX_SC1);
  };
  auto mktag = [&](u64 e0) AI {
    e0 = e0 * 0x9E3779B97F4A7C15ull + 0x632BE59BD9B4E019ull;
    *(LAS u64*)(L + L_CNT + 128) = e0;
    tag = ((u64)(unsigned)__builtin_amdgcn_readfirstlane((int)(e0 >> 32)) << 32) | (unsigned)__builtin_amdgcn_readfirstlane((int)e0);
  };
  const auto rs_wq = __builtin_amdgcn_make_buffer_rsrc((void*)wqkv, 0, D_ * N3_ * 2, RSRC_W3);
  auto piece = [&](bool late, int fv, int sg, int so, int e) AI {
    if (late) {
      if (e < 6 || m < 2) dma16(rs_x, e ? fv : sel(fv >= 32 * D_ * 2, fv - 32 * D_ * 2, OOB), so + (e ? e - 1 : 0) * 32 * (D_ * 2), lb + (sg ? A_X1 + (sg - 1) * A_XS : 0) + (m + 4 * e) * 1024);
    } else {
      if (e < 6) dma16(rs_wq, fv, so + (e / 3) * 32 * (N3_ * 2) + (e % 3) * 896, lb + A_W + sg * A_WS + e * 4096 + m * 1024);
    }
  };
  if (role == 0) {
    if (GDLY) __builtin_amdgcn_s_sleep(GDLY);
    if (GDL2 && isln) __builtin_amdgcn_s_sleep(GDL2);
    dma4(rs_w1, fv, fs, 8 * F_ * 2, lb + reg);
    dma4(rs_w1, fv, fs1, 8 * F_ * 2, lb + reg + SS);
  } else if (role == 2) {
    if (ADLY) __builtin_amdgcn_s_sleep(ADLY);
#pragma unroll
    for (int e = 0; e < 6; ++e) piece(false, fv, 2, fs, e);
    if (AW) WAITVM(0);
#pragma unroll
    for (int e = 0; e < 7; ++e) piece(true, fw, 2, fs1, e);
  }
  asm volatile("" : "+s"(role));
  if (!(role & 1)) {
  } else if (isln) {
    const int hbase = sel(isg, L_HB, A_W + A_WS);
    PRIO(3);
    LANEVARS;
    const bool act = c < 14;
    int hb = sel(act, hbase + (4 * m + g) * HB_PITCH + c * 16, L_DUMMY);
    int tp = hbase + (4 * (g & 1) + q4) * HB_PITCH + (16 * (g >> 1) + 4 * p4) * 2 + m * 64;
    int ho = WS_H + (un >> 1) * KS_ + (2 * m + (g >> 1)) * 1024 + (2 * (un & 1) + (g & 1)) * 256 + c * 16;
    int h3 = sel(m < 2, ho + 3 * 8192, sel((lane < 16) & (m == 3), WS_ST + (row0 + (c & 7) + 2 * (c & 8)) * 16, OOB));
    asm volatile("" : "+v"(hb), "+v"(tp), "+v"(ho), "+v"(h3));
    asm volatile("s_waitcnt vmcnt(1)" : "+v"(hx[0]), "+v"(hx[1]), "+v"(hx[2]), "+v"(hx[3])::"memory");
    const unsigned sa = ln2_rows(hx, act);
#pragma unroll
    for (int e = 0; e < 4; ++e) *(LAS u32x4*)(L + hb + e * 224) = hx[e];
    *(LAS unsigned*)(L + L_SA + (4 * m + g) * 4) = sa;
    asm volatile("s_waitcnt vmcnt(0)" : "+v"(e1)::"memory");
    mktag(e1);
    gbar4(L, lb, 0);
    u32x4 hs[4];
#pragma unroll
    for (int u = 0; u < 4; ++u) {
      const u32x2 a0 = tr_read(L + tp + u * 256), a1 = tr_read(L + tp + u * 256 + 8 * HB_PITCH);
      hs[u] = u32x4{a0.x, a0.y, a1.x, a1.y};
    }
    const unsigned sb = *(const LAS unsigned*)(L + L_SA + c * 4);
#pragma unroll
    for (int u = 0; u < 3; ++u) __builtin_amdgcn_raw_buffer_store_b128(hs[u], rs_ws, ho + u * 8192, 0, AUX_SC1);
    if (m == 3) hs[3] = u32x4{sb, (unsigned)tag, sb, (unsigned)(tag >> 32)};
    __builtin_amdgcn_raw_buffer_store_b128(hs[3], rs_ws, h3, 0, AUX_SC1);
    SB();
    sig(lb, 7);
    DRAIN();
    if (arrive(lb, 1) == 3u) putflag(WS_HF + un * FS, lane);
  } else {
    u64 e0 = peek(rs_ws, WS_E);
    asm volatile("s_waitcnt vmcnt(0)" : "+v"(e0)::"memory");
    mktag(e0);
    sig(lb, 0);
    sig(lb, 7);
  }
  asm volatile("" : "+s"(role));
  int idy = idx;
  asm volatile("" : "+s"(idy));
  const u64 tm = idy < 12 ? 0x59d28518a320820ull : idy < 24 ? 0xbdab49ca2f839acull : 0x18820ffbbcdeb38ull;
  const int tile = sel(xcd & 1, (int)(tm >> (5 * (idy - 12 * ((idy * 43) >> 9)))) & 31, idy);
  const int st = 4 * (xcd ^ SX) + (tile & 3), dt = tile >> 2;
  const int rotl = dt % 6;
  auto pos2 = [&](bool late, int p) AI { const int v = p + rotl; return 14 * m + (late ? 8 + (v >= 6 ? v - 6 : v) : (p + 2 * dt) & 7); };
  constexpr int SP = HW ? F_ : S_;
  const int stx = HW ? 7 * (xcd >> 1) + 4 * (xcd & 1) + min(tile & 3, 3 - (xcd & 1)) : st;
  auto ldw2 = [&](u32x4 (&a)[4], int vr, int p) AI { ld4(a, HW ? rs_w1 : rs_w2, vr, (pos2(false, p & 6) * 32 + 16 * (p & 1)) * (SP * 2), SP * 2); };
  auto w2lane = [&](int lane) AI { return ((32 * ((lane >> 3) & 1) + 4 * (lane >> 4)) * SP + stx * 64 + 8 * (lane & 7)) * 2; };
  auto gemm2 = [&](auto latec, auto nec, auto& f2, auto idc) AI {
    constexpr bool LATE = decltype(latec)::value;
    constexpr int NR2 = LATE ? 6 : 8, NK2 = LATE ? 10 : 8, N0 = decltype(nec)::value & 15, T0 = decltype(nec)::value >> 4;
    {
      constexpr int NB2 = LATE ? NB2L : 3;
      u32x4 uv[NB2][4];
      f32x4 acc[4][4];
      {
        LANEVARS;
        PRIO(PP2);
        const int w2E = (rr * SP + stx * 64) * 2 + ((lane & 7) ^ (2 * ((rr >> 1) & 3))) * 16;
        int tb[4];
#pragma unroll
        for (int i = 0; i < 4; ++i) tb[i] = reg + trbase(i, g, q4, p4);
        const int v0 = lane * 16;
        const int ta = 4 * dt + 3 * ((lane >> 3) & 1), dgp = sel(ta < 20, (ta * 13) >> 6, (ta >> 2) - 1);
        const int fo = WS_UF + ((7 * m + (LATE ? 4 : 0) + min(lane & 7, LATE ? 2 : 3)) * 6 + dgp) * FS;
        const int oo = WS_OF + (((st * 43) >> 7) * 8 + 2 * m + (lane & 1)) * FS;
        auto fill = [&](int p, int dst) AI { dma4(HW ? rs_w1 : rs_w2, w2E, pos2(true, p) * 32 * (SP * 2), 8 * SP * 2, lb + dst); };
        constexpr int XS = L_HB - L_LATE;
        u64 f0;
        const auto rs_wo = __builtin_amdgcn_make_buffer_rsrc((void*)wo, 0, D_ * D_ * 2, RSRC_W3);
        const int woE = (rr * D_ + dt * 64) * 2 + ((lane & 7) ^ (2 * ((rr >> 1) & 3))) * 16;
        auto fillo = [&](int e) AI { if (HO) dma4(rs_w1, (rr * F_ + (7 * (xcd >> 1) + 4 * (xcd & 1)) * 64) * 2 + (lane & 7) * 16, ((4 * m + e) * 32 + 64 * dt) * (F_ * 2), 8 * F_ * 2, lb + L_LATE + m * 4096 + e * SS); else dma4(rs_wo, woE, (4 * m + ((e + RO * st) & 3)) * 28 * (D_ * 2), 8 * D_ * 2, lb + L_LATE + m * 4096 + e * SS); };
        const int ub = WS_U + 4 * dt * 1024, ob = WS_O + (4 * st * 16 + 4 * m) * 1024;
        u64 o0 = 0;
        auto ldu = [&](auto pc, auto tc) AI {
          constexpr int p = decltype(pc)::value, t = decltype(tc)::value;
          if constexpr (LATE && p == 6 && t == 0) { asm volatile("" : "+v"(o0)); settle(rs_ws, oo, o0, tag); }
          if constexpr (LATE && p == 6 - NB2 && t == 0) o0 = peek(rs_ws, oo);
          if constexpr (LATE && p >= 6) ld1_a<0>(uv[p % NB2][t], rs_ws, v0, ob + t * 16384 + ((p - 6 + RO * st) & 3) * 1024);
          else ld1_a<t * 1024>(uv[p % NB2][t], rs_ws, v0, ub + pos2(LATE, p) * KS_);
        };
        auto first = [&]() AI {
          settle(rs_ws, fo, f0, tag);
          PRIO(LATE ? PL2 : PE2);
          SFOR(NB2 * 4, n, ldu(IC(n / 4), IC(n % 4)););
          SB();
        };
        if constexpr (!LATE) {
          const int vr = w2lane(lane);
#pragma unroll
          for (int p = N0; p < 8; ++p) {
            ldw2(f2[p], vr, p);
            if (!SG2 && p == N0 + NSG - 1) { WAITVM(4 * NSG); sig(lb, 2); }
          }
          await(L, 3, 4u);
          await(L, 4, 4u);
          auto turn = [&](auto pc, auto nc) AI {
            constexpr int p = decltype(pc)::value, n = decltype(nc)::value;
            if constexpr (p < T0) return;
            wait4<n>(f2[2 * p]);
            wait4<n>(f2[2 * p + 1]);
            turn_pair(f2[2 * p], f2[2 * p + 1]);
            SB();
          };
#define TURN(p, n) turn(std::integral_constant<int, p>{}, std::integral_constant<int, n>{})
          fill(0, reg); fill(1, reg + SS);
          if (SG2) { WAITVM(4 * (8 - N0) + 8); sig(lb, 2); }
          if (IL) TURN(0, 32);
          await(L, 5, 4u);
          fill(2, reg + 2 * SS); fill(3, reg + 3 * SS);
          if (IL) TURN(1, 32);
          fill(4, L_HB + m * 5120); fill(5, L_LATE + reg + 3 * SS);
          if (IL) TURN(2, 32);
          if (OV == 2) sig(lb, 31);
          if (OV) f0 = peek(rs_ws, fo);
          fillo(0); fillo(1); fillo(2);
          WAITVM(12 + (OV ? 1 : 0));
          sig(lb, 16 + m);
          if (OV) {
            asm volatile("s_waitcnt vmcnt(12)" : "+v"(f0)::"memory");
            first();
            TURN(3, 24);
          } else {
            f0 = peek(rs_ws, fo);
            TURN(3, 13);
            WAITVM(1);
            sig(lb, 16 + m);
            asm volatile("s_waitcnt vmcnt(0)" : "+v"(f0)::"memory");
            first();
          }
        } else {
          auto take = [&](int r, int so) AI {
            slot_frags(L, tb, so, f2[r]);
            asm volatile("s_waitcnt lgkmcnt(0)" : "+v"(f2[r][0]), "+v"(f2[r][1]), "+v"(f2[r][2]), "+v"(f2[r][3])::"memory");
          };
          if (OV == 2) { await(L, 31, 4u); f0 = peek(rs_ws, fo); asm volatile("s_waitcnt vmcnt(0)" : "+v"(f0)::"memory"); first(); }
          await(L, 16 + m, 1u);
          if (OV == 1) f0 = peek(rs_ws, fo);
          take(5, 3 * SS);
          fillo(3);
          if (OV == 1) { asm volatile("s_waitcnt vmcnt(4)" : "+v"(f0)::"memory"); first(); }
          if (!OV) f0 = peek(rs_ws, fo);
#pragma unroll
          for (int r = 0; r < 4; ++r) slot_frags(L, tb, r * SS - L_LATE, f2[r]);
          slot_frags(L, tb, XS + m * 1024, f2[4]);
          sig(lb, 20 + m);
          if constexpr (DF == 2 && decltype(idc)::value == 1) { WAITVM(5); if (arrive(lb, 6) == 3u) putflag(WS_OF + (rg * 8 + hp) * FS, lane); }
          if (!OV) {
            await(L, 16 + m, 2u);
            asm volatile("s_waitcnt vmcnt(0)" : "+v"(f0)::"memory");
            if constexpr (DF == 1 && decltype(idc)::value == 1) { if (arrive(lb, 6) == 3u) putflag(WS_OF + (rg * 8 + hp) * FS, lane); }
            first();
          }
        }
        int wr[4];
#pragma unroll
        for (int e = 0; e < 4; ++e) wr[e] = reg + g * 256 + (LATE ? c ^ (2 * e + (g >> 1)) : (c + 8 * e + 4 * (g >> 1)) & 15) * 16;
        kloop<NK2, NR2, 4, true, LATE ? 6 : 99, NB2>(L, tb, 0, f2, uv, acc, ldu,
            [&](auto ic, auto tc) AI {
              constexpr int i = LATE ? decltype(tc)::value : decltype(ic)::value, t = LATE ? decltype(ic)::value : decltype(tc)::value;
              *(LAS f32x4*)(L + wr[(t & 1) + (LATE ? 2 * (i & 1) : 0)] + i * SS + t * 1024) = acc[i][t];
            }, [&](auto kc) AI {
              constexpr int k = decltype(kc)::value;
              if constexpr (!LATE && k == NK2 - 2) await(L, 20 + m, 1u);
              if constexpr (OV && !LATE && k == 0) sig(lb, 16 + m);
              if constexpr (OV && LATE && k == 1) await(L, 16 + m, 2u);
            });
      }
      if constexpr (LATE) {
        sig(lb, 10);
      } else {
        LANEVARS;
        const int eo = ((st * 64 + 8 * rr + m) * D_ + dt * 64 + (lane & 7) * 8) * 2;
        u32x4 xr[2];
        float bs[2];
#pragma unroll
        for (int e = 0; e < 2; ++e) {
          xr[e] = __builtin_amdgcn_raw_buffer_load_b128(rs_x, eo + e * 4 * D_ * 2, 0, 0);
          bs[e] = lo_f(*(const GAS u16*)(b2 + st * 64 + 8 * rr + 4 * e + m));
        }
        int ra[2];
#pragma unroll
        for (int e = 0; e < 2; ++e) ra[e] = (m * 16 + ((lane & 7) >> 1)) * 1024 + (lane & 1) * 512 + ((rr + 8 * e + 4 * (lane & 3)) & 15) * 16;
        gbar4(L, lb, 9);
        f32x4 tm[4][4], base[4];
#pragma unroll
        for (int j = 0; j < 4; ++j)
#pragma unroll
          for (int e = 0; e < 4; ++e) tm[j][e] = *(const LAS f32x4*)(L + ra[e >> 1] + j * 4096 + (e & 1) * 256);
        SB();
#pragma unroll
        for (int e = 0; e < 4; ++e) {
          const unsigned a = xr[e >> 1][2 * (e & 1)], b = xr[e >> 1][2 * (e & 1) + 1];
          const float s = bs[e >> 1];
          base[e] = add4(add4(add4(f32x4{lo_f(a), hi_f(a), lo_f(b), hi_f(b)}, f32x4{s, s, s, s}), add4(tm[0][e], tm[1][e])), add4(tm[2][e], tm[3][e]));
          asm volatile("" : "+v"(base[e]));
        }
#pragma unroll
        for (int e = 0; e < 2; ++e) {
          ra[e] = L_LATE + (rr >> 1) * SS + ((lane & 7) >> 1) * 1024 + (lane & 1) * 512 + ((8 * (rr & 1) + 4 * e + m) ^ ((lane & 3) + 2 * (rr & 2))) * 16;
          asm volatile("" : "+v"(ra[e]));
        }
        PRIO(3);
        await(L, 10, 4u);
#pragma unroll
        for (int j = 0; j < 4; ++j)
#pragma unroll
          for (int e = 0; e < 4; ++e) tm[j][e] = *(const LAS f32x4*)(L + ra[e >> 1] + j * 4096 + (e & 1) * 256);
        SB();
        const auto rs_out = __builtin_amdgcn_make_buffer_rsrc((void*)out, 0, S_ * D_ * 2, RSRC_W3);
#pragma unroll
        for (int e = 0; e < 2; ++e) {
          const f32x4 s0 = (base[2 * e] + tm[0][2 * e]) + tm[1][2 * e] + (tm[2][2 * e] + tm[3][2 * e]);
          const f32x4 s1 = (base[2 * e + 1] + tm[0][2 * e + 1]) + tm[1][2 * e + 1] + (tm[2][2 * e + 1] + tm[3][2 * e + 1]);
          __builtin_amdgcn_raw_buffer_store_b128(u32x4{pack2(s0[0], s0[1]), pack2(s0[2], s0[3]), pack2(s1[0], s1[1]), pack2(s1[2], s1[3])},
                                                 rs_out, eo + e * 4 * D_ * 2, 0, AUX_SC1);
        }
      }
    }
    asm volatile("s_nop %0" ::"n"(decltype(idc)::value));
    FINISH();
  };
  auto utile = [&](auto latec) AI {
    constexpr bool LATE = decltype(latec)::value;
      auto gemm1 = [&](auto ntc) AI {
        constexpr int NT = decltype(ntc)::value, NL = 8 * NT, NB = NB1, TPN = TP * 4;
        const int t0 = NT == 5 ? 5 * dg : 4 * dg + 4;
        u32x4 fr[8][4], hv[NB], f2[LATE ? 6 : 8][4];
        LANEVARS;
        PRIO(LATE ? PLP : PEP);
        const int v0 = lane * 16, v4 = v0 + 4096, wr = reg + v0;
        int ho[8];
        auto ld = [&](auto nc) AI {
          constexpr int n = decltype(nc)::value, t = n >> 3;
          ld1_a<(t & 3) * 1024>(hv[n % NB], rs_ws, t < 4 ? v0 : v4, ho[n & 7]);
        };
        auto start = [&](int fo, u64 f0) AI {
          settle(rs_ws, fo, f0, tag);
          PRIO(LATE ? PL1 : PE1);
#pragma unroll
          for (int p = 0; p < 8; ++p) ho[p] = WS_H + (m * 16 + k1((LATE ? 8 : 0) + p)) * KS_ + t0 * 1024;
          SFOR(NB, n, ld(n_c););
          SB();
        };
        {
          const int w1E = ((m * 512 + rr) * F_ + ft * 64) * 2 + ((lane & 7) ^ (2 * ((rr >> 1) & 3))) * 16;
          auto fill = [&](int j) AI { dma4(rs_w1, w1E, k1(j) * 32 * (F_ * 2), 8 * F_ * 2, lb + reg + ((j >> 2) & 1) * L_LATE + (j & 3) * SS); };
          int tb[2][4];
#pragma unroll
          for (int i = 0; i < 8; ++i) tb[i >> 2][i & 3] = (i >> 2) * L_LATE + m * 4096 + trbase(i & 3, g, q4, p4);
          auto take = [&](int j) AI { slot_frags(L, tb[j >> 2], (j & 3) * SS, fr[j]); };
          auto taken = [&](int j) AI { asm volatile("s_waitcnt lgkmcnt(0)" : "+v"(fr[j][0]), "+v"(fr[j][1]), "+v"(fr[j][2]), "+v"(fr[j][3])::"memory"); };
          const int fo = WS_HF + (m * 32 + 2 * (((LATE ? 8 : 0) + ((lane >> 1) & 7) + off1) & 15) + (lane & 1)) * FS;
          u64 f0;
          if constexpr (!LATE) {
            if constexpr (NT == 4) WAITVM(W0); else WAITVM(W5);
            if constexpr (W6 < 12) { fill(2); WAITVM(W6); }
#pragma unroll
            for (int j = W6 < 12 ? 3 : 2; j < 8; ++j) fill(j);
            await(L, 0, 4u);
            gettag();
            SFOR(8, j, WAITVM(28 + (j > 8 - PKE)); take(j); taken(j); if constexpr (j == 8 - PKE) f0 = peek(rs_ws, fo); fill(8 + j););
            WAITVM(17); sig(lb, 12 + m);
            asm volatile("s_waitcnt vmcnt(%1)" : "+v"(f0) : "n"(4 * PKE) : "memory");
            start(fo, f0);
            WAITVM(NB); sig(lb, 12 + m);
          } else {
            if (m == 0) {
              const auto rs_b1 = __builtin_amdgcn_make_buffer_rsrc((void*)b1, 0, F_ * 2, RSRC_W3);
              await(L, 0, 4u);
              touch(rs_b1, sel(lane < 32, lane * 4, OOB), ft * 128, lb + L_B1);
            }
            await(L, 12 + m, 1u);
            f0 = peek(rs_ws, fo);
#pragma unroll
            for (int j = 0; j < 4; ++j) take(j);
            asm volatile("s_waitcnt vmcnt(0)" : "+v"(f0)::"memory");
            if (m == 0) sig(lb, 11);
            start(fo, f0);
            await(L, 12 + m, 2u);
#pragma unroll
            for (int j = 4; j < 8; ++j) take(j);
#pragma unroll
            for (int j = 0; j < 8; ++j) taken(j);
            sig(lb, 30);
          }
        }
        const int ra = (lane >> 5) * L_LATE + ((LATE ? 2 : 0) + (g & 1)) * 1024 + m * 256 + c * 16;
        const int ba = L_B1 + (16 * ((LATE ? 2 : 0) + (g & 1)) + 4 * m + 2 * (lane >> 5)) * 2;
        const int uo = WS_U + (2 * ft + (LATE ? 1 : 0)) * KS_ + (t0 + g) * 1024 + m * 256 + c * 16;
        const int vr = w2lane(lane);
        int sa = sel(lane == 0, lb + L_CNT, OOB);
        unsigned one = 1u;
        PINV(sa); PINV(one);
        f32x4 acc[2][4], pt[4];
        float bq[2], v[2], z[2];
        unsigned pr[6], cv, cr;
        auto rd = [&](int t, int o) AI {
#pragma unroll
          for (int e = 0; e < 4; ++e) pt[e] = *(const LAS f32x4*)(L + ra + o + g1buf(NT, t) + e * 4096);
        };
        auto step = [&](auto sc, int t) AI {
          constexpr int s = decltype(sc)::value;
          if constexpr (s == 0) { pt[0] = add4(pt[0], pt[1]); PINV(pt[0]); }
          if constexpr (s == 1) { pt[2] = add4(pt[2], pt[3]); PINV(pt[2]); }
          if constexpr (s == 2) { pt[0] = add4(pt[0], pt[2]); PINV(pt[0]); }
          if constexpr (s == 3) {
#pragma unroll
            for (int e = 0; e < 2; ++e) { v[e] = vadd(fold32(pt[0][e], pt[0][2 + e]), bq[e]); PINV(v[e]); }
          }
          if constexpr (s == 4) {
#pragma unroll
            for (int e = 0; e < 2; ++e) {
              const float p = solo(__builtin_fmaf(vmul(v[e], v[e]), -0.1029432395800235f, -2.302208198144325f));
              z[e] = vmul(v[e], p); PINV(z[e]);
            }
          }
          if constexpr (s == 5) {
#pragma unroll
            for (int e = 0; e < 2; ++e) { z[e] = vadd(__builtin_amdgcn_exp2f(z[e]), 1.0f); PINV(z[e]); }
          }
          if constexpr (s == 6) {
            pr[t] = pack2(vmul(__builtin_amdgcn_rcpf(z[0]), v[0]), vmul(__builtin_amdgcn_rcpf(z[1]), v[1]));
            PINV(pr[t]);
          }
        };
        auto put = [&](unsigned a, unsigned b, unsigned cc, unsigned d, int off) AI {
          const auto s0 = __builtin_amdgcn_permlane32_swap(a, cc, false, false), s1 = __builtin_amdgcn_permlane32_swap(b, d, false, false);
          const auto r0 = __builtin_amdgcn_permlane16_swap(s0[0], s1[0], false, false), r1 = __builtin_amdgcn_permlane16_swap(s0[1], s1[1], false, false);
          __builtin_amdgcn_raw_buffer_store_b128(u32x4{r0[0], r1[0], r0[1], r1[1]}, rs_ws, off, 0, AUX_SC1);
          SB();
        };
        auto bias = [&](int o) AI { const unsigned b = *(const LAS unsigned*)(L + ba + o); bq[0] = lo_f(b); bq[1] = hi_f(b); };
        SFOR(NL, n,
          constexpr int t = n >> 3, p = n & 7, nw = !LATE && t == NT - 1 ? 4 * (p < NE ? p : NE) : 0;
          if constexpr (n == 9 && !LATE) check(L, cr, 30, 4u);
          if constexpr (n == 13) { check(L, cv, 11, 1u); bias(0); }
          if constexpr (t >= 2 && p == 4) { check(L, cv, 24 + t - 2, 8u); rd(t - 2, 0); if constexpr (NT == 5 && t == 2) post<29>(sa, one); }
          if constexpr (!LATE && n == NL - TPN) PRIO(3);
          if constexpr (t >= 2 && p == 3) cv = look(L, 24 + t - 2);
          if constexpr (n == 7 && !LATE) cr = look(L, 30);
          if constexpr (n == 11) cv = look(L, 11);
          if constexpr (n == NL - 2) { cr = look(L, 29); cv = look(L, 24 + NT - 2); }
          SB();
          wait1<(n + NB < NL ? n + NB : NL) - 1 - n + nw>(hv[n % NB]);
          SFOR(4, i,
            mfma_ip<p == 0>(acc[t & 1][i], fr[p][i], hv[n % NB]);
            if constexpr (t >= 1 && (p == 1 || p == 2) && (i & 1)) *(LAS f32x4*)(L + wr + g1buf(NT, t - 1) + (2 * p - 2 + (i >> 1)) * 1024) = acc[(t - 1) & 1][2 * p - 2 + (i >> 1)];
            if constexpr (t >= 2 && p >= 5 && 4 * (p - 5) + i >= 1 && 4 * (p - 5) + i < 8) step(IC(4 * (p - 5) + i - 1), t - 2);
            SB();
          );
          if constexpr (t >= 1 && p == 2) post<24 + t - 1>(sa, one);
          if constexpr (n + NB < NL) ld(IC(n + NB));
          if constexpr (!LATE && t == NT - 1 && p < NE) ldw2(f2[p], vr, p);
          SB();
        );
        PRIO(3);
        if constexpr (NT == 5) check(L, cr, 29, 8u);
        asm volatile("s_nop 7" ::: "memory");
#pragma unroll
        for (int i = 0; i < 4; ++i) *(LAS f32x4*)(L + wr + g1buf(NT, NT - 1) + i * 1024) = acc[(NT - 1) & 1][i];
        post<24 + NT - 1>(sa, one);
        if constexpr (LATE) post<4>(sa, one);
        check(L, cv, 24 + NT - 2, 8u);
        rd(NT - 2, 0);
        if constexpr (!LATE) post<3>(sa, one);
        SFOR(7, s, step(s_c, NT - 2););
        if constexpr (NT == 5) put(pr[0], pr[1], pr[2], pr[3], uo);
        else if constexpr (!LATE) put(pr[0], pr[1], pr[2], pr[2], sel(lane < 48, uo, OOB));
        if constexpr (LATE) {
          const unsigned b2q = *(const LAS unsigned*)(L + ba - 64);
          await(L, 24 + NT - 1, 8u);
          {
            f32x4 qa[8];
#pragma unroll
            for (int e = 0; e < 8; ++e) qa[e] = *(const LAS f32x4*)(L + ra - (e >> 2) * 2048 + g1buf(NT, NT - 1) + (e & 3) * 4096);
            post<5>(sa, one);
            const f32x4 s0 = (qa[0] + qa[1]) + (qa[2] + qa[3]), s1 = (qa[4] + qa[5]) + (qa[6] + qa[7]);
            f32x4 y[1] = {f32x4{fold32(s0[0], s0[2]), fold32(s0[1], s0[3]), fold32(s1[0], s1[2]), fold32(s1[1], s1[3])} + f32x4{bq[0], bq[1], lo_f(b2q), hi_f(b2q)}};
            unsigned o2[2];
            gelu_pack<1>(y, o2);
            pr[NT - 1] = o2[0]; pr[5] = o2[1];
          }
          if constexpr (NT == 4) put(pr[0], pr[1], pr[2], pr[3], uo);
          if constexpr (NT == 5) put(pr[4], pr[5], pr[4], pr[5], sel(lane < 32, uo + 4096 - g * (1024 + KS_), OOB));
          else put(pr[5], pr[5], pr[5], pr[5], sel(lane < 16, uo + 3072 - KS_, OOB));
          DRAIN();
          if (arrive(lb, 6) == 3u) { await(L, 2, 4u); putflag(WS_UF + (ft * 6 + dg) * FS, lane); }
        }
        gemm2(latec, IC(LATE ? 0 : NE), f2, IC(2 * NT - 6 + LATE));
      };
      if (dg < 4) gemm1(IC(5));
      gemm1(IC(4));
  };
  if (role == 0) utile(std::false_type{});
  if (role == 1) utile(std::true_type{});
  if (role == 2) {
    u32x4 f2[8][4];
    constexpr int NA = (7 - EW) * EG < 8 ? (7 - EW) * EG : 8;
    auto na = [](int n) constexpr { return n < EW ? 0 : (n + 1 - EW) * EG < 8 ? (n + 1 - EW) * EG : 8; };
    auto nt = [](int n) constexpr { return n < TW ? 0 : n + 1 - TW < 4 ? n + 1 - TW : 4; };
    {
      LANEVARS;
      const int vr = w2lane(lane);
        const int cq = (lane & 7) ^ (2 * ((rr >> 1) & 1)) ^ (4 * (m & 1));
        const int vq = sel(cq < 7, (8 * m + rr) * (N3_ * 2) + hp * 112 + cq * 16, OOB);
        const int va = (r0 + 16 + 8 * m + rr) * (D_ * 2) + ((lane & 7) ^ (4 * (m & 1) + (rr >> 1))) * 16;
        const int so_ = WS_ST + min(max(r0 - 16 + min(64 * m + lane, 207), 0), S_ - 1) * 16;
        auto pcs = [&](int pp, int from, int to) AI {
          const bool wr = rotq >= 7 - pp;
#pragma unroll
          for (int n = from; n < to; ++n) {
            if (n < 6) piece(false, vq, (pp + 2) % 3, fs + sel(wr, (pp - 7) * 64 * N3_ * 2, pp * 64 * N3_ * 2), n);
            else piece(true, va, (pp + 2) % 3, rotq * 128 + sel(wr, (pp - 7) * 128, pp * 128), n - 6);
          }
        };
        u64 e0 = peek(rs_ws, WS_E);
        if (AV < 14) WAITVM(AV);
        pcs(1, 0, LA0);
        asm volatile("s_waitcnt vmcnt(%1)" : "+v"(e0) : "n"(LA0) : "memory");
        mktag(e0);
        WAITLG();
        BAR();
        auto warm = [&](int e) AI {
          const auto rs_wo = __builtin_amdgcn_make_buffer_rsrc((void*)wo, 0, D_ * D_ * 2, RSRC_W3);
          const int j = idx - 28, o = WORD ? (e + 7) % 11 : e;
          if (o < 3) touch(rs_w2, (lane >> 2) * (S_ * 2) + (lane & 3) * 128, (448 * m + 256 + 48 * j + 16 * o) * (S_ * 2) + (xcd ^ SX) * 512, lb + L_DUMMY);
          else if (o < 7) touch(rs_wo, lane * 128, ((o - 3) * 16 + j * 4 + m) * 8192, lb + L_DUMMY);
          else touch(rs_w2, (lane >> 2) * (S_ * 2) + (lane & 3) * 128, (448 * m + 64 * j + 16 * (o - 7)) * (S_ * 2) + (xcd ^ SX) * 512, lb + L_DUMMY);
        };
        u32x4 sg;
        SFOR(6, n0,
          constexpr int n = n0 + 1, lb_ = n == 5 ? LA6 : n < 3 ? LA0 : LA1, la = lb_ < 12 ? lb_ : 12, lp_ = n == 6 ? LA6 : n < 4 ? LA0 : LA1;
          pcs(n, lp_, 13);
          if constexpr (n == 3) asm volatile("buffer_load_dwordx4 %0, %1, %2, 0 offen sc1" : "=v"(sg) : "v"(so_), "s"(rs_ws) : "memory");
          if constexpr (n < 6) pcs(n + 1, 0, lb_);
          if constexpr (n == 4) {
            asm volatile("" : "+v"(sg));
            _Pragma("unroll 1")
            for (unsigned spins = 0; spins < SPIN_CAP; ++spins) {
              if (__all((sg.y == (unsigned)tag) & (sg.w == (unsigned)(tag >> 32)) & (sg.x == sg.z))) break;
              __builtin_amdgcn_s_sleep(4);
              asm volatile("buffer_load_dwordx4 %0, %1, %2, 0 offen sc1\n\ts_waitcnt vmcnt(0)" : "=v"(sg) : "v"(so_), "s"(rs_ws) : "memory");
            }
            *(LAS float*)(L + A_MEAN + (64 * m + lane) * 4) = -lo_f(sg.x);
            *(LAS float*)(L + A_RSTD + (64 * m + lane) * 4) = __builtin_amdgcn_rcpf(hi_f(sg.x));
            WAITLG();
          }
          constexpr int ge = na(n) - na(n - 1);
          SFOR(ge, e, ldw2(f2[na(n - 1) + e], vr, na(n - 1) + e););
          if constexpr (nt(n) > nt(n - 1)) {
            static_assert(2 * nt(n) <= na(n - 2), "");
            wait4<63>(f2[2 * nt(n) - 2]); wait4<63>(f2[2 * nt(n) - 1]);
            turn_pair(f2[2 * nt(n) - 2], f2[2 * nt(n) - 1]);
            SB();
          }
          if (n >= WST && idx >= 28) {
            SFOR(WPI, e, warm((n - WST) * WPI + e););
            WAITVM((n < 6 ? la : 0) + WPI + 4 * ge);
          } else {
            WAITVM((n < 6 ? la : 0) + 4 * ge);
          }
          BAR();
        );
        sig(lb, 3);
        if (WRM && idx >= 28) {
          if (WSL) touch(rs_w2, lane * (S_ * 2), (448 * m + 64 * (idx - 28)) * (S_ * 2) + (xcd ^ SX) * 512 + 384, lb + L_DUMMY);
#pragma unroll
          for (int e = (7 - WST) * WPI; e < WN; ++e) warm(e);
          DRAIN();
          SFOR(NA, p, wait4<0>(f2[p]););
        }
    }
    if (idx < 28) gemm2(std::false_type{}, IC(NA + 16 * nt(6)), f2, IC(0));
    FINISH();
  } else {
        auto att = [&](auto jc) AI {
          constexpr int J = decltype(jc)::value, NH = J ? 2 : 4, H0 = J ? 0 : 2;
          const int H = m & 1;
          LANEVARS;
          f32x4 qa[6][6], qh[NH];
          int wa[2], xa[2][2];
#pragma unroll
          for (int i = 0; i < 2; ++i) {
            const int by = sel((i == 1) & (p4 == 3), 112, 56 * H + 32 * i + 8 * p4);
            wa[i] = A_W + (8 * g + q4) * 128 + (((by >> 4) ^ (2 * ((q4 >> 1) | ((g & 1) << 1)))) << 4) + (by & 8);
          }
          int wb[2] = {wa[0] + A_WS + 12288, wa[1] + A_WS + 12288};
#pragma unroll
          for (int i = 0; i < 4; ++i) xa[i >> 1][i & 1] = (i >> 1) * A_X1 + c * 128 + (((4 * (i & 1) + g) ^ ((c >> 1) & 7)) << 4);
          asm volatile("" : "+v"(wa[0]), "+v"(wa[1]), "+v"(wb[0]), "+v"(wb[1]), "+v"(xa[0][0]), "+v"(xa[0][1]), "+v"(xa[1][0]), "+v"(xa[1][1]));
          u32x4 wf[6], xf[7];
          u32x2 lo;
          auto rdx = [&](int k, int t) AI {
            const int sg = ((k >> 1) + 2) % 3;
            xf[t] = *(const LAS u32x4*)(L + xa[sg ? 1 : 0][k & 1] + (sg == 2 ? A_XS : 0) + (t == 6 ? 0 : (1 + 6 * J + t) * 2048));
          };
          auto rdw = [&](int k, int i, int hf) AI {
            const int o = (((k >> 1) + 2) % 3) * 2 + (k & 1);
            const LAS char* p = L + (o < 3 ? wa[i & 1] + o * 12288 : wb[i & 1] + (o - 3) * 12288) + (i >> 1) * 4096;
            if (hf) { const u32x2 hi = tr_read(p + 512); wf[i] = u32x4{lo.x, lo.y, hi.x, hi.y}; }
            else lo = tr_read(p);
          };
          const bool c0 = c == 0;
          const int exa = A_X1 + H * 4096 + lane * 16;
          if (!ND) DRAIN();
          BAR();
          PRIO(2);
#pragma unroll
          for (int t = 0; t < 7; ++t) rdx(0, t);
#pragma unroll
          for (int i = 0; i < 10; ++i) rdw(0, i >> 1, i & 1);
          gettag();
          SB();
          SFOR(14, k,
            SFOR(6, ci,
              constexpr int i = (k == 13 && J == 0) ? (ci + 2) % 6 : ci;
              SFOR(7, t,
                if constexpr (t < 6) {
                  if constexpr (i < 4) mfma_ip<k == 0>(qa[t][i], wf[i], xf[t]); else mfma_ip<k == 0>(qa[t][i], xf[t], wf[i]);
                } else if constexpr (i >= H0 && i < H0 + NH) {
                  if constexpr (i < 4) mfma_ip<k == 0>(qh[i - H0], wf[i], xf[6]); else mfma_ip<k == 0>(qh[i - H0], xf[6], wf[i]);
                  if constexpr (k < 13 && i == H0 + NH - 1 && J == 0) rdx(k + 1, 6);
                }
                if constexpr (ci == 0 && t < 2) rdw(k, 5, t);
                if constexpr (ci == (k == 13 ? 0 : H0) && t == 2) { SFOR(4, j, xf[6][j] = sel(c0, 0x3f803f80u, xf[6][j]);); asm volatile("" : "+v"(xf[6])); }
                if constexpr ((k & 1) && k < 13 && ci == 1 && t == 1) { WAITLG(); BAR(); }
                if constexpr (k < 13 && ci >= 1 && (t == 2 || t == 3)) rdw(k + 1, ci - 1, t - 2);
                if constexpr (k < 13 && ci == 5 && t < 6) rdx(k + 1, t);
                if constexpr (k < 13 && J == 1 && ci == 2 && t == 0) rdx(k + 1, 6);
                if constexpr (k == 13 && t == 2 && ci >= 2 && ci < NH + 2) {
                  constexpr int ip = H0 + ci - 2;
                  if constexpr (ip < 4) *(LAS f32x4*)(L + sel(c0, A_CS + (H * 96 + 16 * ip + 4 * g) * 4, L_DUMMY)) = qh[ci - 2];
                  else *(LAS float*)(L + sel(g == 0, A_CS + (H * 96 + 16 * ip + c) * 4, L_DUMMY)) = qh[ci - 2][0];
                }
                if constexpr (k == 13 && J == 0 && t == 4 && ci >= 2) *(LAS f32x4*)(L + exa + (ci - 2) * 1024) = qa[5][ci];
                SB();
              );
            );
          );
          const bool odd = c & 1;
          float mc[7], rc[6];
          f32x2 rk[7];
#pragma unroll
          for (int t = 0; t < 7; ++t) {
            mc[t] = *(const LAS float*)(L + A_MEAN + (96 * J + 16 * t + c) * 4);
            if (t) rc[t - 1] = *(const LAS float*)(L + A_RSTD + (96 * J + 16 * t + c) * 4);
            const LAS float* p = (const LAS float*)(L + A_RSTD + (96 * J + 16 * t + 4 * g + (c & 1)) * 4);
            rk[t] = f32x2{p[0], p[2]};
          }
          f32x2 mk[2][2];
          {
            const int d0 = 16 + (c & 14) - 4 * g, hi = min(r0 + 96 * J + c, 14);
#pragma unroll
            for (int e = 0; e < 4; ++e) {
              const int dk = d0 - 16 * (e >> 1) - 2 * (e & 1);
              mk[0][e >> 1][e & 1] = sel((dk >= 0) & (dk <= hi), 0.f, -3.0e38f);
              mk[1][e >> 1][e & 1] = sel((dk >= 0) & (dk <= 14), 0.f, -3.0e38f);
            }
          }
          gbar4(L, lb, 4);
          f32x4 c4[4], ex[4];
          float cv[2];
#pragma unroll
          for (int i = 0; i < 4; ++i) c4[i] = *(const LAS f32x4*)(L + A_CS + (H * 96 + 16 * i + 4 * g) * 4);
#pragma unroll
          for (int i = 0; i < 2; ++i) cv[i] = *(const LAS float*)(L + A_CS + (H * 96 + 64 + 16 * i + c) * 4);
#pragma unroll
          for (int i = 0; i < 4; ++i) { if constexpr (J) ex[i] = *(const LAS f32x4*)(L + exa + i * 1024); else ex[i] = qh[i & (NH - 1)]; }
          sig(lb, 5);
          auto pk4 = [&](f32x4 v) AI { return u32x2{pack2(v[0], v[1]), pack2(v[2], v[3])}; };
          auto frag = [&](f32x4 a, f32x4 b, float mu, int i) AI {
            const u32x2 l = pk4(c4[i] * mu + a), h = pk4(c4[i + 1] * mu + b);
            return u32x4{l.x, l.y, h.x, h.y};
          };
          f32x4 s0[6], s1[6];
          u32x4 kp = frag(ex[0], ex[1], mc[0], 2);
#pragma unroll
          for (int u = 0; u < 6; ++u) {
            const u32x4 qf = frag(qa[u][0], qa[u][1], mc[u + 1], 0);
            s0[u] = mfma16(kp, qf, Z4);
            kp = frag(qa[u][2], qa[u][3], mc[u + 1], 2);
            s1[u] = mfma16(kp, qf, Z4);
          }
          SB();
          f32x4 m4[7];
#pragma unroll
          for (int t = 0; t < 7; ++t) m4[t] = *(const LAS f32x4*)(L + A_MEAN + (96 * J + 16 * t + 4 * g) * 4);
          f32x2 lg[6][2];
          float mx[6], inv[6];
#pragma unroll
          for (int u = 0; u < 6; ++u) {
            const float sc = rc[u] * (0.18898223650461362f * 1.4426950408889634f);
#pragma unroll
            for (int e = 0; e < 2; ++e) {
              const f32x4 s = e ? s1[u] : s0[u];
              lg[u][e] = __builtin_elementwise_fma(f32x2{sel(odd, s[1], s[0]), sel(odd, s[3], s[2])}, rk[u + e] * sc, mk[u ? 1 : 0][e]);
            }
          }
#pragma unroll
          for (int u = 0; u < 6; ++u) mx[u] = gmax(max2(max2(lg[u][0][0], lg[u][0][1]), max2(lg[u][1][0], lg[u][1][1])));
          SB();
          u32x2 vv[7][2];
#pragma unroll
          for (int t = 0; t < 7; ++t)
#pragma unroll
            for (int i = 0; i < 2; ++i) vv[t][i] = pk4(m4[t] * cv[i] + (t ? qa[t - 1][4 + i] : ex[2 + i]));
          SB();
#pragma unroll
          for (int u = 0; u < 6; ++u)
#pragma unroll
            for (int e = 0; e < 2; ++e) {
              lg[u][e] -= mx[u];
              lg[u][e] = f32x2{__builtin_amdgcn_exp2f(lg[u][e][0]), __builtin_amdgcn_exp2f(lg[u][e][1])};
            }
#pragma unroll
          for (int u = 0; u < 6; ++u) { const f32x2 t = lg[u][0] + lg[u][1]; inv[u] = __builtin_amdgcn_rcpf(gsum(t[0] + t[1])); }
          const f32x2 par = {sel(odd, 0.f, 1.f), sel(odd, 1.f, 0.f)};
          f32x4 o0[6], o1[6];
#pragma unroll
          for (int u = 0; u < 6; ++u) {
            const f32x2 iv = par * inv[u];
            u32x4 pp;
#pragma unroll
            for (int e = 0; e < 4; ++e) { const f32x2 p = lg[u][e >> 1] * rk[u + (e >> 1)], t = iv * p[e & 1]; pp[e] = pack2(t[0], t[1]); }
            o0[u] = mfma16(u32x4{vv[u][0].x, vv[u][0].y, vv[u + 1][0].x, vv[u + 1][0].y}, pp, Z4);
            o1[u] = mfma16(u32x4{vv[u][1].x, vv[u][1].y, vv[u + 1][1].x, vv[u + 1][1].y}, pp, Z4);
          }
          PRIO(3);
#pragma unroll
          for (int u = 0; u < 6; ++u) {
            const u32x2 l = pk4(o0[u]), h = pk4(o1[u]);
            __builtin_amdgcn_raw_buffer_store_b128(u32x4{l.x, l.y, h.x, h.y}, rs_ws, WS_O + ((12 * rg + 6 * J + u) * 16 + 2 * hp + H) * 1024 + lane * 16, 0, AUX_SC1);
          }
          SB();
          };
        if (m < 2) att(std::integral_constant<int, 0>{}); else att(std::integral_constant<int, 1>{});
    LANEVARS;
    if (DF && idx < 28) { u32x4 f2[6][4]; gemm2(std::true_type{}, IC(0), f2, IC(1)); }
    DRAIN();
    if (arrive(lb, 6) == 3u) putflag(WS_OF + (rg * 8 + hp) * FS, lane);
    if (!DF && idx < 28) { u32x4 f2[6][4]; gemm2(std::true_type{}, IC(0), f2, IC(1)); }
    if (bid == EPB) {
      __builtin_amdgcn_s_sleep(64);
      poll_flags<16>((const GAS u64*)(ws + WS_UF + (m * 64 + lane) * FS), tag);
      if (arrive(lb, 8) == 3u) putflag(WS_E, lane);
    }
  }
  FINISH();
}

extern "C" void kernel_launch(void* const* d_in, const int* in_sizes, int n_in,
                              void* d_out, int out_size, void* d_ws, size_t ws_size,
                              hipStream_t stream) {
  (void)in_sizes; (void)n_in; (void)out_size; (void)ws_size;
  (void)hipFuncSetAttribute((const void*)fused_block, hipFuncAttributeMaxDynamicSharedMemorySize, LDS_BYTES);
  fused_block<<<256, 512, LDS_BYTES, stream>>>((const u16*)d_in[0], (const u16*)d_in[1], (const u16*)d_in[2],
                                               (const u16*)d_in[4], (const u16*)d_in[5], (const u16*)d_in[6],
                                               (const u16*)d_in[7], (u16*)d_out, (char*)d_ws);
}
```

```cpp
#include <hip/hip_runtime.h>
#include <stdint.h>
#include <type_traits>
#include <utility>

#define __float_as_uint(x) __builtin_bit_cast(unsigned, (float)(x))
#define __uint_as_float(x) __builtin_bit_cast(float, (unsigned)(x))
#define __all(p) (__builtin_amdgcn_ballot_w64(!(p)) == 0)
typedef unsigned short u16;
typedef unsigned long long u64;
using bfv8 = __attribute__((ext_vector_type(8))) __bf16;
using bfv2 = __attribute__((ext_vector_type(2))) __bf16;
using f32x4 = __attribute__((ext_vector_type(4))) float;
using f32x2 = __attribute__((ext_vector_type(2))) float;
using u32x2 = __attribute__((ext_vector_type(2))) unsigned int;
using u32x4 = __attribute__((ext_vector_type(4))) unsigned int;
using u32x16 = __attribute__((ext_vector_type(16))) unsigned int;
using s16x4 = __attribute__((ext_vector_type(4))) short;

#define LAS __attribute__((address_space(3)))
#define GAS __attribute__((address_space(1)))
#define AI __attribute__((always_inline))
#define SB() __builtin_amdgcn_sched_barrier(0)
#define SGB(mask, n) __builtin_amdgcn_sched_group_barrier(mask, n, 0)
#define DRAIN() asm volatile("s_waitcnt vmcnt(0)" ::: "memory")
#define WAITVM(n) asm volatile("s_waitcnt vmcnt(%0)" ::"n"(n) : "memory")
#define WAITLG() asm volatile("s_waitcnt lgkmcnt(0)" ::: "memory")
#define RLX_AGENT __ATOMIC_RELAXED, __HIP_MEMORY_SCOPE_AGENT
#define PRIO(n) __builtin_amdgcn_s_setprio(n)

constexpr int RQ1 = 2, RQ2 = 5, PLP = 0, PEP = 1, PE2 = 2, PL2 = 1, PP2 = 0, SX = 1, WST = 5, WPI = 2, WN = 7, WORD = 0, WRM = 1, NB2L = 3, IL = 1, ADLY = 0, LA0 = 6, LA1 = 6, W0 = 0, PE1 = 1, PL1 = 2, NB1 = 8, PKE = 1, NSG = 2, NE = 4, WSL = 1, TP = 20, SG2 = 0, SLP = 4, OV = 0, GDLY = 5, GDL2 = 4, W5 = 8, W6 = 12, EW = 7, EG = 2, TW = 9, LA6 = 6, AV = 14, HW = 0, HO = 0, RO = 0, DF = 0, TR = 0, TR5 = 12816, TR4 = 12816, RP = 2, MSP = 1, CH = 1, SKA = 0, CE = 2, CD = 1, PH = 0, OH = 0, GK = -1, PF1 = 0, OPK = 3, PT = 0, PZ = 0, XR = 0, PG = 0, PGE = 3, PGF = 3, CT = 3, FLP = 1, LDF = 1, FA = 0, PA = 0, HB = 1, TD = 0, TMP = 0, WT = 9, WP = 99, AO = 0, AS = 5, GS = 0, OFS = 14320928;
constexpr int S_ = 2048, D_ = 448, F_ = 1792, N3_ = 1344;
constexpr int AUX_SC1 = 16;
constexpr int RSRC_W3 = 0x00020000;

constexpr int FS = 128;
constexpr int WS_E = 0, WS_HF = 4096, WS_UF = WS_HF + 128 * FS, WS_OF = WS_UF + 168 * FS;
constexpr int WS_ST = 65536;
constexpr int KS_ = 28 * 1024;
constexpr int WS_H = 131072;
constexpr int WS_U = WS_H + 64 * KS_;
constexpr int WS_O = WS_U + 56 * KS_;
constexpr int WS_END = WS_O + 132 * 16384;

constexpr int L_LATE = 65536, SS = 16384;
constexpr int L_HB = 131072;
constexpr int L_CNT = 162176;
constexpr int L_SA = 162432;
constexpr int L_DUMMY = 162560;
constexpr int LDS_BYTES = 163840;
constexpr int HB_PITCH = 928, AT_PITCH = 144;

__device__ __forceinline__ unsigned pack2(float a, float b) {
  bfv2 v; v[0] = (__bf16)a; v[1] = (__bf16)b;
  return __builtin_bit_cast(unsigned, v);
}
__device__ __forceinline__ float bfr(float f) { return __uint_as_float(pack2(f, 0.f) << 16); }
__device__ __forceinline__ float lo_f(unsigned u) { return __uint_as_float(u << 16); }
__device__ __forceinline__ float hi_f(unsigned u) { return __uint_as_float(u & 0xffff0000u); }
__device__ __forceinline__ float dot2(unsigned a, unsigned b, float c) {
  return __builtin_amdgcn_fdot2_f32_bf16(__builtin_bit_cast(bfv2, a), __builtin_bit_cast(bfv2, b), c, false);
}
__device__ __forceinline__ f32x4 mfma16(u32x4 a, u32x4 b, f32x4 c) {
  return __builtin_amdgcn_mfma_f32_16x16x32_bf16(__builtin_bit_cast(bfv8, a), __builtin_bit_cast(bfv8, b), c, 0, 0, 0);
}
__device__ __forceinline__ float gsum(float v) {
  unsigned u = __float_as_uint(v);
  auto a = __builtin_amdgcn_permlane32_swap(u, u, false, false);
  float s = __uint_as_float(a[0]) + __uint_as_float(a[1]);
  u = __float_as_uint(s);
  auto b = __builtin_amdgcn_permlane16_swap(u, u, false, false);
  return __uint_as_float(b[0]) + __uint_as_float(b[1]);
}
__device__ __forceinline__ float max2(float a, float b) { return __builtin_elementwise_maximum(a, b); }
template <bool FIRST> __device__ __forceinline__ void mfma_ip(f32x4& d, u32x4 a, u32x4 b) {
  if constexpr (FIRST) asm volatile("v_mfma_f32_16x16x32_bf16 %0, %1, %2, 0" : "=&v"(d) : "v"(a), "v"(b));
  else asm volatile("v_mfma_f32_16x16x32_bf16 %0, %1, %2, %0" : "+v"(d) : "v"(a), "v"(b));
}
__device__ __forceinline__ float gmax(float v) {
  unsigned u = __float_as_uint(v);
  auto a = __builtin_amdgcn_permlane32_swap(u, u, false, false);
  float s = max2(__uint_as_float(a[0]), __uint_as_float(a[1]));
  u = __float_as_uint(s);
  auto b = __builtin_amdgcn_permlane16_swap(u, u, false, false);
  return max2(__uint_as_float(b[0]), __uint_as_float(b[1]));
}
template <class OP, class FN, class FL> __device__ __forceinline__ void across6(float (&v)[6], OP op, FN fin, FL fill) {
  auto s32 = [](float a, float b) AI { const auto r = __builtin_amdgcn_permlane32_swap(__float_as_uint(a), __float_as_uint(b), false, false); return f32x2{__uint_as_float(r[0]), __uint_as_float(r[1])}; };
  auto s16 = [](float a, float b) AI { const auto r = __builtin_amdgcn_permlane16_swap(__float_as_uint(a), __float_as_uint(b), false, false); return f32x2{__uint_as_float(r[0]), __uint_as_float(r[1])}; };
  __builtin_amdgcn_sched_barrier(0);
  fill(0);
  __builtin_amdgcn_sched_barrier(0);
  const f32x2 a = s32(v[0], v[2]), b = s32(v[1], v[3]), d = s32(v[4], v[5]);
  float x = op(a[0], a[1]), y = op(b[0], b[1]), z = op(d[0], d[1]), z2 = z;
  asm volatile("" : "+v"(z2));
  __builtin_amdgcn_sched_barrier(0);
  fill(1);
  __builtin_amdgcn_sched_barrier(0);
  const f32x2 e = s16(x, y), f = s16(z, z2);
  float p = fin(op(e[0], e[1])), q = fin(op(f[0], f[1])), p2 = p, q2 = q;
  asm volatile("" : "+v"(p2), "+v"(q2));
  __builtin_amdgcn_sched_barrier(0);
  fill(2);
  __builtin_amdgcn_sched_barrier(0);
  const f32x2 h = s16(p, p2), k = s32(q, q2);
  float h0 = h[0], h1 = h[1];
  asm volatile("" : "+v"(h0), "+v"(h1));
  __builtin_amdgcn_sched_barrier(0);
  fill(3);
  __builtin_amdgcn_sched_barrier(0);
  const f32x2 r = s32(h[0], h0), t = s32(h[1], h1);
  v[0] = r[0]; v[2] = r[1]; v[1] = t[0]; v[3] = t[1]; v[4] = k[0]; v[5] = k[1];
}
template <int CTRL>
__device__ __forceinline__ float dpp_add(float v) {
  return v + __uint_as_float(__builtin_amdgcn_update_dpp(0u, __float_as_uint(v), CTRL, 0xf, 0xf, true));
}
__device__ __forceinline__ void dma16(__amdgpu_buffer_rsrc_t rs, int voff, int soff, int dst) {
  asm volatile("s_mov_b32 m0, %3\n\ts_nop 0\n\tbuffer_load_dwordx4 %0, %1, %2 offen lds" ::"v"(voff), "s"(rs), "s"(soff), "s"(dst) : "memory");
}
__device__ __forceinline__ void touch(__amdgpu_buffer_rsrc_t rs, int voff, int soff, int dst) {
  asm volatile("s_mov_b32 m0, %3\n\ts_nop 0\n\tbuffer_load_dword %0, %1, %2 offen lds" ::"v"(voff), "s"(rs), "s"(soff), "s"(dst) : "memory");
}
__device__ __forceinline__ void ldx4(u32x4 (&a)[4], __amdgpu_buffer_rsrc_t rs, int voff, int soff) {
  asm volatile("buffer_load_dwordx4 %0, %4, %5, %6 offen\n\tbuffer_load_dwordx4 %1, %4, %5, %6 offen offset:224\n\t"
               "buffer_load_dwordx4 %2, %4, %5, %6 offen offset:448\n\tbuffer_load_dwordx4 %3, %4, %5, %6 offen offset:672"
               : "=&v"(a[0]), "=&v"(a[1]), "=&v"(a[2]), "=&v"(a[3]) : "v"(voff), "s"(rs), "s"(soff) : "memory");
}
__device__ __forceinline__ void dma4(__amdgpu_buffer_rsrc_t rs, int v, int so, int step, int dst) {
  const int s1 = so + step - 1024, s2 = so + 2 * step - 2048, s3 = so + 3 * step - 3072;
  asm volatile("s_mov_b32 m0, %6\n\ts_nop 0\n\t"
               "buffer_load_dwordx4 %0, %1, %2 offen lds\n\t"
               "buffer_load_dwordx4 %0, %1, %3 offen offset:1024 lds\n\t"
               "buffer_load_dwordx4 %0, %1, %4 offen offset:2048 lds\n\t"
               "buffer_load_dwordx4 %0, %1, %5 offen offset:3072 lds"
               ::"v"(v), "s"(rs), "s"(so), "s"(s1), "s"(s2), "s"(s3), "s"(dst) : "memory");
}
__device__ __forceinline__ void ld4(u32x4 (&a)[4], __amdgpu_buffer_rsrc_t rs, int v, int so, int step) {
  asm volatile("buffer_load_dwordx4 %0, %4, %5, %6 offen\n\tbuffer_load_dwordx4 %1, %4, %5, %7 offen\n\t"
               "buffer_load_dwordx4 %2, %4, %5, %8 offen\n\tbuffer_load_dwordx4 %3, %4, %5, %9 offen"
               : "=&v"(a[0]), "=&v"(a[1]), "=&v"(a[2]), "=&v"(a[3])
               : "v"(v), "s"(rs), "s"(so), "s"(so + step), "s"(so + 2 * step), "s"(so + 3 * step) : "memory");
}
template <int N> __device__ __forceinline__ void wait4(u32x4 (&a)[4]) {
  asm volatile("s_waitcnt vmcnt(%4)" : "+v"(a[0]), "+v"(a[1]), "+v"(a[2]), "+v"(a[3]) : "n"(N) : "memory");
}
__device__ __forceinline__ void turn_pair(u32x4 (&a)[4], u32x4 (&b)[4]) {
  unsigned xa[8][2], xb[8][2];
  if constexpr (TD) {
#define CM4(M, D, S, T) asm volatile("s_mov_b64 vcc, %24\n\ts_nop 1\n\t" \
      "v_cndmask_b32_dpp %0, %8, %16, vcc row_ror:8 row_mask:0xf bank_mask:0xf\n\tv_cndmask_b32_dpp %1, %9, %17, vcc row_ror:8 row_mask:0xf bank_mask:0xf\n\t" \
      "v_cndmask_b32_dpp %2, %10, %18, vcc row_ror:8 row_mask:0xf bank_mask:0xf\n\tv_cndmask_b32_dpp %3, %11, %19, vcc row_ror:8 row_mask:0xf bank_mask:0xf\n\t" \
      "v_cndmask_b32_dpp %4, %12, %20, vcc row_ror:8 row_mask:0xf bank_mask:0xf\n\tv_cndmask_b32_dpp %5, %13, %21, vcc row_ror:8 row_mask:0xf bank_mask:0xf\n\t" \
      "v_cndmask_b32_dpp %6, %14, %22, vcc row_ror:8 row_mask:0xf bank_mask:0xf\n\tv_cndmask_b32_dpp %7, %15, %23, vcc row_ror:8 row_mask:0xf bank_mask:0xf" \
      : "=&v"(D[0][0]), "=&v"(D[0][1]), "=&v"(D[1][0]), "=&v"(D[1][1]), "=&v"(D[2][0]), "=&v"(D[2][1]), "=&v"(D[3][0]), "=&v"(D[3][1]) \
      : "v"(V[0][S]), "v"(V[0][S + 1]), "v"(V[1][S]), "v"(V[1][S + 1]), "v"(V[2][S]), "v"(V[2][S + 1]), "v"(V[3][S]), "v"(V[3][S + 1]), \
        "v"(V[0][T]), "v"(V[0][T + 1]), "v"(V[1][T]), "v"(V[1][T + 1]), "v"(V[2][T]), "v"(V[2][T + 1]), "v"(V[3][T]), "v"(V[3][T + 1]), "s"(M) : "vcc")
    const u64 lo = 0x00ff00ff00ff00ffull;
    { u32x4 (&V)[4] = a; CM4(lo, xa, 2, 0); }
    { u32x4 (&V)[4] = b; CM4(lo, (xa + 4), 2, 0); }
#pragma unroll
    for (int r = 0; r < 8; ++r)
#pragma unroll
      for (int e = 0; e < 2; ++e) {
        const u32x4 v = r < 4 ? a[r] : b[r - 4];
        xb[r][e] = __builtin_amdgcn_update_dpp(v[2 + e], v[e], 0x128, 0xf, 0x3, false);
      }
  } else {
#pragma unroll
  for (int r = 0; r < 8; ++r)
#pragma unroll
    for (int e = 0; e < 2; ++e) {
      const u32x4 v = r < 4 ? a[r] : b[r - 4];
      xa[r][e] = __builtin_amdgcn_update_dpp(v[e], v[2 + e], 0x128, 0xf, 0xc, false);
      xb[r][e] = __builtin_amdgcn_update_dpp(v[2 + e], v[e], 0x128, 0xf, 0x3, false);
    }
  }
#pragma unroll
  for (int j = 0; j < 4; ++j)
#pragma unroll
    for (int q = 0; q < 4; ++q) {
      a[j][q] = __builtin_amdgcn_perm(xa[2 * q + 1][j >> 1], xa[2 * q][j >> 1], (j & 1) ? 0x07060302u : 0x05040100u);
      b[j][q] = __builtin_amdgcn_perm(xb[2 * q + 1][j >> 1], xb[2 * q][j >> 1], (j & 1) ? 0x07060302u : 0x05040100u);
    }
#pragma unroll
  for (int j = 0; j < 4; ++j) asm volatile("" : "+v"(a[j]), "+v"(b[j]));
}
__device__ __forceinline__ u32x2 tr_read(const LAS char* p) {
  return __builtin_bit_cast(u32x2, __builtin_amdgcn_ds_read_tr16_b64_v4i16((LAS s16x4*)p));
}
__device__ __forceinline__ float ln_sd(float var) { return bfr(__builtin_amdgcn_sqrtf(bfr(bfr(var) + 1e-5f))); }

__device__ __forceinline__ float row_sum(float v) {
  v = dpp_add<0xB1>(v);
  v = dpp_add<0x4E>(v);
  v = dpp_add<0x141>(v);
  return dpp_add<0x140>(v);
}
__device__ __forceinline__ unsigned ln2_rows(u32x4 (&f)[4], bool act) {
  f32x2 d[16];
#pragma unroll
  for (int e = 0; e < 16; ++e) d[e] = f32x2{lo_f(f[e >> 2][e & 3]), hi_f(f[e >> 2][e & 3])};
  f32x2 t[4];
#pragma unroll
  for (int e = 0; e < 4; ++e) t[e] = (d[e] + d[4 + e]) + (d[8 + e] + d[12 + e]);
  t[0] = (t[0] + t[1]) + (t[2] + t[3]);
  const float mean = row_sum(t[0][0] + t[0][1]) * (1.0f / D_);
  const float mr = bfr(mean);
  const float mb = act ? mr : 0.f;
#pragma unroll
  for (int e = 0; e < 16; ++e) d[e] -= f32x2{mb, mb};
#pragma unroll
  for (int e = 0; e < 4; ++e) t[e] = d[e] * d[e];
#pragma unroll
  for (int e = 4; e < 16; ++e) t[e & 3] = __builtin_elementwise_fma(d[e], d[e], t[e & 3]);
  t[0] = (t[0] + t[1]) + (t[2] + t[3]);
  const float dm = mean - mr;
  const float var = fmaxf(row_sum(t[0][0] + t[0][1]) * (1.0f / D_) - dm * dm, 0.f);
  const float s1 = ln_sd(var), r1 = __builtin_amdgcn_rcpf(s1);
  const float r2 = __builtin_amdgcn_rcpf(ln_sd(var * r1 * r1));
  const float a = r1 * r2, cc = -bfr(dm * r1) * r2;
#pragma unroll
  for (int e = 0; e < 16; ++e) {
    const f32x2 v = __builtin_elementwise_fma(d[e], f32x2{a, a}, f32x2{cc, cc});
    f[e >> 2][e & 3] = pack2(v[0], v[1]);
  }
  return (__float_as_uint(mr) >> 16) | __float_as_uint(s1);
}
__device__ __forceinline__ float solo(float v) { asm("" : "+v"(v)); return v; }
__device__ __forceinline__ float vadd(float a, float b) { return solo(a + b); }
__device__ __forceinline__ float vmul(float a, float b) { return solo(a * b); }
__device__ __forceinline__ f32x4 add4(f32x4 a, f32x4 b) { return f32x4{vadd(a[0], b[0]), vadd(a[1], b[1]), vadd(a[2], b[2]), vadd(a[3], b[3])}; }

constexpr unsigned SPIN_CAP = 1u << 18;
template <int SL>
__device__ __forceinline__ void poll_flags(const GAS u64* p, u64 tag) {
#pragma unroll 1
  for (unsigned spins = 0; spins < SPIN_CAP; ++spins) {
    const u64 v = __hip_atomic_load(p, RLX_AGENT);
    if (__all(v == tag)) break;
    __builtin_amdgcn_s_sleep(SL);
  }
  __builtin_amdgcn_fence(__ATOMIC_ACQUIRE, "wavefront");
}

__device__ __forceinline__ unsigned arrive(int lb, int i) {
  unsigned r;
  asm volatile("s_waitcnt lgkmcnt(0)\n\ts_mov_b64 exec, 1\n\tds_add_rtn_u32 %0, %1, %2\n\ts_mov_b64 exec, -1\n\ts_waitcnt lgkmcnt(0)"
               : "=&v"(r) : "v"(lb + L_CNT + 4 * i), "v"(1u) : "memory");
  return __builtin_amdgcn_readfirstlane(r);
}
__device__ __forceinline__ void sig(int lb, int i) {
  asm volatile("s_waitcnt lgkmcnt(0)\n\ts_mov_b64 exec, 1\n\tds_add_u32 %0, %1\n\ts_mov_b64 exec, -1" ::"v"(lb + L_CNT + 4 * i), "v"(1u) : "memory");
}
template <int I> __device__ __forceinline__ void post(int sa, unsigned one) {
  asm volatile("ds_add_u32 %0, %1 offset:%2" ::"v"(sa), "v"(one), "n"(4 * I) : "memory");
}
__device__ __forceinline__ unsigned look(LAS char* L, int i) {
  return __hip_atomic_load((LAS unsigned*)(L + L_CNT) + i, __ATOMIC_RELAXED, __HIP_MEMORY_SCOPE_WORKGROUP);
}
__device__ __forceinline__ void check(LAS char* L, unsigned v, int i, unsigned target) {
  unsigned t, n, a;
  asm volatile("s_nop 0\n\tv_readfirstlane_b32 %0, %3\n\ts_cmp_lt_u32 %0, %5\n\ts_cbranch_scc1 .Lcw%=\n.Lcb%=:\n\t.subsection 1\n"
               ".Lcw%=:\n\ts_mov_b32 %1, 0x400000\n\tv_mov_b32 %2, %4\n"
               ".Lcl%=:\n\tds_read_b32 %3, %2\n\ts_waitcnt lgkmcnt(0)\n\tv_readfirstlane_b32 %0, %3\n\ts_cmp_ge_u32 %0, %5\n\t"
               "s_cbranch_scc1 .Lcb%=\n\ts_sleep 0\n\ts_sub_u32 %1, %1, 1\n\ts_cbranch_scc0 .Lcl%=\n\ts_branch .Lcb%=\n\t.subsection 0"
               : "=&s"(t), "=&s"(n), "=&v"(a), "+v"(v) : "s"((int)(unsigned)(size_t)L + L_CNT + 4 * i), "n"(target) : "memory", "scc");
}
__device__ __forceinline__ void await(LAS char* L, int i, unsigned target) {
  unsigned t, n, v;
  asm volatile("s_mov_b32 %1, 0x400000\n.Lal%=:\n\tds_read_b32 %2, %3\n\ts_waitcnt lgkmcnt(0)\n\tv_readfirstlane_b32 %0, %2\n\ts_cmp_ge_u32 %0, %4\n\t"
               "s_cbranch_scc1 .Lab%=\n\ts_sleep 0\n\ts_sub_u32 %1, %1, 1\n\ts_cbranch_scc0 .Lal%=\n.Lab%=:"
               : "=&s"(t), "=&s"(n), "=&v"(v) : "v"((int)(unsigned)(size_t)L + L_CNT + 4 * i), "n"(target) : "memory", "scc");
}
__device__ __forceinline__ void await_at(int a, unsigned target) {
  unsigned t, n, v;
  asm volatile("s_mov_b32 %1, 0x400000\n.Lal%=:\n\tds_read_b32 %2, %3\n\ts_waitcnt lgkmcnt(0)\n\tv_readfirstlane_b32 %0, %2\n\ts_cmp_ge_u32 %0, %4\n\t"
               "s_cbranch_scc1 .Lab%=\n\ts_sleep 0\n\ts_sub_u32 %1, %1, 1\n\ts_cbranch_scc0 .Lal%=\n.Lab%=:"
               : "=&s"(t), "=&s"(n), "=&v"(v) : "v"(a), "n"(target) : "memory", "scc");
}
__device__ __forceinline__ void gbar4(LAS char* L, int lb, int i) { sig(lb, i); await(L, i, 4u); }

#define BAR() __builtin_amdgcn_s_barrier()
#define LANEVARS                                                                                         \
  int lane;                                                                                              \
  asm volatile("v_mbcnt_lo_u32_b32 %0, -1, 0\n\tv_mbcnt_hi_u32_b32 %0, -1, %0" : "=v"(lane));             \
  const int c = lane & 15, g = lane >> 4, q4 = c >> 2, p4 = c & 3, rr = lane >> 3;                       \
  (void)c; (void)g; (void)q4; (void)p4; (void)rr
constexpr f32x4 Z4 = {0.f, 0.f, 0.f, 0.f};
constexpr int OOB = 0x40000000;
template <int A, int B> __device__ __forceinline__ void pin(f32x4 (&a)[A][B]) {
#pragma unroll
  for (int i = 0; i < A; ++i)
#pragma unroll
    for (int t = 0; t < B; ++t) asm volatile("" : "+v"(a[i][t]));
}
template <class T> __device__ __forceinline__ T sel(bool c, T a, T b) { return __builtin_unpredictable(c) ? a : b; }
__device__ __forceinline__ int trbase(int i, int g, int q4, int p4) {
  return (4 * g + q4) * 128 + (((2 * i + (p4 >> 1)) ^ (2 * ((q4 >> 1) | ((g & 1) << 1)))) << 4) + 8 * (p4 & 1);
}
__device__ __forceinline__ void slot_frags(const LAS char* L, const int (&tb)[4], int so, u32x4 (&a)[4]) {
#pragma unroll
  for (int i = 0; i < 4; ++i) {
    const u32x2 lo = tr_read(L + tb[i] + so), hi = tr_read(L + tb[i] + so + 2048);
    a[i] = u32x4{lo.x, lo.y, hi.x, hi.y};
  }
}
__device__ __forceinline__ u64 peek(__amdgpu_buffer_rsrc_t rs, int off) {
  u64 v;
  asm volatile("buffer_load_dwordx2 %0, %1, %2, 0 offen sc1" : "=v"(v) : "v"(off), "s"(rs) : "memory");
  return v;
}
__device__ __forceinline__ void settle(__amdgpu_buffer_rsrc_t rs, int off, u64 first, u64 tag) {
  unsigned n;
  asm volatile("v_cmp_ne_u64 vcc, %4, %1\n\ts_cbranch_vccz .Lsb%=\n\ts_mov_b32 %0, 0x40000\n"
               ".Lsl%=:\n\tbuffer_load_dwordx2 %1, %2, %3, 0 offen sc1\n\ts_waitcnt vmcnt(0)\n\tv_cmp_ne_u64 vcc, %4, %1\n\t"
               "s_cbranch_vccz .Lsb%=\n\ts_sleep %5\n\ts_sub_u32 %0, %0, 1\n\ts_cbranch_scc0 .Lsl%=\n.Lsb%=:"
               : "=&s"(n), "+v"(first) : "v"(off), "s"(rs), "s"(tag), "n"(SLP) : "memory", "scc", "vcc");
}
template <class F, int... I> __device__ __forceinline__ void static_for(F f, std::integer_sequence<int, I...>) { (f(std::integral_constant<int, I>{}), ...); }
#define SFOR(n, var, ...) static_for([&](auto var##_c) AI { constexpr int var = decltype(var##_c)::value; __VA_ARGS__ }, std::make_integer_sequence<int, n>{})
template <int OFF> __device__ __forceinline__ void ld1_a(u32x4& v, __amdgpu_buffer_rsrc_t rs, int vo, int so) {
  asm volatile("buffer_load_dwordx4 %0, %1, %2, %3 offen offset:%4 sc1" : "=v"(v) : "v"(vo), "s"(rs), "s"(so), "n"(OFF) : "memory");
}
template <int N> __device__ __forceinline__ void wait1(u32x4& a) { asm volatile("s_waitcnt vmcnt(%1)" : "+v"(a) : "n"(N) : "memory"); }
template <int N> __device__ __forceinline__ void wait2(u32x4& a, u32x4& b) { asm volatile("s_waitcnt vmcnt(%2)" : "+v"(a), "+v"(b) : "n"(N) : "memory"); }

template <int NK, int NR, int NT, bool G2, int KX, int NB, int GR, class LD, class PUT, class HK>
__device__ __forceinline__ void kloop(LAS char* L, const int (&tb)[4], int jit0, u32x4 (&fr)[NR][4], u32x4 (&hv)[NB][NT],
                                      f32x4 (&acc)[4][NT], LD ld, PUT put, HK hook) {
  constexpr int NL = NK * NT;
  auto mm = [&](auto kc, auto ic, auto tc) AI {
    constexpr int k = decltype(kc)::value, i = decltype(ic)::value, t = decltype(tc)::value;
    f32x4& a = k >= KX ? acc[t & 3][i] : acc[i][t];
    const f32x4 cz = k == 0 ? Z4 : a;
    a = (G2 && k < KX) ? mfma16(hv[k % NB][t], fr[k % NR][i], cz) : mfma16(fr[k % NR][i], hv[k % NB][t], cz);
    asm volatile("" : "+v"(a));
  };
  constexpr bool COL = CE && KX < NK;
  SFOR(COL ? KX : NK - 2, k,
    u32x2 lo;
    SFOR(NT, t,
      constexpr int n = k * NT + t, i0 = k < GR ? (NB - 1) * NT + n : k == GR ? (NB - 1) * NT + n + t : NB * NT + n, is = i0 < NL ? i0 : NL;
      wait1<is - n - 1>(hv[k % NB][t]);
      if constexpr (t == 0) hook(std::integral_constant<int, k>{});
      SFOR(4, i,
        mm(k_c, i_c, t_c);
        constexpr int e = t * 4 + i;
        if constexpr (k >= 1 && k - 1 + NR < NK && e < 8) {
          const LAS char* q = L + tb[e >> 1] + ((jit0 + k - 1) & 3) * SS;
          if constexpr (e & 1) { const u32x2 hi = tr_read(q + 2048); fr[k - 1][e >> 1] = u32x4{lo.x, lo.y, hi.x, hi.y}; }
          else lo = tr_read(q);
        }
        SB();
      );
      if constexpr (i0 < NL) ld(std::integral_constant<int, i0 / NT>{}, std::integral_constant<int, i0 % NT>{});
      if constexpr (k == GR && i0 + 1 < NL) ld(std::integral_constant<int, (i0 + 1) / NT>{}, std::integral_constant<int, (i0 + 1) % NT>{});
      SB();
    );
  );
  if constexpr (COL) {
    SFOR(NK - KX, t,
      constexpr int k = KX + t, is = (k + NB < NK ? k + NB : NK) - k - 1;
      wait4<4 * is>(hv[k % NB]);
      hook(std::integral_constant<int, k>{});
      SFOR(16, e,
        constexpr int i = e >> 2, h = e & 3, d = 4 * t + i - CD;
        acc[t][i] = mfma16(fr[(KX + h) % NR][i], hv[k % NB][h], acc[t][i]);
        asm volatile("" : "+v"(acc[t][i]));
        if constexpr (h == 3 && d >= 0) put(std::integral_constant<int, d & 3>{}, std::integral_constant<int, d / 4>{});
        SB();
      );
      if constexpr (k + NB < NK) { SFOR(NT, h, ld(std::integral_constant<int, k + NB>{}, h_c);); }
      SB();
    );
    SFOR(CD, e, put(std::integral_constant<int, 4 - CD + e>{}, std::integral_constant<int, NT - 1>{}););
    return;
  }
  SFOR(NT, t,
    wait2<NT - 1 - t>(hv[(NK - 2) % NB][t], hv[(NK - 1) % NB][t]);
    if constexpr (t == 0) hook(std::integral_constant<int, NK - 2>{});
    SFOR(8, e,
      mm(std::integral_constant<int, NK - 2 + (e & 1)>{}, std::integral_constant<int, (e >> 1)>{}, t_c);
      constexpr int d = t * 4 + (e >> 1) - 2;
      if constexpr ((e & 1) && d >= 0) put(std::integral_constant<int, d & 3>{}, std::integral_constant<int, d / 4>{});
      SB();
    );
  );
  put(std::integral_constant<int, 2>{}, std::integral_constant<int, NT - 1>{});
  put(std::integral_constant<int, 3>{}, std::integral_constant<int, NT - 1>{});
}
template <int N> __device__ __forceinline__ void gelu_pack(const f32x4 (&v)[N], unsigned (&pr)[2 * N]) {
  f32x4 z[N];
#pragma unroll
  for (int e = 0; e < N; ++e) z[e] = v[e] * (v[e] * v[e] * -0.1029432395800235f - 2.302208198144325f);
  SB();
#pragma unroll
  for (int e = 0; e < 4 * N; ++e) z[e >> 2][e & 3] = __builtin_amdgcn_exp2f(z[e >> 2][e & 3]);
  SB();
#pragma unroll
  for (int e = 0; e < N; ++e) z[e] += 1.0f;
#pragma unroll
  for (int e = 0; e < 4 * N; ++e) z[e >> 2][e & 3] = __builtin_amdgcn_rcpf(z[e >> 2][e & 3]);
  SB();
#pragma unroll
  for (int e = 0; e < N; ++e) { z[e] *= v[e]; pr[2 * e] = pack2(z[e][0], z[e][1]); pr[2 * e + 1] = pack2(z[e][2], z[e][3]); }
}
__device__ __forceinline__ float fold32(float a, float b) {
  const auto s = __builtin_amdgcn_permlane32_swap(__float_as_uint(a), __float_as_uint(b), false, false);
  return vadd(__uint_as_float(s[0]), __uint_as_float(s[1]));
}
#define LA_(a) ((LAS char*)(size_t)(unsigned)(a))
#define PINV(v) asm volatile("" : "+v"(v))
#define IC(n) std::integral_constant<int, (n)>{}
constexpr int g1buf(int nt, int t) { return ((t + 8 - nt) & 3) * SS; }
constexpr int L_B1 = 162816;
constexpr int A_W = 26624, A_WS = 24576, A_X1 = 100352, A_XS = 26624;
constexpr int A_MEAN = 153600, A_RSTD = A_MEAN + 1024, A_CS = A_RSTD + 1024;
#define FINISH() __builtin_amdgcn_endpgm()
__global__ __launch_bounds__(512, 2) void fused_block(const u16*, const u16*, const u16*, const u16*, const u16*, const u16*,
                                                      const u16*, u16*, char*) {
  extern __shared__ __attribute__((aligned(16))) char smem[];
  LAS char* L = (LAS char*)smem;
  const int lb = (int)(unsigned)(size_t)L;
  const auto kp = __builtin_amdgcn_kernarg_segment_ptr();
  u32x16 ka; u32x2 kb;
  asm volatile("s_load_dwordx16 %0, %2, 0x0\n\ts_load_dwordx2 %1, %2, 0x40" : "=&s"(ka), "=&s"(kb) : "s"(kp));
  SB();
  const int w = __builtin_amdgcn_readfirstlane((int)threadIdx.x >> 6);
  const int bid = blockIdx.x, xcd = bid & 7, idz = bid >> 3;
  const int m = w & 3;
#define ARGP(i) ((const u16*)(((u64)ka[2 * (i) + 1] << 32) | ka[2 * (i)]))
#define ARGWAIT() asm volatile("s_waitcnt lgkmcnt(0)" : "+s"(ka), "+s"(kb) : "s"(kp) : "memory")
  u64 tag;
  const int un = xcd * 16 + idz, row0 = 32 * (un >> 1) + 8 * (un & 1);
  const bool isln = idz < 16;
  u32x4 hx[4];
  u64 e1;
  if (isln && w >= 4) {
    int lv = sel(((int)threadIdx.x & 15) < 14, (((int)threadIdx.x >> 4) & 3) * (D_ * 2) + ((int)threadIdx.x & 15) * 16, OOB);
    int ls = (row0 + 4 * (m & 1) + 8 * (m & 2)) * (D_ * 2), z = 0;
    asm volatile("" : "+v"(lv), "+v"(z));
    asm volatile("" : "+s"(ls));
    ARGWAIT();
    ldx4(hx, __builtin_amdgcn_make_buffer_rsrc((void*)ARGP(0), 0, S_ * D_ * 2, RSRC_W3), lv, ls);
    e1 = peek(__builtin_amdgcn_make_buffer_rsrc((void*)(((u64)kb[1] << 32) | kb[0]), 0, WS_END, RSRC_W3), z);
    BAR();
  } else {
    u64 pc;
    unsigned co = sel(isln, 0, (((idz - 16) * 8 + w) * 8 + ((int)threadIdx.x & 7)) * 128);
    asm volatile(".Lfb_start:\n\ts_getpc_b64 %0\n\ts_mov_b32 m0, %2\n\tv_min_u32 %1, .Lfb_end-.Lfb_start-8, %1\n\t"
                 "global_load_lds_dword %1, %0\n\t.subsection 2\n.Lfb_end:\n\ts_endpgm\n\t.subsection 0" : "=&s"(pc), "+v"(co) : "s"(lb + L_DUMMY) : "memory");
    *((LAS unsigned*)(L + L_CNT) + ((int)threadIdx.x & 31)) = 0u;
  }
  SB();
  auto gettag = [&]() AI {
    const u32x2 tv = *(const LAS u32x2*)(L + L_CNT + 128);
    tag = ((u64)(unsigned)__builtin_amdgcn_readfirstlane((int)tv.y) << 32) | (unsigned)__builtin_amdgcn_readfirstlane((int)tv.x);
  };

  const int ng = 24 - 6 * (xcd & 1);
  const int k4 = idz + ng - 32, k5 = idz - 16 - 4 * (xcd & 1);
  const bool isg = k4 >= 0;
  const int lp = sel(k5 < 0, ((0x24 - 3 * ((8 >> xcd) & 1) + 0xc0) >> (k4 & 6)) & 3, k5 >> 2);
  int ft = 7 * (xcd >> 1) + 4 * (xcd & 1) + lp, dg = sel(k5 < 0, 4 + (idz & 1), idz & 3);
  int idx = sel(isg, 6 * lp + dg, ng + idz);
  asm volatile("" : "+s"(idx));
  int off1 = (OFS >> (4 * dg)) & 15;
  const int q = 22 * (xcd >> 2) + 8 * (xcd & 1) + idx - ng;
  int hp = 2 * (xcd & 2) + (q & 3), rg = q >> 2;
  int rotq = RQ1 * (q & 3) + RQ2 * rg + 70;
  rotq -= 7 * ((rotq * 147) >> 10);
  constexpr int EPB = 56;
  auto k1 = [&](int j) AI { return (j + off1) & 15; };
  int reg = (w >> 2) * L_LATE + m * 4096;
  int fv, fw, fs, fs1, role = (isg ? 0 : 2) + (w >> 2);
  {
    const int l = (int)threadIdx.x & 63, rr = l >> 3, ch = l & 7;
    const int v1 = ((m * 512 + rr) * F_ + ft * 64) * 2 + (ch ^ (2 * ((rr >> 1) & 3))) * 16;
    const int cq = ch ^ (2 * ((rr >> 1) & 1)) ^ (4 * (m & 1));
    const int vq = sel(cq < 7, (8 * m + rr) * (N3_ * 2) + hp * 112 + cq * 16, OOB);
    const int va = (192 * rg + 16 + 8 * m + rr) * (D_ * 2) + (ch ^ (4 * (m & 1) + (rr >> 1))) * 16;
    fv = sel(isg, v1, vq);
    fw = va;
    fs = sel(isg, k1(0) * 32 * (F_ * 2), rotq * (64 * N3_ * 2));
    fs1 = sel(isg, k1(1) * 32 * (F_ * 2), rotq * 128);
  }
  asm volatile("" : "+v"(fv), "+v"(fw));
  asm volatile("" : "+s"(fs), "+s"(fs1), "+s"(ft), "+s"(dg), "+s"(reg), "+s"(off1), "+s"(rotq), "+s"(role), "+s"(hp), "+s"(rg));
  const int r0 = 192 * rg;
  ARGWAIT();
  if (!HB && !(isln && w >= 4)) BAR();
  const u16 *x = ARGP(0), *wqkv = ARGP(1), *wo = ARGP(2), *w1 = ARGP(3), *b1 = ARGP(4), *w2 = ARGP(5), *b2 = ARGP(6);
  u16* out = (u16*)ARGP(7);
  char* ws = (char*)(((u64)kb[1] << 32) | kb[0]);
  const auto rs_x = __builtin_amdgcn_make_buffer_rsrc((void*)x, 0, S_ * D_ * 2, RSRC_W3);
  const auto rs_ws = __builtin_amdgcn_make_buffer_rsrc((void*)ws, 0, WS_END, RSRC_W3);
  const auto rs_w1 = __builtin_amdgcn_make_buffer_rsrc((void*)w1, 0, S_ * F_ * 2, RSRC_W3);
  const auto rs_w2 = __builtin_amdgcn_make_buffer_rsrc((void*)w2, 0, F_ * S_ * 2, RSRC_W3);
  auto putflag = [&](int off, int lane) AI {
    __builtin_amdgcn_raw_buffer_store_b128(u32x4{(unsigned)tag, (unsigned)(tag >> 32), (unsigned)tag, (unsigned)(tag >> 32)}, rs_ws,
                                           sel(lane < 8, off + lane * 16, OOB), 0, AUX_SC1);
  };
  auto mktag = [&](u64 e0) AI {
    e0 = e0 * 0x9E3779B97F4A7C15ull + 0x632BE59BD9B4E019ull;
    *(LAS u64*)(L + L_CNT + 128) = e0;
    tag = ((u64)(unsigned)__builtin_amdgcn_readfirstlane((int)(e0 >> 32)) << 32) | (unsigned)__builtin_amdgcn_readfirstlane((int)e0);
  };
  const auto rs_wq = __builtin_amdgcn_make_buffer_rsrc((void*)wqkv, 0, D_ * N3_ * 2, RSRC_W3);
  auto piece = [&](bool late, int fv, int sg, int so, int e) AI {
    if (late) {
      if (e < 6 || m < 2) dma16(rs_x, e ? fv : sel(fv >= 32 * D_ * 2, fv - 32 * D_ * 2, OOB), so + (e ? e - 1 : 0) * 32 * (D_ * 2), lb + (sg ? A_X1 + (sg - 1) * A_XS : 0) + (m + 4 * e) * 1024);
    } else {
      if (e < 6) dma16(rs_wq, fv, so + (e / 3) * 32 * (N3_ * 2) + (e % 3) * 896, lb + A_W + sg * A_WS + e * 4096 + m * 1024);
    }
  };
  if (HB) {
    if (role == 0) {
      if (isln) { BAR(); __builtin_amdgcn_s_sleep(GDL2); }
      if (GDLY) __builtin_amdgcn_s_sleep(GDLY);
      dma4(rs_w1, fv, fs, 8 * F_ * 2, lb + reg);
      dma4(rs_w1, fv, fs1, 8 * F_ * 2, lb + reg + SS);
    }
    asm volatile("" : "+s"(role));
    if (!isln || role == 2) BAR();
  }
  if (!HB && role == 0) {
    if (GDLY) __builtin_amdgcn_s_sleep(GDLY);
    if (GDL2 && isln) __builtin_amdgcn_s_sleep(GDL2);
    dma4(rs_w1, fv, fs, 8 * F_ * 2, lb + reg);
    dma4(rs_w1, fv, fs1, 8 * F_ * 2, lb + reg + SS);
  } else if (role == 2) {
    if (ADLY) __builtin_amdgcn_s_sleep(ADLY);
#pragma unroll
    for (int e = 0; e < 13; ++e) {
      if (e == AS) await(L, 7, 4u);
      const int o = AO ? (e + 6) % 13 : e;
      if (o < 6) piece(false, fv, 2, fs, o); else piece(true, fw, 2, fs1, o - 6);
    }
  }
  asm volatile("" : "+s"(role));
  if (!(role & 1)) {
  } else if (isln) {
    const int hbase = sel(isg, L_HB, A_W + A_WS);
    PRIO(3);
    LANEVARS;
    const bool act = c < 14;
    int hb = sel(act, hbase + (4 * m + g) * HB_PITCH + c * 16, L_DUMMY);
    int tp = hbase + (4 * (g & 1) + q4) * HB_PITCH + (16 * (g >> 1) + 4 * p4) * 2 + m * 64;
    int ho = WS_H + (un >> 1) * KS_ + (2 * m + (g >> 1)) * 1024 + (2 * (un & 1) + (g & 1)) * 256 + c * 16;
    int h3 = sel(m < 2, ho + 3 * 8192, sel((lane < 16) & (m == 3), WS_ST + (row0 + (c & 7) + 2 * (c & 8)) * 16, OOB));
    asm volatile("" : "+v"(hb), "+v"(tp), "+v"(ho), "+v"(h3));
    asm volatile("s_waitcnt vmcnt(1)" : "+v"(hx[0]), "+v"(hx[1]), "+v"(hx[2]), "+v"(hx[3])::"memory");
    const unsigned sa = ln2_rows(hx, act);
#pragma unroll
    for (int e = 0; e < 4; ++e) *(LAS u32x4*)(L + hb + e * 224) = hx[e];
    *(LAS unsigned*)(L + L_SA + (4 * m + g) * 4) = sa;
    asm volatile("s_waitcnt vmcnt(0)" : "+v"(e1)::"memory");
    mktag(e1);
    gbar4(L, lb, 0);
    u32x4 hs[4];
#pragma unroll
    for (int u = 0; u < 4; ++u) {
      const u32x2 a0 = tr_read(L + tp + u * 256), a1 = tr_read(L + tp + u * 256 + 8 * HB_PITCH);
      hs[u] = u32x4{a0.x, a0.y, a1.x, a1.y};
    }
    const unsigned sb = *(const LAS unsigned*)(L + L_SA + c * 4);
#pragma unroll
    for (int u = 0; u < 3; ++u) __builtin_amdgcn_raw_buffer_store_b128(hs[u], rs_ws, ho + u * 8192, 0, AUX_SC1);
    if (m == 3) hs[3] = u32x4{sb, (unsigned)tag, sb, (unsigned)(tag >> 32)};
    __builtin_amdgcn_raw_buffer_store_b128(hs[3], rs_ws, h3, 0, AUX_SC1);
    SB();
    sig(lb, 7);
    if (!LDF || isg) {
      DRAIN();
      if (arrive(lb, 1) == 3u) putflag(WS_HF + un * FS, lane);
    }
  } else {
    u64 e0 = peek(rs_ws, WS_E);
    asm volatile("s_waitcnt vmcnt(0)" : "+v"(e0)::"memory");
    mktag(e0);
    sig(lb, 0);
    sig(lb, 7);
  }
  asm volatile("" : "+s"(role));
  int idy = idx;
  asm volatile("" : "+s"(idy));
  constexpr u64 TMS[5][3] = {{0x59d28518a320820ull, 0xbdab49ca2f839acull, 0x18820ffbbcdeb38ull}, {0x7358a4a0c520820ull, 0xbcdeb38ed5a4a30ull, 0xffbbcd671bull}, {0x7358a4a0c520820ull, 0x9e1f65d4e3a4a30ull, 0xffbbcdeaf9ull}, {0x1b58a4a0c520820ull, 0xc4ed57d1679460eull, 0xffbbcdeaf9ull}, {0x7358a4a0c520820ull, 0xcbf0bb1c75a4a30ull, 0xffbbcddf53ull}};
  const u64 tm = idy < 12 ? TMS[TMP][0] : idy < 24 ? TMS[TMP][1] : TMS[TMP][2];
  const int tile = sel(xcd & 1, (int)(tm >> (5 * (idy - 12 * ((idy * 43) >> 9)))) & 31, idy);
  const int st = 4 * (xcd ^ SX) + (tile & 3), dt = tile >> 2;
  const int rotl = dt % 6;
  auto pos2 = [&](bool late, int p) AI { const int v = p + rotl; return 14 * m + (late ? 8 + (v >= 6 ? v - 6 : v) : (p + 2 * dt) & 7); };
  constexpr int SP = HW ? F_ : S_;
  const int stx = HW ? 7 * (xcd >> 1) + 4 * (xcd & 1) + min(tile & 3, 3 - (xcd & 1)) : st;
  auto ldw2 = [&](u32x4 (&a)[4], int vr, int p) AI { ld4(a, HW ? rs_w1 : rs_w2, vr, (pos2(false, p & 6) * 32 + 16 * (p & 1)) * (SP * 2), SP * 2); };
  auto w2lane = [&](int lane) AI { return ((32 * ((lane >> 3) & 1) + 4 * (lane >> 4)) * SP + stx * 64 + 8 * (lane & 7)) * 2; };
  auto gemm2 = [&](auto latec, auto nec, auto& f2, auto idc) AI {
    constexpr bool LATE = decltype(latec)::value;
    constexpr int NR2 = LATE ? 6 : 8, NK2 = LATE ? 10 : 8, N0 = decltype(nec)::value & 15, T0 = decltype(nec)::value >> 4;
    {
      constexpr int NB2 = LATE ? NB2L : 3;
      u32x4 uv[NB2][4];
      f32x4 acc[4][4];
      {
        LANEVARS;
        PRIO(PP2);
        const int w2E = (rr * SP + stx * 64) * 2 + ((lane & 7) ^ (2 * ((rr >> 1) & 3))) * 16;
        int tb[4];
#pragma unroll
        for (int i = 0; i < 4; ++i) tb[i] = reg + trbase(i, g, q4, p4);
        const int v0 = lane * 16;
        const int ta = 4 * dt + 3 * ((lane >> 3) & 1), dgp = sel(ta < 20, (ta * 13) >> 6, (ta >> 2) - 1);
        const int fo = WS_UF + ((7 * m + (LATE ? 4 : 0) + min(lane & 7, LATE ? 2 : 3)) * 6 + dgp) * FS;
        const int oo = WS_OF + (((st * 43) >> 7) * 8 + 2 * m + (lane & 1)) * FS;
        auto fill = [&](int p, int dst) AI { dma4(HW ? rs_w1 : rs_w2, w2E, pos2(true, p) * 32 * (SP * 2), 8 * SP * 2, lb + dst); };
        constexpr int XS = L_HB - L_LATE;
        u64 f0;
        const auto rs_wo = __builtin_amdgcn_make_buffer_rsrc((void*)wo, 0, D_ * D_ * 2, RSRC_W3);
        const int woE = (rr * D_ + dt * 64) * 2 + ((lane & 7) ^ (2 * ((rr >> 1) & 3))) * 16;
        auto fillo = [&](int e) AI { if (HO) dma4(rs_w1, (rr * F_ + (7 * (xcd >> 1) + 4 * (xcd & 1)) * 64) * 2 + (lane & 7) * 16, ((4 * m + e) * 32 + 64 * dt) * (F_ * 2), 8 * F_ * 2, lb + L_LATE + m * 4096 + e * SS); else dma4(rs_wo, woE, (4 * m + ((e + RO * st) & 3)) * 28 * (D_ * 2), 8 * D_ * 2, lb + L_LATE + m * 4096 + e * SS); };
        const int ub = WS_U + 4 * dt * 1024, ob = (OH ? WS_H + (4 * (st & 15) * 16 + 4 * m) * 1024 : WS_O + (4 * st * 16 + 4 * m) * 1024);
        u64 o0 = 0;
        auto ldu = [&](auto pc, auto tc) AI {
          constexpr int p = decltype(pc)::value, t = decltype(tc)::value;
          if constexpr (LATE && p == 6 && t == 0) { asm volatile("s_waitcnt vmcnt(%1)" : "+v"(o0) : "n"(4 * (6 - OPK)) : "memory"); settle(rs_ws, oo, o0, tag); }
          if constexpr (LATE && p == OPK && t == 0) o0 = peek(rs_ws, oo);
          if constexpr (LATE && p >= 6 && CE) ld1_a<t * 1024>(uv[p % NB2][t], rs_ws, v0, ob + (p - 6) * 16384);
          else if constexpr (LATE && p >= 6) ld1_a<0>(uv[p % NB2][t], rs_ws, v0, ob + t * 16384 + ((p - 6 + RO * st) & 3) * 1024);
          else ld1_a<t * 1024>(uv[p % NB2][t], rs_ws, v0, ub + pos2(LATE, p) * KS_);
        };
        auto first = [&]() AI {
          settle(rs_ws, fo, f0, tag);
          PRIO(LATE ? PL2 : PE2);
          if (PF1 && LATE) PRIO(3);
          SFOR((NB2 - (LATE && GK >= 0)) * 4, n, ldu(IC(n / 4), IC(n % 4)); if constexpr (LATE && n + 1 == PF1) PRIO(PL2););
          SB();
        };
        if constexpr (!LATE) {
          const int vr = w2lane(lane);
#pragma unroll
          for (int p = N0; p < 8; ++p) {
            ldw2(f2[p], vr, p);
            if (!SG2 && p == N0 + NSG - 1) { WAITVM(4 * NSG); sig(lb, 2); }
          }
          await(L, 3, 4u);
          await(L, 4, 4u);
          auto turn = [&](auto pc, auto nc) AI {
            constexpr int p = decltype(pc)::value, n = decltype(nc)::value;
            if constexpr (p < T0) return;
            wait4<n>(f2[2 * p]);
            wait4<n>(f2[2 * p + 1]);
            turn_pair(f2[2 * p], f2[2 * p + 1]);
            SB();
          };
#define TURN(p, n) turn(std::integral_constant<int, p>{}, std::integral_constant<int, n>{})
          if constexpr (FA && decltype(idc)::value == 0) {
            fill(0, reg); fill(1, reg + SS);
            await(L, 5, 4u);
            fill(2, reg + 2 * SS); fill(3, reg + 3 * SS);
            fill(4, L_HB + m * 5120); fill(5, L_LATE + reg + 3 * SS);
            WAITVM(48);
            fillo(0); fillo(1); fillo(2);
            PRIO(PA);
            if (FA == 2) { TURN(0, 60); TURN(1, 52); }
            WAITVM(12);
            sig(lb, 16 + m);
            if (FA == 1) { TURN(0, 12); TURN(1, 12); }
            WAITVM(0);
            sig(lb, 16 + m);
            TURN(2, 0);
            f0 = peek(rs_ws, fo);
            TURN(3, 1);
            asm volatile("s_waitcnt vmcnt(0)" : "+v"(f0)::"memory");
            first();
          } else {
          fill(0, reg); fill(1, reg + SS);
          if (SG2) { WAITVM(4 * (8 - N0) + 8); sig(lb, 2); }
          if (IL) TURN(0, 32);
          await(L, 5, 4u);
          fill(2, reg + 2 * SS); fill(3, reg + 3 * SS);
          if (IL) TURN(1, 32);
          fill(4, L_HB + m * 5120); fill(5, L_LATE + reg + 3 * SS);
          if (IL) TURN(2, 32);
          if (OV == 2) sig(lb, 31);
          if (OV) f0 = peek(rs_ws, fo);
          fillo(0); fillo(1); fillo(2);
          WAITVM(12 + (OV ? 1 : 0));
          sig(lb, 16 + m);
          if (OV) {
            asm volatile("s_waitcnt vmcnt(12)" : "+v"(f0)::"memory");
            first();
            TURN(3, 24);
          } else {
            f0 = peek(rs_ws, fo);
            TURN(3, 13);
            WAITVM(1);
            sig(lb, 16 + m);
            asm volatile("s_waitcnt vmcnt(0)" : "+v"(f0)::"memory");
            first();
          }
          }
        } else {
          auto take = [&](int r, int so) AI {
            slot_frags(L, tb, so, f2[r]);
            asm volatile("s_waitcnt lgkmcnt(0)" : "+v"(f2[r][0]), "+v"(f2[r][1]), "+v"(f2[r][2]), "+v"(f2[r][3])::"memory");
          };
          if (OV == 2) { await(L, 31, 4u); f0 = peek(rs_ws, fo); asm volatile("s_waitcnt vmcnt(0)" : "+v"(f0)::"memory"); first(); }
          await(L, 16 + m, 1u);
          if (OV == 1) f0 = peek(rs_ws, fo);
          take(5, 3 * SS);
          fillo(3);
          if (OV == 1) { asm volatile("s_waitcnt vmcnt(4)" : "+v"(f0)::"memory"); first(); }
          if (!OV) f0 = peek(rs_ws, fo);
#pragma unroll
          for (int r = 0; r < 4; ++r) slot_frags(L, tb, r * SS - L_LATE, f2[r]);
          slot_frags(L, tb, XS + m * 1024, f2[4]);
          sig(lb, 20 + m);
          if constexpr (DF == 2 && decltype(idc)::value == 1) { WAITVM(5); if (arrive(lb, 6) == 3u) putflag(WS_OF + (rg * 8 + hp) * FS, lane); }
          if (!OV) {
            await(L, 16 + m, 2u);
            asm volatile("s_waitcnt vmcnt(0)" : "+v"(f0)::"memory");
            if constexpr (DF == 1 && decltype(idc)::value == 1) { if (arrive(lb, 6) == 3u) putflag(WS_OF + (rg * 8 + hp) * FS, lane); }
            first();
          }
        }
        int wr[4], sa = sel(lane == 0, lb + L_CNT, OOB);
        unsigned one = 1u;
        if constexpr (LATE && CE == 2) { PINV(sa); PINV(one); }
#pragma unroll
        for (int e = 0; e < 4; ++e) wr[e] = reg + g * 256 + (LATE ? c ^ (2 * e + (g >> 1)) : (c + 8 * e + 4 * (g >> 1)) & 15) * 16;
        kloop<NK2, NR2, 4, true, LATE ? 6 : 99, NB2, LATE ? GK : -1>(L, tb, 0, f2, uv, acc, ldu,
            [&](auto ic, auto tc) AI {
              constexpr int i = LATE ? decltype(tc)::value : decltype(ic)::value, t = LATE ? decltype(ic)::value : decltype(tc)::value;
              *(LAS f32x4*)(L + wr[(t & 1) + (LATE ? 2 * (i & 1) : 0)] + i * SS + t * 1024) = acc[i][t];
              if constexpr (LATE && CE == 2 && (i & 1) && t == 3) { SB(); post<(i == 1 ? 31 : 10)>(sa, one); }
            }, [&](auto kc) AI {
              constexpr int k = decltype(kc)::value;
              if constexpr (!LATE && k == NK2 - 2) await(L, 20 + m, 1u);
              if constexpr (OV && !LATE && k == 0) sig(lb, 16 + m);
              if constexpr (OV && LATE && k == 1) await(L, 16 + m, 2u);
            });
      }
      if constexpr (LATE) {
        if constexpr (CE != 2) sig(lb, 10);
      } else if constexpr (CE == 2) {
        PRIO(PT);
        LANEVARS;
        const int rw = st * 64 + 8 * (rr & 3) + (rr & 4) + m, eo = (rw * D_ + dt * 64 + (lane & 7) * 8) * 2;
        u32x4 xr[2];
        float bs[2];
#pragma unroll
        for (int e = 0; e < 2; ++e) {
          xr[e] = __builtin_amdgcn_raw_buffer_load_b128(rs_x, eo + e * 32 * D_ * 2, 0, 0);
          bs[e] = lo_f(*(const GAS u16*)(b2 + rw + 32 * e));
        }
        int ra[2];
#pragma unroll
        for (int e = 0; e < 2; ++e) ra[e] = (m * 16 + ((lane & 7) >> 1)) * 1024 + (lane & 1) * 512 + ((4 * e + (rr & 3) + 2 * (rr & 4) + 4 * (lane & 3)) & 15) * 16;
        gbar4(L, lb, 9);
        f32x4 tm[4][4], base[4];
#pragma unroll
        for (int j = 0; j < 4; ++j)
#pragma unroll
          for (int e = 0; e < 4; ++e) tm[j][e] = *(const LAS f32x4*)(L + ra[e >> 1] + j * 4096 + (e & 1) * 256);
        SB();
#pragma unroll
        for (int e = 0; e < 4; ++e) {
          const unsigned a = xr[e >> 1][2 * (e & 1)], b = xr[e >> 1][2 * (e & 1) + 1];
          const float s = bs[e >> 1];
          if (XR) base[e] = tm[0][e] + f32x4{s, lo_f(a), lo_f(b), s}; else
          base[e] = add4(add4(add4(f32x4{lo_f(a), hi_f(a), lo_f(b), hi_f(b)}, f32x4{s, s, s, s}), add4(tm[0][e], tm[1][e])), add4(tm[2][e], tm[3][e]));
          asm volatile("" : "+v"(base[e]));
        }
        int rl = L_LATE + ((rr >> 1) & 1) * SS + ((lane & 7) >> 1) * 1024 + (lane & 1) * 512 + ((8 * (rr & 1) + (rr & 4) + m) ^ ((lane & 3) + 2 * (rr & 2))) * 16;
        int e1 = eo + 32 * D_ * 2;
        asm volatile("" : "+v"(rl), "+v"(e1));
        const auto rs_out = __builtin_amdgcn_make_buffer_rsrc((void*)out, 0, S_ * D_ * 2, RSRC_W3);
        if constexpr (FLP) {
          int ca = lb + L_CNT + 4 * 31, ev = eo, it = 0;
          f32x4 b0 = base[0], b1 = base[1];
          _Pragma("unroll 1") do {
            asm volatile("" : "+v"(ca), "+v"(ev), "+v"(rl), "+v"(b0), "+v"(b1));
            await_at(ca, 4u);
            SFOR(8, j, tm[j >> 1][j & 1] = *(const LAS f32x4*)(L + rl + (j >> 1) * 4096 + (j & 1) * 256););
            SB();
            const f32x4 s0 = (b0 + tm[0][0]) + tm[1][0] + (tm[2][0] + tm[3][0]), s1 = (b1 + tm[0][1]) + tm[1][1] + (tm[2][1] + tm[3][1]);
            __builtin_amdgcn_raw_buffer_store_b128(u32x4{pack2(s0[0], s0[1]), pack2(s0[2], s0[3]), pack2(s1[0], s1[1]), pack2(s1[2], s1[3])}, rs_out, ev, 0, AUX_SC1);
            SB();
            ca = lb + L_CNT + 4 * 10; ev = e1; rl += 2 * SS; b0 = base[2]; b1 = base[3];
            asm volatile("" : "+s"(it));
          } while (++it < 2);
        } else {
        SFOR(2, e,
          PRIO(e ? PZ : PH);
          await(L, e ? 10 : 31, 4u);
          SFOR(8, j, tm[j >> 1][j & 1] = *(const LAS f32x4*)(L + rl + 2 * e * SS + (j >> 1) * 4096 + (j & 1) * 256););
          SB();
          f32x4 s0, s1;
          if constexpr (e) {
            s0 = (base[2] + tm[0][0]) + tm[1][0] + (tm[2][0] + tm[3][0]);
            s1 = (base[3] + tm[0][1]) + tm[1][1] + (tm[2][1] + tm[3][1]);
          } else {
            s0 = add4(add4(add4(base[0], tm[0][0]), tm[1][0]), add4(tm[2][0], tm[3][0]));
            s1 = add4(add4(add4(base[1], tm[0][1]), tm[1][1]), add4(tm[2][1], tm[3][1]));
          }
          __builtin_amdgcn_raw_buffer_store_b128(u32x4{pack2(s0[0], s0[1]), pack2(s0[2], s0[3]), pack2(s1[0], s1[1]), pack2(s1[2], s1[3])}, rs_out, e ? e1 : eo, 0, AUX_SC1);
          SB();
        );
        }
      } else {
        LANEVARS;
        const int eo = ((st * 64 + 8 * rr + m) * D_ + dt * 64 + (lane & 7) * 8) * 2;
        u32x4 xr[2];
        float bs[2];
#pragma unroll
        for (int e = 0; e < 2; ++e) {
          xr[e] = __builtin_amdgcn_raw_buffer_load_b128(rs_x, eo + e * 4 * D_ * 2, 0, 0);
          bs[e] = lo_f(*(const GAS u16*)(b2 + st * 64 + 8 * rr + 4 * e + m));
        }
        int ra[2];
#pragma unroll
        for (int e = 0; e < 2; ++e) ra[e] = (m * 16 + ((lane & 7) >> 1)) * 1024 + (lane & 1) * 512 + ((rr + 8 * e + 4 * (lane & 3)) & 15) * 16;
        gbar4(L, lb, 9);
        f32x4 tm[4][4], base[4];
#pragma unroll
        for (int j = 0; j < 4; ++j)
#pragma unroll
          for (int e = 0; e < 4; ++e) tm[j][e] = *(const LAS f32x4*)(L + ra[e >> 1] + j * 4096 + (e & 1) * 256);
        SB();
#pragma unroll
        for (int e = 0; e < 4; ++e) {
          const unsigned a = xr[e >> 1][2 * (e & 1)], b = xr[e >> 1][2 * (e & 1) + 1];
          const float s = bs[e >> 1];
          base[e] = add4(add4(add4(f32x4{lo_f(a), hi_f(a), lo_f(b), hi_f(b)}, f32x4{s, s, s, s}), add4(tm[0][e], tm[1][e])), add4(tm[2][e], tm[3][e]));
          asm volatile("" : "+v"(base[e]));
        }
#pragma unroll
        for (int e = 0; e < 2; ++e) {
          ra[e] = L_LATE + (rr >> 1) * SS + ((lane & 7) >> 1) * 1024 + (lane & 1) * 512 + ((8 * (rr & 1) + 4 * e + m) ^ ((lane & 3) + 2 * (rr & 2))) * 16;
          asm volatile("" : "+v"(ra[e]));
        }
        PRIO(3);
        await(L, 10, 4u);
#pragma unroll
        for (int j = 0; j < 4; ++j)
#pragma unroll
          for (int e = 0; e < 4; ++e) tm[j][e] = *(const LAS f32x4*)(L + ra[e >> 1] + j * 4096 + (e & 1) * 256);
        SB();
        const auto rs_out = __builtin_amdgcn_make_buffer_rsrc((void*)out, 0, S_ * D_ * 2, RSRC_W3);
#pragma unroll
        for (int e = 0; e < 2; ++e) {
          const f32x4 s0 = (base[2 * e] + tm[0][2 * e]) + tm[1][2 * e] + (tm[2][2 * e] + tm[3][2 * e]);
          const f32x4 s1 = (base[2 * e + 1] + tm[0][2 * e + 1]) + tm[1][2 * e + 1] + (tm[2][2 * e + 1] + tm[3][2 * e + 1]);
          __builtin_amdgcn_raw_buffer_store_b128(u32x4{pack2(s0[0], s0[1]), pack2(s0[2], s0[3]), pack2(s1[0], s1[1]), pack2(s1[2], s1[3])},
                                                 rs_out, eo + e * 4 * D_ * 2, 0, AUX_SC1);
        }
      }
    }
    asm volatile("s_nop %0" ::"n"(decltype(idc)::value));
    FINISH();
  };
  auto utile = [&](auto latec) AI {
    constexpr bool LATE = decltype(latec)::value;
      auto gemm1 = [&](auto ntc) AI {
        constexpr int NT = decltype(ntc)::value, NL = 8 * NT, NB = NB1, TPN = TP * 4;
        const int t0 = NT == 5 ? 5 * dg : 4 * dg + 4;
        u32x4 fr[8][4], hv[NB], f2[LATE ? 6 : 8][4];
        LANEVARS;
        PRIO(LATE ? PLP : PEP);
        const int v0 = lane * 16, v4 = v0 + 4096, wr = reg + v0;
        const int tr = ((NT == 5 ? TR5 : TR4) >> (4 * (ft - 7 * (xcd >> 1) - 4 * (xcd & 1)))) & 7;
        auto tl = [&](int t) AI { return TR ? t + tr - sel(t + tr >= NT, NT, 0) : t; };
        int ho[8], vt;
        auto ld = [&](auto nc) AI {
          constexpr int n = decltype(nc)::value, t = n >> 3;
          if constexpr (TR) {
            if constexpr ((n & 7) == 0) { vt = v0 + tl(t) * 1024; PINV(vt); }
            ld1_a<0>(hv[n % NB], rs_ws, vt, ho[n & 7]);
          } else ld1_a<(t & 3) * 1024>(hv[n % NB], rs_ws, t < 4 ? v0 : v4, ho[n & 7]);
        };
        auto start = [&](int fo, u64 f0) AI {
          settle(rs_ws, fo, f0, tag);
          PRIO(LATE ? PL1 : PE1);
#pragma unroll
          for (int p = 0; p < 8; ++p) ho[p] = WS_H + (m * 16 + k1((LATE ? 8 : 0) + p)) * KS_ + t0 * 1024;
          SFOR(NB, n, ld(n_c););
          SB();
        };
        {
          const int w1E = ((m * 512 + rr) * F_ + ft * 64) * 2 + ((lane & 7) ^ (2 * ((rr >> 1) & 3))) * 16;
          auto fill = [&](int j) AI { dma4(rs_w1, w1E, k1(j) * 32 * (F_ * 2), 8 * F_ * 2, lb + reg + ((j >> 2) & 1) * L_LATE + (j & 3) * SS); };
          int tb[2][4];
#pragma unroll
          for (int i = 0; i < 8; ++i) tb[i >> 2][i & 3] = (i >> 2) * L_LATE + m * 4096 + trbase(i & 3, g, q4, p4);
          auto take = [&](int j) AI { slot_frags(L, tb[j >> 2], (j & 3) * SS, fr[j]); };
          auto taken = [&](int j) AI { asm volatile("s_waitcnt lgkmcnt(0)" : "+v"(fr[j][0]), "+v"(fr[j][1]), "+v"(fr[j][2]), "+v"(fr[j][3])::"memory"); };
          const int fo = WS_HF + (m * 32 + 2 * (((LATE ? 8 : 0) + ((lane >> 1) & 7) + off1) & 15) + (lane & 1)) * FS;
          u64 f0;
          if constexpr (!LATE) {
            if constexpr (NT == 4) { WAITVM(W0); if (GS) await(L, 7, 4u); } else WAITVM(W5);
            if constexpr (W6 < 12) { fill(2); WAITVM(W6); }
#pragma unroll
            for (int j = W6 < 12 ? 3 : 2; j < 8; ++j) fill(j);
            await(L, 0, 4u);
            gettag();
            SFOR(8, j, WAITVM(28 + (j > 8 - PKE)); take(j); taken(j); if constexpr (j == 8 - PKE) f0 = peek(rs_ws, fo); fill(8 + j););
            WAITVM(17); sig(lb, 12 + m);
            asm volatile("s_waitcnt vmcnt(%1)" : "+v"(f0) : "n"(4 * PKE) : "memory");
            start(fo, f0);
            WAITVM(NB); sig(lb, 12 + m);
          } else {
            if (m == 0) {
              const auto rs_b1 = __builtin_amdgcn_make_buffer_rsrc((void*)b1, 0, F_ * 2, RSRC_W3);
              await(L, 0, 4u);
              touch(rs_b1, sel(lane < 32, lane * 4, OOB), ft * 128, lb + L_B1);
            }
            await(L, 12 + m, 1u);
            f0 = peek(rs_ws, fo);
#pragma unroll
            for (int j = 0; j < 4; ++j) take(j);
            asm volatile("s_waitcnt vmcnt(0)" : "+v"(f0)::"memory");
            if (m == 0) sig(lb, 11);
            start(fo, f0);
            await(L, 12 + m, 2u);
#pragma unroll
            for (int j = 4; j < 8; ++j) take(j);
#pragma unroll
            for (int j = 0; j < 8; ++j) taken(j);
            sig(lb, 30);
          }
        }
        const int ra = (lane >> 5) * L_LATE + ((LATE ? 2 : 0) + (g & 1)) * 1024 + m * 256 + c * 16;
        const int ba = L_B1 + (16 * ((LATE ? 2 : 0) + (g & 1)) + 4 * m + 2 * (lane >> 5)) * 2;
        const int ub = WS_U + (2 * ft + (LATE ? 1 : 0)) * KS_ + t0 * 1024 + m * 256 + c * 16, uo = ub + tl(g) * 1024;
        const int vr = w2lane(lane);
        int sa = sel(lane == 0, lb + L_CNT, OOB);
        unsigned one = 1u;
        PINV(sa); PINV(one);
        f32x4 acc[2][4], pt[4];
        float bq[2], v[2], z[2];
        unsigned pr[6], cv, cr;
        auto rd = [&](int t, int o) AI {
#pragma unroll
          for (int e = 0; e < 4; ++e) pt[e] = *(const LAS f32x4*)(L + ra + o + g1buf(NT, t) + e * 4096);
        };
        auto step = [&](auto sc, int t) AI {
          constexpr int s = decltype(sc)::value;
          if constexpr (s == 0) { pt[0] = add4(pt[0], pt[1]); PINV(pt[0]); }
          if constexpr (s == 1) { pt[2] = add4(pt[2], pt[3]); PINV(pt[2]); }
          if constexpr (s == 2) { pt[0] = add4(pt[0], pt[2]); PINV(pt[0]); }
          if constexpr (s == 3) {
#pragma unroll
            for (int e = 0; e < 2; ++e) { v[e] = vadd(fold32(pt[0][e], pt[0][2 + e]), bq[e]); PINV(v[e]); }
          }
          if constexpr (s == 4) {
#pragma unroll
            for (int e = 0; e < 2; ++e) {
              const float p = solo(__builtin_fmaf(vmul(v[e], v[e]), -0.1029432395800235f, -2.302208198144325f));
              z[e] = vmul(v[e], p); PINV(z[e]);
            }
          }
          if constexpr (s == 5) {
#pragma unroll
            for (int e = 0; e < 2; ++e) { z[e] = vadd(__builtin_amdgcn_exp2f(z[e]), 1.0f); PINV(z[e]); }
          }
          if constexpr (s == 6) {
            pr[t] = pack2(vmul(__builtin_amdgcn_rcpf(z[0]), v[0]), vmul(__builtin_amdgcn_rcpf(z[1]), v[1]));
            PINV(pr[t]);
          }
        };
        unsigned sw[4];
        auto fine = [&](auto sc, int t) AI {
          constexpr int s = decltype(sc)::value;
          if constexpr (s < 6) {
            constexpr int d = s < 2 || s >= 4 ? 0 : 2, o = s < 2 ? 1 : s < 4 ? 3 : 2, e = 2 * (s & 1);
            pt[d][e] = vadd(pt[d][e], pt[o][e]); pt[d][e + 1] = vadd(pt[d][e + 1], pt[o][e + 1]); PINV(pt[d]);
          }
          if constexpr (s == 6) {
#pragma unroll
            for (int e = 0; e < 2; ++e) {
              const auto r = __builtin_amdgcn_permlane32_swap(__float_as_uint(pt[0][e]), __float_as_uint(pt[0][2 + e]), false, false);
              sw[2 * e] = r[0]; sw[2 * e + 1] = r[1]; PINV(sw[2 * e]); PINV(sw[2 * e + 1]);
            }
          }
          auto two = [&](float (&d)[2], auto f) AI { d[0] = f(0); d[1] = f(1); PINV(d[0]); PINV(d[1]); };
          if constexpr (s == 7) two(v, [&](int e) AI { return vadd(__uint_as_float(sw[2 * e]), __uint_as_float(sw[2 * e + 1])); });
          if constexpr (s == 8) two(v, [&](int e) AI { return vadd(v[e], bq[e]); });
          if constexpr (s == 9) two(z, [&](int e) AI { return vmul(v[e], v[e]); });
          if constexpr (s == 10) two(z, [&](int e) AI { return solo(__builtin_fmaf(z[e], -0.1029432395800235f, -2.302208198144325f)); });
          if constexpr (s == 11) two(z, [&](int e) AI { return vmul(v[e], z[e]); });
          if constexpr (s == 12 || s == 13) { z[s - 12] = __builtin_amdgcn_exp2f(z[s - 12]); PINV(z[s - 12]); }
          if constexpr (s == 14) two(z, [&](int e) AI { return vadd(z[e], 1.0f); });
          if constexpr (s == 15 || s == 16) { z[s - 15] = __builtin_amdgcn_rcpf(z[s - 15]); PINV(z[s - 15]); }
          if constexpr (s == 17) two(z, [&](int e) AI { return vmul(z[e], v[e]); });
          if constexpr (s == 18) { pr[t] = pack2(z[0], z[1]); PINV(pr[t]); }
        };
        auto put = [&](unsigned a, unsigned b, unsigned cc, unsigned d, int off) AI {
          const auto s0 = __builtin_amdgcn_permlane32_swap(a, cc, false, false), s1 = __builtin_amdgcn_permlane32_swap(b, d, false, false);
          const auto r0 = __builtin_amdgcn_permlane16_swap(s0[0], s1[0], false, false), r1 = __builtin_amdgcn_permlane16_swap(s0[1], s1[1], false, false);
          __builtin_amdgcn_raw_buffer_store_b128(u32x4{r0[0], r1[0], r0[1], r1[1]}, rs_ws, off, 0, AUX_SC1);
          SB();
        };
        auto bias = [&](int o) AI { const unsigned b = *(const LAS unsigned*)(L + ba + o); bq[0] = lo_f(b); bq[1] = hi_f(b); };
        SFOR(NL, n,
          constexpr int t = n >> 3, p = n & 7, nw = !LATE && t == NT - 1 ? 4 * (p < NE ? p : NE) : 0;
          if constexpr (n == 9 && !LATE) check(L, cr, 30, 4u);
          if constexpr (n == 13) { check(L, cv, 11, 1u); bias(0); }
          if constexpr (t >= 2 && p == RP) { check(L, cv, 24 + t - 2, 8u); rd(t - 2, 0); if constexpr (NT == 5 && t == 2) post<29>(sa, one); }
          if constexpr (!LATE && n == NL - TPN) PRIO(3);
          if constexpr (t >= 2 && p == RP - 1) cv = look(L, 24 + t - 2);
          if constexpr (n == 7 && !LATE) cr = look(L, 30);
          if constexpr (n == 11) cv = look(L, 11);
          if constexpr (n == NL - 2) { cr = look(L, 29); cv = look(L, 24 + NT - 2); }
          SB();
          if constexpr (!CH) wait1<(n + NB < NL ? n + NB : NL) - 1 - n + nw>(hv[n % NB]);
          else if constexpr (!(n & 1)) wait2<(n + NB < NL ? n + NB : NL) - 2 - n + nw>(hv[n % NB], hv[(n + 1) % NB]);
          SFOR(4, i,
            constexpr int ci = CH ? 2 * (p & 1) + (i >> 1) : i, cn = CH ? (n & ~1) + (i & 1) : n;
            mfma_ip<(cn & 7) == 0>(acc[t & 1][ci], fr[cn & 7][ci], hv[cn % NB]);
            if constexpr (t >= 1 && (p == 1 || p == 2) && (i & 1)) *(LAS f32x4*)(L + wr + g1buf(NT, t - 1) + (2 * p - 2 + (i >> 1)) * 1024) = acc[(t - 1) & 1][2 * p - 2 + (i >> 1)];
            constexpr int se = 4 * (p - RP - 1) + i - 1;
            if constexpr (!MSP && t >= 2 && se >= 0 && se < 7) step(IC(se), t - 2);
            if constexpr (MSP && t >= 2 && se >= 0) SFOR(MSP, k, if constexpr (se * MSP + k < 19) fine(IC(se * MSP + k), t - 2););
            SB();
          );
          if constexpr (t >= 1 && p == 2) post<24 + t - 1>(sa, one);
          if constexpr (CH && (n & 1) && n + NB - 1 < NL) ld(IC(n + NB - 1));
          if constexpr ((!CH || (n & 1)) && n + NB < NL) ld(IC(n + NB));
          if constexpr (CH && !LATE && t == NT - 1 && (p & 1) && p - 1 < NE) ldw2(f2[p - 1], vr, p - 1);
          if constexpr ((!CH || (p & 1)) && !LATE && t == NT - 1 && p < NE) ldw2(f2[p], vr, p);
          SB();
        );
        PRIO(LATE ? PG : PGE);
        if constexpr (NT == 5) check(L, cr, 29, 8u);
        asm volatile("s_nop 7" ::: "memory");
#pragma unroll
        for (int i = 0; i < 4; ++i) *(LAS f32x4*)(L + wr + g1buf(NT, NT - 1) + i * 1024) = acc[(NT - 1) & 1][i];
        post<24 + NT - 1>(sa, one);
        if constexpr (LATE) post<4>(sa, one);
        check(L, cv, 24 + NT - 2, 8u);
        rd(NT - 2, 0);
        if constexpr (!LATE) { post<3>(sa, one); PRIO(PGF); }
        SFOR(7, s, step(s_c, NT - 2););
        if constexpr (NT == 5) put(pr[0], pr[1], pr[2], pr[3], uo);
        else if constexpr (!LATE) put(pr[0], pr[1], pr[2], pr[2], sel(lane < 48, uo, OOB));
        if constexpr (LATE) {
          const unsigned b2q = *(const LAS unsigned*)(L + ba - 64);
          await(L, 24 + NT - 1, 8u);
          if (PG != 3) PRIO(3);
          {
            f32x4 qa[8];
#pragma unroll
            for (int e = 0; e < 8; ++e) qa[e] = *(const LAS f32x4*)(L + ra - (e >> 2) * 2048 + g1buf(NT, NT - 1) + (e & 3) * 4096);
            post<5>(sa, one);
            const f32x4 s0 = (qa[0] + qa[1]) + (qa[2] + qa[3]), s1 = (qa[4] + qa[5]) + (qa[6] + qa[7]);
            f32x4 y[1] = {f32x4{fold32(s0[0], s0[2]), fold32(s0[1], s0[3]), fold32(s1[0], s1[2]), fold32(s1[1], s1[3])} + f32x4{bq[0], bq[1], lo_f(b2q), hi_f(b2q)}};
            unsigned o2[2];
            gelu_pack<1>(y, o2);
            pr[NT - 1] = o2[0]; pr[5] = o2[1];
          }
          if constexpr (NT == 4) put(pr[0], pr[1], pr[2], pr[3], uo);
          if constexpr (NT == 5) put(pr[4], pr[5], pr[4], pr[5], sel(lane < 32, ub + tl(4) * 1024 - g * KS_, OOB));
          else put(pr[5], pr[5], pr[5], pr[5], sel(lane < 16, ub + tl(3) * 1024 - KS_, OOB));
          DRAIN();
          if (arrive(lb, 6) == 3u) { await(L, 2, 4u); putflag(WS_UF + (ft * 6 + dg) * FS, lane); }
        }
        gemm2(latec, IC(LATE ? 0 : NE), f2, IC(2 * NT - 6 + LATE));
      };
      if (dg < 4) gemm1(IC(5));
      gemm1(IC(4));
  };
  if (role == 0) utile(std::false_type{});
  if (role == 1) utile(std::true_type{});
  if (role == 2) {
    u32x4 f2[8][4];
    constexpr int NA = (7 - EW) * EG < 8 ? (7 - EW) * EG : 8;
    auto na = [](int n) constexpr { return n < EW ? 0 : (n + 1 - EW) * EG < 8 ? (n + 1 - EW) * EG : 8; };
    auto nt = [](int n) constexpr { return n < TW ? 0 : n + 1 - TW < 4 ? n + 1 - TW : 4; };
    {
      LANEVARS;
      const int vr = w2lane(lane);
        const int cq = (lane & 7) ^ (2 * ((rr >> 1) & 1)) ^ (4 * (m & 1));
        const int vq = sel(cq < 7, (8 * m + rr) * (N3_ * 2) + hp * 112 + cq * 16, OOB);
        const int va = (r0 + 16 + 8 * m + rr) * (D_ * 2) + ((lane & 7) ^ (4 * (m & 1) + (rr >> 1))) * 16;
        const int so_ = WS_ST + min(max(r0 - 16 + min(64 * m + lane, 207), 0), S_ - 1) * 16;
        auto pcs = [&](int pp, int from, int to) AI {
          const bool wr = rotq >= 7 - pp;
#pragma unroll
          for (int n = from; n < to; ++n) {
            if (n < 6) piece(false, vq, (pp + 2) % 3, fs + sel(wr, (pp - 7) * 64 * N3_ * 2, pp * 64 * N3_ * 2), n);
            else piece(true, va, (pp + 2) % 3, rotq * 128 + sel(wr, (pp - 7) * 128, pp * 128), n - 6);
          }
        };
        u64 e0 = peek(rs_ws, WS_E);
        if (AV < 14) WAITVM(AV);
        pcs(1, 0, LA0);
        asm volatile("s_waitcnt vmcnt(%1)" : "+v"(e0) : "n"(LA0) : "memory");
        mktag(e0);
        WAITLG();
        BAR();
        auto warm = [&](int e) AI {
          const auto rs_wo = __builtin_amdgcn_make_buffer_rsrc((void*)wo, 0, D_ * D_ * 2, RSRC_W3);
          const int j = idx - 28, o = WORD ? (e + 7) % 11 : e;
          if (o < 3) touch(rs_w2, (lane >> 2) * (S_ * 2) + (lane & 3) * 128, (448 * m + 256 + 48 * j + 16 * o) * (S_ * 2) + (xcd ^ SX) * 512, lb + L_DUMMY);
          else if (o < 7) touch(rs_wo, lane * 128, ((o - 3) * 16 + j * 4 + m) * 8192, lb + L_DUMMY);
          else touch(rs_w2, (lane >> 2) * (S_ * 2) + (lane & 3) * 128, (448 * m + 64 * j + 16 * (o - 7)) * (S_ * 2) + (xcd ^ SX) * 512, lb + L_DUMMY);
        };
        u32x4 sg;
        SFOR(6, n0,
          constexpr int n = n0 + 1, lb_ = n == 5 ? LA6 : n < 3 ? LA0 : LA1, la = lb_ < 12 ? lb_ : 12, lp_ = n == 6 ? LA6 : n < 4 ? LA0 : LA1;
          pcs(n, lp_, 13);
          if constexpr (n == 3) asm volatile("buffer_load_dwordx4 %0, %1, %2, 0 offen sc1" : "=v"(sg) : "v"(so_), "s"(rs_ws) : "memory");
          if constexpr (n < 6) pcs(n + 1, 0, lb_);
          if constexpr (n == 4) {
            asm volatile("" : "+v"(sg));
            _Pragma("unroll 1")
            for (unsigned spins = 0; spins < SPIN_CAP; ++spins) {
              if (__all((sg.y == (unsigned)tag) & (sg.w == (unsigned)(tag >> 32)) & (sg.x == sg.z))) break;
              __builtin_amdgcn_s_sleep(4);
              asm volatile("buffer_load_dwordx4 %0, %1, %2, 0 offen sc1\n\ts_waitcnt vmcnt(0)" : "=v"(sg) : "v"(so_), "s"(rs_ws) : "memory");
            }
            *(LAS float*)(L + A_MEAN + (64 * m + lane) * 4) = -lo_f(sg.x);
            *(LAS float*)(L + A_RSTD + (64 * m + lane) * 4) = __builtin_amdgcn_rcpf(hi_f(sg.x));
            WAITLG();
          }
          constexpr int ge = na(n) - na(n - 1);
          SFOR(ge, e, ldw2(f2[na(n - 1) + e], vr, na(n - 1) + e););
          if constexpr (nt(n) > nt(n - 1)) {
            static_assert(2 * nt(n) <= na(n - 2), "");
            wait4<63>(f2[2 * nt(n) - 2]); wait4<63>(f2[2 * nt(n) - 1]);
            turn_pair(f2[2 * nt(n) - 2], f2[2 * nt(n) - 1]);
            SB();
          }
          if (n >= WST && idx >= 28) {
            SFOR(WPI, e, warm((n - WST) * WPI + e););
            WAITVM((n < 6 ? la : 0) + WPI + 4 * ge);
          } else {
            WAITVM((n < 6 ? la : 0) + 4 * ge);
          }
          BAR();
        );
        sig(lb, 3);
        if (WRM && idx >= 28) {
          auto code = [&]() AI {
            u64 pc;
            unsigned co = (((idx - 28) * 4 + m) * 64 + lane) * 128;
            unsigned dl;
            asm volatile("s_getpc_b64 %0\n.Lct%=:\n\ts_mov_b32 %1, .Lct%=-.Lfb_start" : "=s"(pc), "=s"(dl));
            pc -= dl;
            asm volatile("s_mov_b32 m0, %2\n\tv_min_u32 %0, .Lfb_end-.Lfb_start-8, %0\n\tglobal_load_lds_dword %0, %1" : "+v"(co) : "s"(pc), "s"(lb + L_DUMMY) : "memory");
          };
          if (CT == 1) code();
          if (WSL) touch(rs_w2, lane * (S_ * 2), (448 * m + 64 * (idx - 28)) * (S_ * 2) + (xcd ^ SX) * 512 + 384, lb + L_DUMMY);
#pragma unroll
          for (int e = (7 - WST) * WPI; e < WN; ++e) { if (WT < 9) WAITVM(WT < 9 ? WT : 0); if (e == WP) await(L, 6, 4u); warm(e); if (CT == 2 && e == 2) code(); }
          if (CT == 3) code();
          DRAIN();
          SFOR(NA, p, wait4<0>(f2[p]););
        }
    }
    if (idx < 28) gemm2(std::false_type{}, IC(NA + 16 * nt(6)), f2, IC(0));
    FINISH();
  } else {
        auto att = [&](auto jc) AI {
          constexpr int J = decltype(jc)::value, NH = J ? 2 : 4, H0 = J ? 0 : 2;
          const int H = m & 1;
          LANEVARS;
          f32x4 qa[6][6], qh[NH];
          int wa[2], xa[2][2];
#pragma unroll
          for (int i = 0; i < 2; ++i) {
            const int by = sel((i == 1) & (p4 == 3), 112, 56 * H + 32 * i + 8 * p4);
            wa[i] = A_W + (8 * g + q4) * 128 + (((by >> 4) ^ (2 * ((q4 >> 1) | ((g & 1) << 1)))) << 4) + (by & 8);
          }
          int wb[2] = {wa[0] + A_WS + 12288, wa[1] + A_WS + 12288};
#pragma unroll
          for (int i = 0; i < 4; ++i) xa[i >> 1][i & 1] = (i >> 1) * A_X1 + c * 128 + (((4 * (i & 1) + g) ^ ((c >> 1) & 7)) << 4);
          asm volatile("" : "+v"(wa[0]), "+v"(wa[1]), "+v"(wb[0]), "+v"(wb[1]), "+v"(xa[0][0]), "+v"(xa[0][1]), "+v"(xa[1][0]), "+v"(xa[1][1]));
          u32x4 wf[6], xf[7];
          u32x2 lo;
          auto rdx = [&](int k, int t) AI {
            const int sg = ((k >> 1) + 2) % 3;
            xf[t] = *(const LAS u32x4*)(L + xa[sg ? 1 : 0][k & 1] + (sg == 2 ? A_XS : 0) + (t == 6 ? 0 : (1 + 6 * J + t) * 2048));
          };
          auto rdw = [&](int k, int i, int hf) AI {
            const int o = (((k >> 1) + 2) % 3) * 2 + (k & 1);
            const LAS char* p = L + (o < 3 ? wa[i & 1] + o * 12288 : wb[i & 1] + (o - 3) * 12288) + (i >> 1) * 4096;
            if (hf) { const u32x2 hi = tr_read(p + 512); wf[i] = u32x4{lo.x, lo.y, hi.x, hi.y}; }
            else lo = tr_read(p);
          };
          const bool c0 = c == 0;
          int exa = lb + A_X1 + H * 4096 + lane * 16;
          int ta = lb + A_MEAN + (96 * J + c) * 4, tk = lb + A_RSTD + (96 * J + 4 * g + (c & 1)) * 4, tc = lb + A_CS + (H * 96 + 4 * g) * 4, tv = lb + A_CS + (H * 96 + 64 + c) * 4;
          int sa = sel(lane == 0, lb + L_CNT, OOB);
          unsigned one = 1u;
          f32x2 mk[2][2];
          {
            const int d0 = 16 + (c & 14) - 4 * g, hi = min(r0 + c, 14);
#pragma unroll
            for (int e = 0; e < 4; ++e) {
              const int dk = d0 - 16 * (e >> 1) - 2 * (e & 1);
              mk[1][e >> 1][e & 1] = sel((unsigned)dk <= 14u, 0.f, -3.0e38f);
              mk[0][e >> 1][e & 1] = sel((unsigned)dk <= (unsigned)hi, 0.f, -3.0e38f);
            }
          }
          PINV(exa); PINV(ta); PINV(tk); PINV(tc); PINV(tv); PINV(sa); PINV(one); PINV(mk[1][0]); PINV(mk[1][1]);
          if constexpr (J) { mk[0][0] = mk[1][0]; mk[0][1] = mk[1][1]; } else { PINV(mk[0][0]); PINV(mk[0][1]); }
          float mc[7], rc[6];
          f32x2 rk[7];
          auto tab = [&](int e) AI {
            const int t = e / 3;
            if (e % 3 == 0) mc[t] = *(const LAS float*)(LA_(ta) + 64 * t);
            if (e % 3 == 1) { const LAS float* p = (const LAS float*)(LA_(tk) + 64 * t); rk[t] = f32x2{p[0], p[2]}; }
            if (e % 3 == 2) rc[t] = *(const LAS float*)(LA_(ta) + A_RSTD - A_MEAN + 64 * (t + 1));
          };
          if (LDF) {
            DRAIN();
            if (arrive(lb, 1) == 3u) putflag(WS_HF + un * FS, lane);
          }
          BAR();
          PRIO(2);
#pragma unroll
          for (int t = 0; t < 7; ++t) rdx(0, t);
#pragma unroll
          for (int i = 0; i < 10; ++i) rdw(0, i >> 1, i & 1);
          gettag();
          SB();
          SFOR(14, k,
            SFOR(6, ci,
              constexpr int i = (k == 13 && J == 0) ? (ci + 2) % 6 : ci;
              SFOR(7, t,
                if constexpr (SKA && k > 0 && (J ? (t == 6 || (SKA > 1 && t == 4 && i < 4)) : (t == 4 && i < 4) || (SKA > 1 && t == 3 && i < 4))) {
                } else if constexpr (t < 6) {
                  if constexpr (i < 4) mfma_ip<k == 0>(qa[t][i], wf[i], xf[t]); else mfma_ip<k == 0>(qa[t][i], xf[t], wf[i]);
                } else if constexpr (i >= H0 && i < H0 + NH) {
                  if constexpr (i < 4) mfma_ip<k == 0>(qh[i - H0], wf[i], xf[6]); else mfma_ip<k == 0>(qh[i - H0], xf[6], wf[i]);
                  if constexpr (k < 13 && i == H0 + NH - 1 && J == 0) rdx(k + 1, 6);
                }
                if constexpr (ci == 0 && t < 2) rdw(k, 5, t);
                if constexpr (ci == (k == 13 ? 0 : H0) && t == 2) { SFOR(4, j, xf[6][j] = sel(c0, 0x3f803f80u, xf[6][j]);); asm volatile("" : "+v"(xf[6])); }
                if constexpr ((k & 1) && k < 13 && ci == 1 && t == 1) { WAITLG(); BAR(); }
                if constexpr (k < 13 && ci >= 1 && (t == 2 || t == 3)) rdw(k + 1, ci - 1, t - 2);
                if constexpr (k < 13 && ci == 5 && t < 6) rdx(k + 1, t);
                if constexpr (k < 13 && J == 1 && ci == 2 && t == 0) rdx(k + 1, 6);
                if constexpr (k == 13 && t == 2 && ci >= 2 && ci < NH + 2) {
                  constexpr int ip = H0 + ci - 2;
                  if constexpr (ip < 4) *(LAS f32x4*)(LA_(sel(c0, tc, lb + L_DUMMY)) + 64 * ip) = qh[ci - 2];
                  else *(LAS float*)(LA_(sel(g == 0, tv, lb + L_DUMMY)) + 64 * (ip - 4)) = qh[ci - 2][0];
                }
                if constexpr (k == 13 && J == 0 && t == 4 && ci >= 2) *(LAS f32x4*)(LA_(exa) + (ci - 2) * 1024) = qa[5][ci];
                if constexpr (k == 13 && (J ? ci == 3 && t == 2 : ci == 5 && t == 4)) { SB(); post<4>(sa, one); }
                if constexpr (k == 13 && ci >= 2 && t < 5) tab(5 * (ci - 2) + t);
                SB();
              );
            );
          );
          await(L, 4, 4u);
          f32x4 c4[4], ex[4];
          float cv[2];
#pragma unroll
          for (int i = 0; i < 4; ++i) c4[(i + 2) & 3] = *(const LAS f32x4*)(LA_(tc) + 64 * ((i + 2) & 3));
#pragma unroll
          for (int i = 0; i < 4; ++i) { if constexpr (J) ex[i] = *(const LAS f32x4*)(LA_(exa) + i * 1024); else ex[i] = qh[i & (NH - 1)]; }
#pragma unroll
          for (int i = 0; i < 2; ++i) cv[i] = *(const LAS float*)(LA_(tv) + 64 * i);
          SB();
          post<5>(sa, one);
          const bool odd = ta & 4;
          f32x2 lg[6][2];
#pragma unroll
          for (int u = 0; u < 6; ++u) {
            const float sc = rc[u] * (0.18898223650461362f * 1.4426950408889634f);
            lg[u][0] = rk[u] * sc; lg[u][1] = rk[u + 1] * sc;
            PINV(lg[u][0]); PINV(lg[u][1]);
          }
          int tm = (tk & ~4) + A_MEAN - A_RSTD;
          int ov = exa - lb + WS_O - A_X1 + ((12 * rg + 6 * J) * 16 + 2 * hp - 3 * H) * 1024;
          unsigned p0 = sel(odd, 0x01000c0cu, 0x0c0c0100u), p1 = sel(odd, 0x03020c0cu, 0x0c0c0302u);
          PINV(tm); PINV(ov); PINV(p0); PINV(p1);
          SB();
          auto pk4 = [&](f32x4 v) AI { return u32x2{pack2(v[0], v[1]), pack2(v[2], v[3])}; };
          auto frag = [&](f32x4 a, f32x4 b, float mu, int i) AI {
            const u32x2 l = pk4(c4[i] * mu + a), h = pk4(c4[i + 1] * mu + b);
            return u32x4{l.x, l.y, h.x, h.y};
          };
          f32x4 s0[6], s1[6], m4[7];
          u32x4 kf[7], qf[6];
          kf[0] = frag(ex[0], ex[1], mc[0], 2);
          qf[0] = frag(qa[0][0], qa[0][1], mc[1], 0);
#pragma unroll
          for (int u = 0; u < 6; ++u) {
            kf[u + 1] = frag(qa[u][2], qa[u][3], mc[u + 1], 2);
            SB();
            s0[u] = mfma16(kf[u], qf[u], Z4);
            m4[u] = *(const LAS f32x4*)(LA_(tm) + 64 * u);
            SB();
            if (u < 5) qf[u + 1] = frag(qa[u + 1][0], qa[u + 1][1], mc[u + 2], 0); else m4[6] = *(const LAS f32x4*)(LA_(tm) + 64 * 6);
            SB();
            s1[u] = mfma16(kf[u + 1], qf[u], Z4);
            SB();
          }
          float mx[6], inv[6];
#pragma unroll
          for (int u = 0; u < 6; ++u) {
#pragma unroll
            for (int e = 0; e < 2; ++e) {
              const f32x4 s = e ? s1[u] : s0[u];
              lg[u][e] = __builtin_elementwise_fma(f32x2{sel(odd, s[1], s[0]), sel(odd, s[3], s[2])}, lg[u][e], mk[u ? 1 : 0][e]);
            }
            mx[u] = max2(max2(lg[u][0][0], lg[u][0][1]), max2(lg[u][1][0], lg[u][1][1]));
          }
          u32x16 vv[2];
          auto pkv = [&](int t) AI {
#pragma unroll
            for (int i = 0; i < 2; ++i) { u32x2 y = pk4(m4[t] * cv[i] + (t ? qa[t - 1][4 + i] : ex[2 + i])); PINV(y); vv[i][2 * t] = y.x; vv[i][2 * t + 1] = y.y; }
          };
          across6(mx, [](float a, float b) AI { return max2(a, b); }, [](float a) AI { return a; }, pkv);
          pkv(4); pkv(5); pkv(6);
          PINV(vv[0]); PINV(vv[1]);
          SB();
#pragma unroll
          for (int u = 0; u < 6; ++u) {
#pragma unroll
            for (int e = 0; e < 2; ++e) {
              lg[u][e] -= mx[u];
              lg[u][e] = f32x2{__builtin_amdgcn_exp2f(lg[u][e][0]), __builtin_amdgcn_exp2f(lg[u][e][1])};
            }
            const f32x2 t = lg[u][0] + lg[u][1];
            inv[u] = t[0] + t[1];
          }
          across6(inv, [](float a, float b) AI { return a + b; }, [](float a) AI { return __builtin_amdgcn_rcpf(a); },
                  [&](int q) AI { for (int e = 3 * q; e < 3 * q + 3; ++e) lg[e >> 1][e & 1] *= rk[(e >> 1) + (e & 1)]; });
          f32x4 o0[6], o1[6];
          auto sto = [&](int u) AI {
            const u32x2 l = pk4(o0[u]), h = pk4(o1[u]);
            __builtin_amdgcn_raw_buffer_store_b128(u32x4{l.x, l.y, h.x, h.y}, rs_ws, ov + u * 16384, 0, AUX_SC1);
          };
          PRIO(3);
          SFOR(6, u,
            const f32x2 a = lg[u][0] * inv[u], b = lg[u][1] * inv[u];
            const unsigned wa_ = pack2(a[0], a[1]), wb_ = pack2(b[0], b[1]);
            const u32x4 pp = {__builtin_amdgcn_perm(wa_, wa_, p0), __builtin_amdgcn_perm(wa_, wa_, p1), __builtin_amdgcn_perm(wb_, wb_, p0), __builtin_amdgcn_perm(wb_, wb_, p1)};
            if constexpr (u >= 2) sto(u - 2);
            SB();
            o0[u] = mfma16(__builtin_shufflevector(vv[0], vv[0], 2 * u, 2 * u + 1, 2 * u + 2, 2 * u + 3), pp, Z4);
            o1[u] = mfma16(__builtin_shufflevector(vv[1], vv[1], 2 * u, 2 * u + 1, 2 * u + 2, 2 * u + 3), pp, Z4);
            SB();
          );
          sto(4); sto(5);
          SB();
          };
        if (m < 2) att(std::integral_constant<int, 0>{}); else att(std::integral_constant<int, 1>{});
    LANEVARS;
    if (DF && idx < 28) { u32x4 f2[6][4]; gemm2(std::true_type{}, IC(0), f2, IC(1)); }
    DRAIN();
    if (arrive(lb, 6) == 3u) putflag(WS_OF + (rg * 8 + hp) * FS, lane);
    if (!DF && idx < 28) { u32x4 f2[6][4]; gemm2(std::true_type{}, IC(0), f2, IC(1)); }
    if (bid == EPB) {
      __builtin_amdgcn_s_sleep(64);
      poll_flags<16>((const GAS u64*)(ws + WS_UF + (m * 64 + lane) * FS), tag);
      if (arrive(lb, 8) == 3u) putflag(WS_E, lane);
    }
  }
  FINISH();
}

extern "C" void kernel_launch(void* const* d_in, const int* in_sizes, int n_in,
                              void* d_out, int out_size, void* d_ws, size_t ws_size,
                              hipStream_t stream) {
  (void)in_sizes; (void)n_in; (void)out_size; (void)ws_size;
  (void)hipFuncSetAttribute((const void*)fused_block, hipFuncAttributeMaxDynamicSharedMemorySize, LDS_BYTES);
  fused_block<<<256, 512, LDS_BYTES, stream>>>((const u16*)d_in[0], (const u16*)d_in[1], (const u16*)d_in[2],
                                               (const u16*)d_in[4], (const u16*)d_in[5], (const u16*)d_in[6],
                                               (const u16*)d_in[7], (u16*)d_out, (char*)d_ws);
}
```

```cpp
#include <hip/hip_runtime.h>
#include <stdint.h>
#include <type_traits>
#include <utility>

#define __float_as_uint(x) __builtin_bit_cast(unsigned, (float)(x))
#define __uint_as_float(x) __builtin_bit_cast(float, (unsigned)(x))
#define __all(p) (__builtin_amdgcn_ballot_w64(!(p)) == 0)
typedef unsigned short u16;
typedef unsigned long long u64;
using bfv8 = __attribute__((ext_vector_type(8))) __bf16;
using bfv2 = __attribute__((ext_vector_type(2))) __bf16;
using f32x4 = __attribute__((ext_vector_type(4))) float;
using f32x2 = __attribute__((ext_vector_type(2))) float;
using u32x2 = __attribute__((ext_vector_type(2))) unsigned int;
using u32x4 = __attribute__((ext_vector_type(4))) unsigned int;
using u32x16 = __attribute__((ext_vector_type(16))) unsigned int;
using s16x4 = __attribute__((ext_vector_type(4))) short;

#define LAS __attribute__((address_space(3)))
#define GAS __attribute__((address_space(1)))
#define AI __attribute__((always_inline))
#define SB() __builtin_amdgcn_sched_barrier(0)
#define SGB(mask, n) __builtin_amdgcn_sched_group_barrier(mask, n, 0)
#define DRAIN() asm volatile("s_waitcnt vmcnt(0)" ::: "memory")
#define WAITVM(n) asm volatile("s_waitcnt vmcnt(%0)" ::"n"(n) : "memory")
#define WAITLG() asm volatile("s_waitcnt lgkmcnt(0)" ::: "memory")
#define RLX_AGENT __ATOMIC_RELAXED, __HIP_MEMORY_SCOPE_AGENT
#define PRIO(n) __builtin_amdgcn_s_setprio(n)

constexpr int RQ1 = 2, RQ2 = 5, PLP = 0, PEP = 1, PE2 = 2, PL2 = 1, PP2 = 0, SX = 1, WST = 5, WPI = 2, WN = 7, WORD = 0, WRM = 1, NB2L = 3, IL = 1, ADLY = 0, LA0 = 6, LA1 = 6, W0 = 0, PE1 = 1, PL1 = 2, NB1 = 8, PKE = 1, NSG = 2, NE = 4, WSL = 1, TP = 20, SG2 = 0, SLP = 4, OV = 0, GDLY = 5, GDL2 = 4, W5 = 8, W6 = 12, EW = 6, EG = 2, TW = 9, LA6 = 13, AV = 14, HW = 0, HO = 0, RO = 0, DF = 0, TR = 0, TR5 = 12816, TR4 = 12816, RP = 2, MSP = 1, CH = 1, SKA = 0, CE = 2, CD = 1, PH = 0, OH = 0, GK = 1, PF1 = 0, OPK = 3, PT = 0, PZ = 0, XR = 0, PG = 0, PGE = 3, PGF = 3, CT = 3, FLP = 1, LDF = 1, FA = 0, PA = 0, HB = 1, TD = 0, TMP = 0, WT = 9, WP = 99, AO = 0, AS = 4, GS = 0, OFS = 14320928, S1 = 0, S2 = 0, S3 = 0, S4 = 2, OAUX = 16, FB = 0, FT = 2, LT = 2, LP = 1, LS = 0, C2 = 0, GE = -1, NB2E = 3;
constexpr int S_ = 2048, D_ = 448, F_ = 1792, N3_ = 1344;
constexpr int AUX_SC1 = 16;
constexpr int RSRC_W3 = 0x00020000;

constexpr int FS = 128;
constexpr int WS_E = 0, WS_HF = 4096, WS_UF = WS_HF + 128 * FS, WS_OF = WS_UF + 168 * FS;
constexpr int WS_ST = 65536;
constexpr int KS_ = 28 * 1024;
constexpr int WS_H = 131072;
constexpr int WS_U = WS_H + 64 * KS_;
constexpr int WS_O = WS_U + 56 * KS_;
constexpr int WS_END = WS_O + 132 * 16384;

constexpr int L_LATE = 65536, SS = 16384;
constexpr int L_HB = 131072;
constexpr int L_CNT = 162176;
constexpr int L_SA = 162432;
constexpr int L_DUMMY = 162560;
constexpr int LDS_BYTES = 163840;
constexpr int HB_PITCH = 928, AT_PITCH = 144;

__device__ __forceinline__ unsigned pack2(float a, float b) {
  bfv2 v; v[0] = (__bf16)a; v[1] = (__bf16)b;
  return __builtin_bit_cast(unsigned, v);
}
__device__ __forceinline__ float bfr(float f) { return __uint_as_float(pack2(f, 0.f) << 16); }
__device__ __forceinline__ float lo_f(unsigned u) { return __uint_as_float(u << 16); }
__device__ __forceinline__ float hi_f(unsigned u) { return __uint_as_float(u & 0xffff0000u); }
__device__ __forceinline__ float dot2(unsigned a, unsigned b, float c) {
  return __builtin_amdgcn_fdot2_f32_bf16(__builtin_bit_cast(bfv2, a), __builtin_bit_cast(bfv2, b), c, false);
}
__device__ __forceinline__ f32x4 mfma16(u32x4 a, u32x4 b, f32x4 c) {
  return __builtin_amdgcn_mfma_f32_16x16x32_bf16(__builtin_bit_cast(bfv8, a), __builtin_bit_cast(bfv8, b), c, 0, 0, 0);
}
__device__ __forceinline__ float gsum(float v) {
  unsigned u = __float_as_uint(v);
  auto a = __builtin_amdgcn_permlane32_swap(u, u, false, false);
  float s = __uint_as_float(a[0]) + __uint_as_float(a[1]);
  u = __float_as_uint(s);
  auto b = __builtin_amdgcn_permlane16_swap(u, u, false, false);
  return __uint_as_float(b[0]) + __uint_as_float(b[1]);
}
__device__ __forceinline__ float max2(float a, float b) { return __builtin_elementwise_maximum(a, b); }
template <bool FIRST> __device__ __forceinline__ void mfma_ip(f32x4& d, u32x4 a, u32x4 b) {
  if constexpr (FIRST) asm volatile("v_mfma_f32_16x16x32_bf16 %0, %1, %2, 0" : "=&v"(d) : "v"(a), "v"(b));
  else asm volatile("v_mfma_f32_16x16x32_bf16 %0, %1, %2, %0" : "+v"(d) : "v"(a), "v"(b));
}
__device__ __forceinline__ float gmax(float v) {
  unsigned u = __float_as_uint(v);
  auto a = __builtin_amdgcn_permlane32_swap(u, u, false, false);
  float s = max2(__uint_as_float(a[0]), __uint_as_float(a[1]));
  u = __float_as_uint(s);
  auto b = __builtin_amdgcn_permlane16_swap(u, u, false, false);
  return max2(__uint_as_float(b[0]), __uint_as_float(b[1]));
}
template <class OP, class FN, class FL> __device__ __forceinline__ void across6(float (&v)[6], OP op, FN fin, FL fill) {
  auto s32 = [](float a, float b) AI { const auto r = __builtin_amdgcn_permlane32_swap(__float_as_uint(a), __float_as_uint(b), false, false); return f32x2{__uint_as_float(r[0]), __uint_as_float(r[1])}; };
  auto s16 = [](float a, float b) AI { const auto r = __builtin_amdgcn_permlane16_swap(__float_as_uint(a), __float_as_uint(b), false, false); return f32x2{__uint_as_float(r[0]), __uint_as_float(r[1])}; };
  __builtin_amdgcn_sched_barrier(0);
  fill(0);
  __builtin_amdgcn_sched_barrier(0);
  const f32x2 a = s32(v[0], v[2]), b = s32(v[1], v[3]), d = s32(v[4], v[5]);
  float x = op(a[0], a[1]), y = op(b[0], b[1]), z = op(d[0], d[1]), z2 = z;
  asm volatile("" : "+v"(z2));
  __builtin_amdgcn_sched_barrier(0);
  fill(1);
  __builtin_amdgcn_sched_barrier(0);
  const f32x2 e = s16(x, y), f = s16(z, z2);
  float p = fin(op(e[0], e[1])), q = fin(op(f[0], f[1])), p2 = p, q2 = q;
  asm volatile("" : "+v"(p2), "+v"(q2));
  __builtin_amdgcn_sched_barrier(0);
  fill(2);
  __builtin_amdgcn_sched_barrier(0);
  const f32x2 h = s16(p, p2), k = s32(q, q2);
  float h0 = h[0], h1 = h[1];
  asm volatile("" : "+v"(h0), "+v"(h1));
  __builtin_amdgcn_sched_barrier(0);
  fill(3);
  __builtin_amdgcn_sched_barrier(0);
  const f32x2 r = s32(h[0], h0), t = s32(h[1], h1);
  v[0] = r[0]; v[2] = r[1]; v[1] = t[0]; v[3] = t[1]; v[4] = k[0]; v[5] = k[1];
}
template <int CTRL>
__device__ __forceinline__ float dpp_add(float v) {
  return v + __uint_as_float(__builtin_amdgcn_update_dpp(0u, __float_as_uint(v), CTRL, 0xf, 0xf, true));
}
__device__ __forceinline__ void dma16(__amdgpu_buffer_rsrc_t rs, int voff, int soff, int dst) {
  asm volatile("s_mov_b32 m0, %3\n\ts_nop 0\n\tbuffer_load_dwordx4 %0, %1, %2 offen lds" ::"v"(voff), "s"(rs), "s"(soff), "s"(dst) : "memory");
}
__device__ __forceinline__ void touch(__amdgpu_buffer_rsrc_t rs, int voff, int soff, int dst) {
  asm volatile("s_mov_b32 m0, %3\n\ts_nop 0\n\tbuffer_load_dword %0, %1, %2 offen lds" ::"v"(voff), "s"(rs), "s"(soff), "s"(dst) : "memory");
}
__device__ __forceinline__ void ldx4(u32x4 (&a)[4], __amdgpu_buffer_rsrc_t rs, int voff, int soff) {
  asm volatile("buffer_load_dwordx4 %0, %4, %5, %6 offen\n\tbuffer_load_dwordx4 %1, %4, %5, %6 offen offset:224\n\t"
               "buffer_load_dwordx4 %2, %4, %5, %6 offen offset:448\n\tbuffer_load_dwordx4 %3, %4, %5, %6 offen offset:672"
               : "=&v"(a[0]), "=&v"(a[1]), "=&v"(a[2]), "=&v"(a[3]) : "v"(voff), "s"(rs), "s"(soff) : "memory");
}
__device__ __forceinline__ void dma4(__amdgpu_buffer_rsrc_t rs, int v, int so, int step, int dst) {
  const int s1 = so + step - 1024, s2 = so + 2 * step - 2048, s3 = so + 3 * step - 3072;
  asm volatile("s_mov_b32 m0, %6\n\ts_nop 0\n\t"
               "buffer_load_dwordx4 %0, %1, %2 offen lds\n\t"
               "buffer_load_dwordx4 %0, %1, %3 offen offset:1024 lds\n\t"
               "buffer_load_dwordx4 %0, %1, %4 offen offset:2048 lds\n\t"
               "buffer_load_dwordx4 %0, %1, %5 offen offset:3072 lds"
               ::"v"(v), "s"(rs), "s"(so), "s"(s1), "s"(s2), "s"(s3), "s"(dst) : "memory");
}
__device__ __forceinline__ void ld4(u32x4 (&a)[4], __amdgpu_buffer_rsrc_t rs, int v, int so, int step) {
  asm volatile("buffer_load_dwordx4 %0, %4, %5, %6 offen\n\tbuffer_load_dwordx4 %1, %4, %5, %7 offen\n\t"
               "buffer_load_dwordx4 %2, %4, %5, %8 offen\n\tbuffer_load_dwordx4 %3, %4, %5, %9 offen"
               : "=&v"(a[0]), "=&v"(a[1]), "=&v"(a[2]), "=&v"(a[3])
               : "v"(v), "s"(rs), "s"(so), "s"(so + step), "s"(so + 2 * step), "s"(so + 3 * step) : "memory");
}
template <int N> __device__ __forceinline__ void wait4(u32x4 (&a)[4]) {
  asm volatile("s_waitcnt vmcnt(%4)" : "+v"(a[0]), "+v"(a[1]), "+v"(a[2]), "+v"(a[3]) : "n"(N) : "memory");
}
__device__ __forceinline__ void turn_pair(u32x4 (&a)[4], u32x4 (&b)[4]) {
  unsigned xa[8][2], xb[8][2];
  if constexpr (TD) {
#define CM4(M, D, S, T) asm volatile("s_mov_b64 vcc, %24\n\ts_nop 1\n\t" \
      "v_cndmask_b32_dpp %0, %8, %16, vcc row_ror:8 row_mask:0xf bank_mask:0xf\n\tv_cndmask_b32_dpp %1, %9, %17, vcc row_ror:8 row_mask:0xf bank_mask:0xf\n\t" \
      "v_cndmask_b32_dpp %2, %10, %18, vcc row_ror:8 row_mask:0xf bank_mask:0xf\n\tv_cndmask_b32_dpp %3, %11, %19, vcc row_ror:8 row_mask:0xf bank_mask:0xf\n\t" \
      "v_cndmask_b32_dpp %4, %12, %20, vcc row_ror:8 row_mask:0xf bank_mask:0xf\n\tv_cndmask_b32_dpp %5, %13, %21, vcc row_ror:8 row_mask:0xf bank_mask:0xf\n\t" \
      "v_cndmask_b32_dpp %6, %14, %22, vcc row_ror:8 row_mask:0xf bank_mask:0xf\n\tv_cndmask_b32_dpp %7, %15, %23, vcc row_ror:8 row_mask:0xf bank_mask:0xf" \
      : "=&v"(D[0][0]), "=&v"(D[0][1]), "=&v"(D[1][0]), "=&v"(D[1][1]), "=&v"(D[2][0]), "=&v"(D[2][1]), "=&v"(D[3][0]), "=&v"(D[3][1]) \
      : "v"(V[0][S]), "v"(V[0][S + 1]), "v"(V[1][S]), "v"(V[1][S + 1]), "v"(V[2][S]), "v"(V[2][S + 1]), "v"(V[3][S]), "v"(V[3][S + 1]), \
        "v"(V[0][T]), "v"(V[0][T + 1]), "v"(V[1][T]), "v"(V[1][T + 1]), "v"(V[2][T]), "v"(V[2][T + 1]), "v"(V[3][T]), "v"(V[3][T + 1]), "s"(M) : "vcc")
    const u64 lo = 0x00ff00ff00ff00ffull;
    { u32x4 (&V)[4] = a; CM4(lo, xa, 2, 0); }
    { u32x4 (&V)[4] = b; CM4(lo, (xa + 4), 2, 0); }
#pragma unroll
    for (int r = 0; r < 8; ++r)
#pragma unroll
      for (int e = 0; e < 2; ++e) {
        const u32x4 v = r < 4 ? a[r] : b[r - 4];
        xb[r][e] = __builtin_amdgcn_update_dpp(v[2 + e], v[e], 0x128, 0xf, 0x3, false);
      }
  } else {
#pragma unroll
  for (int r = 0; r < 8; ++r)
#pragma unroll
    for (int e = 0; e < 2; ++e) {
      const u32x4 v = r < 4 ? a[r] : b[r - 4];
      xa[r][e] = __builtin_amdgcn_update_dpp(v[e], v[2 + e], 0x128, 0xf, 0xc, false);
      xb[r][e] = __builtin_amdgcn_update_dpp(v[2 + e], v[e], 0x128, 0xf, 0x3, false);
    }
  }
#pragma unroll
  for (int j = 0; j < 4; ++j)
#pragma unroll
    for (int q = 0; q < 4; ++q) {
      a[j][q] = __builtin_amdgcn_perm(xa[2 * q + 1][j >> 1], xa[2 * q][j >> 1], (j & 1) ? 0x07060302u : 0x05040100u);
      b[j][q] = __builtin_amdgcn_perm(xb[2 * q + 1][j >> 1], xb[2 * q][j >> 1], (j & 1) ? 0x07060302u : 0x05040100u);
    }
#pragma unroll
  for (int j = 0; j < 4; ++j) asm volatile("" : "+v"(a[j]), "+v"(b[j]));
}
__device__ __forceinline__ u32x2 tr_read(const LAS char* p) {
  return __builtin_bit_cast(u32x2, __builtin_amdgcn_ds_read_tr16_b64_v4i16((LAS s16x4*)p));
}
__device__ __forceinline__ float ln_sd(float var) { return bfr(__builtin_amdgcn_sqrtf(bfr(bfr(var) + 1e-5f))); }

__device__ __forceinline__ float row_sum(float v) {
  v = dpp_add<0xB1>(v);
  v = dpp_add<0x4E>(v);
  v = dpp_add<0x141>(v);
  return dpp_add<0x140>(v);
}
__device__ __forceinline__ unsigned ln2_rows(u32x4 (&f)[4], bool act) {
  f32x2 d[16];
#pragma unroll
  for (int e = 0; e < 16; ++e) d[e] = f32x2{lo_f(f[e >> 2][e & 3]), hi_f(f[e >> 2][e & 3])};
  f32x2 t[4];
#pragma unroll
  for (int e = 0; e < 4; ++e) t[e] = (d[e] + d[4 + e]) + (d[8 + e] + d[12 + e]);
  t[0] = (t[0] + t[1]) + (t[2] + t[3]);
  const float mean = row_sum(t[0][0] + t[0][1]) * (1.0f / D_);
  const float mr = bfr(mean);
  const float mb = act ? mr : 0.f;
#pragma unroll
  for (int e = 0; e < 16; ++e) d[e] -= f32x2{mb, mb};
#pragma unroll
  for (int e = 0; e < 4; ++e) t[e] = d[e] * d[e];
#pragma unroll
  for (int e = 4; e < 16; ++e) t[e & 3] = __builtin_elementwise_fma(d[e], d[e], t[e & 3]);
  t[0] = (t[0] + t[1]) + (t[2] + t[3]);
  const float dm = mean - mr;
  const float var = fmaxf(row_sum(t[0][0] + t[0][1]) * (1.0f / D_) - dm * dm, 0.f);
  const float s1 = ln_sd(var), r1 = __builtin_amdgcn_rcpf(s1);
  const float r2 = __builtin_amdgcn_rcpf(ln_sd(var * r1 * r1));
  const float a = r1 * r2, cc = -bfr(dm * r1) * r2;
#pragma unroll
  for (int e = 0; e < 16; ++e) {
    const f32x2 v = __builtin_elementwise_fma(d[e], f32x2{a, a}, f32x2{cc, cc});
    f[e >> 2][e & 3] = pack2(v[0], v[1]);
  }
  return (__float_as_uint(mr) >> 16) | __float_as_uint(s1);
}
__device__ __forceinline__ float solo(float v) { asm("" : "+v"(v)); return v; }
__device__ __forceinline__ float vadd(float a, float b) { return solo(a + b); }
__device__ __forceinline__ float vmul(float a, float b) { return solo(a * b); }
__device__ __forceinline__ f32x4 add4(f32x4 a, f32x4 b) { return f32x4{vadd(a[0], b[0]), vadd(a[1], b[1]), vadd(a[2], b[2]), vadd(a[3], b[3])}; }

constexpr unsigned SPIN_CAP = 1u << 18;
template <int SL>
__device__ __forceinline__ void poll_flags(const GAS u64* p, u64 tag) {
#pragma unroll 1
  for (unsigned spins = 0; spins < SPIN_CAP; ++spins) {
    const u64 v = __hip_atomic_load(p, RLX_AGENT);
    if (__all(v == tag)) break;
    __builtin_amdgcn_s_sleep(SL);
  }
  __builtin_amdgcn_fence(__ATOMIC_ACQUIRE, "wavefront");
}

__device__ __forceinline__ unsigned arrive(int lb, int i) {
  unsigned r;
  asm volatile("s_waitcnt lgkmcnt(0)\n\ts_mov_b64 exec, 1\n\tds_add_rtn_u32 %0, %1, %2\n\ts_mov_b64 exec, -1\n\ts_waitcnt lgkmcnt(0)"
               : "=&v"(r) : "v"(lb + L_CNT + 4 * i), "v"(1u) : "memory");
  return __builtin_amdgcn_readfirstlane(r);
}
__device__ __forceinline__ void sig(int lb, int i) {
  asm volatile("s_waitcnt lgkmcnt(0)\n\ts_mov_b64 exec, 1\n\tds_add_u32 %0, %1\n\ts_mov_b64 exec, -1" ::"v"(lb + L_CNT + 4 * i), "v"(1u) : "memory");
}
template <int I> __device__ __forceinline__ void post(int sa, unsigned one) {
  asm volatile("ds_add_u32 %0, %1 offset:%2" ::"v"(sa), "v"(one), "n"(4 * I) : "memory");
}
__device__ __forceinline__ unsigned look(LAS char* L, int i) {
  return __hip_atomic_load((LAS unsigned*)(L + L_CNT) + i, __ATOMIC_RELAXED, __HIP_MEMORY_SCOPE_WORKGROUP);
}
__device__ __forceinline__ void check(LAS char* L, unsigned v, int i, unsigned target) {
  unsigned t, n, a;
  asm volatile("s_nop 0\n\tv_readfirstlane_b32 %0, %3\n\ts_cmp_lt_u32 %0, %5\n\ts_cbranch_scc1 .Lcw%=\n.Lcb%=:\n\t.subsection 1\n"
               ".Lcw%=:\n\ts_mov_b32 %1, 0x400000\n\tv_mov_b32 %2, %4\n"
               ".Lcl%=:\n\tds_read_b32 %3, %2\n\ts_waitcnt lgkmcnt(0)\n\tv_readfirstlane_b32 %0, %3\n\ts_cmp_ge_u32 %0, %5\n\t"
               "s_cbranch_scc1 .Lcb%=\n\ts_sleep 0\n\ts_sub_u32 %1, %1, 1\n\ts_cbranch_scc0 .Lcl%=\n\ts_branch .Lcb%=\n\t.subsection 0"
               : "=&s"(t), "=&s"(n), "=&v"(a), "+v"(v) : "s"((int)(unsigned)(size_t)L + L_CNT + 4 * i), "n"(target) : "memory", "scc");
}
template <int SL = 0> __device__ __forceinline__ void await(LAS char* L, int i, unsigned target) {
  unsigned t, n, v;
  asm volatile("s_mov_b32 %1, 0x400000\n.Lal%=:\n\tds_read_b32 %2, %3\n\ts_waitcnt lgkmcnt(0)\n\tv_readfirstlane_b32 %0, %2\n\ts_cmp_ge_u32 %0, %4\n\t"
               "s_cbranch_scc1 .Lab%=\n\ts_sleep %5\n\ts_sub_u32 %1, %1, 1\n\ts_cbranch_scc0 .Lal%=\n.Lab%=:"
               : "=&s"(t), "=&s"(n), "=&v"(v) : "v"((int)(unsigned)(size_t)L + L_CNT + 4 * i), "n"(target), "n"(SL) : "memory", "scc");
}
__device__ __forceinline__ void await_at(int a, unsigned target) {
  unsigned t, n, v;
  asm volatile("s_mov_b32 %1, 0x400000\n.Lal%=:\n\tds_read_b32 %2, %3\n\ts_waitcnt lgkmcnt(0)\n\tv_readfirstlane_b32 %0, %2\n\ts_cmp_ge_u32 %0, %4\n\t"
               "s_cbranch_scc1 .Lab%=\n\ts_sleep 0\n\ts_sub_u32 %1, %1, 1\n\ts_cbranch_scc0 .Lal%=\n.Lab%=:"
               : "=&s"(t), "=&s"(n), "=&v"(v) : "v"(a), "n"(target) : "memory", "scc");
}
__device__ __forceinline__ void take8(f32x4 (&t)[4][4], int a, int ca) {
  unsigned s, n, v;
  asm volatile("s_mov_b32 %9, 0x400000\n.Ltl%=:\n\tds_read_b32 %10, %12\n\t"
               "ds_read_b128 %0, %11\n\tds_read_b128 %1, %11 offset:256\n\tds_read_b128 %2, %11 offset:4096\n\tds_read_b128 %3, %11 offset:4352\n\t"
               "ds_read_b128 %4, %11 offset:8192\n\tds_read_b128 %5, %11 offset:8448\n\tds_read_b128 %6, %11 offset:12288\n\tds_read_b128 %7, %11 offset:12544\n\t"
               "s_waitcnt lgkmcnt(8)\n\tv_readfirstlane_b32 %8, %10\n\ts_cmp_ge_u32 %8, 8\n\ts_cbranch_scc1 .Ltb%=\n\t"
               "s_waitcnt lgkmcnt(4)\n\ts_sub_u32 %9, %9, 1\n\ts_cbranch_scc0 .Ltl%=\n.Ltb%=:\n\ts_waitcnt lgkmcnt(0)"
               : "=&v"(t[0][0]), "=&v"(t[0][1]), "=&v"(t[1][0]), "=&v"(t[1][1]), "=&v"(t[2][0]), "=&v"(t[2][1]), "=&v"(t[3][0]), "=&v"(t[3][1]), "=&s"(s), "=&s"(n), "=&v"(v)
               : "v"(a), "v"(ca) : "memory", "scc");
}
__device__ __forceinline__ void gbar4(LAS char* L, int lb, int i) { sig(lb, i); await(L, i, 4u); }

#define BAR() __builtin_amdgcn_s_barrier()
#define LANEVARS                                                                                         \
  int lane;                                                                                              \
  asm volatile("v_mbcnt_lo_u32_b32 %0, -1, 0\n\tv_mbcnt_hi_u32_b32 %0, -1, %0" : "=v"(lane));             \
  const int c = lane & 15, g = lane >> 4, q4 = c >> 2, p4 = c & 3, rr = lane >> 3;                       \
  (void)c; (void)g; (void)q4; (void)p4; (void)rr
constexpr f32x4 Z4 = {0.f, 0.f, 0.f, 0.f};
constexpr int OOB = 0x40000000;
template <int A, int B> __device__ __forceinline__ void pin(f32x4 (&a)[A][B]) {
#pragma unroll
  for (int i = 0; i < A; ++i)
#pragma unroll
    for (int t = 0; t < B; ++t) asm volatile("" : "+v"(a[i][t]));
}
template <class T> __device__ __forceinline__ T sel(bool c, T a, T b) { return __builtin_unpredictable(c) ? a : b; }
__device__ __forceinline__ int trbase(int i, int g, int q4, int p4) {
  return (4 * g + q4) * 128 + (((2 * i + (p4 >> 1)) ^ (2 * ((q4 >> 1) | ((g & 1) << 1)))) << 4) + 8 * (p4 & 1);
}
__device__ __forceinline__ void slot_frags(const LAS char* L, const int (&tb)[4], int so, u32x4 (&a)[4]) {
#pragma unroll
  for (int i = 0; i < 4; ++i) {
    const u32x2 lo = tr_read(L + tb[i] + so), hi = tr_read(L + tb[i] + so + 2048);
    a[i] = u32x4{lo.x, lo.y, hi.x, hi.y};
  }
}
__device__ __forceinline__ u64 peek(__amdgpu_buffer_rsrc_t rs, int off) {
  u64 v;
  asm volatile("buffer_load_dwordx2 %0, %1, %2, 0 offen sc1" : "=v"(v) : "v"(off), "s"(rs) : "memory");
  return v;
}
__device__ __forceinline__ void settle(__amdgpu_buffer_rsrc_t rs, int off, u64 first, u64 tag) {
  unsigned n;
  asm volatile("v_cmp_ne_u64 vcc, %4, %1\n\ts_cbranch_vccz .Lsb%=\n\ts_mov_b32 %0, 0x40000\n"
               ".Lsl%=:\n\tbuffer_load_dwordx2 %1, %2, %3, 0 offen sc1\n\ts_waitcnt vmcnt(0)\n\tv_cmp_ne_u64 vcc, %4, %1\n\t"
               "s_cbranch_vccz .Lsb%=\n\ts_sleep %5\n\ts_sub_u32 %0, %0, 1\n\ts_cbranch_scc0 .Lsl%=\n.Lsb%=:"
               : "=&s"(n), "+v"(first) : "v"(off), "s"(rs), "s"(tag), "n"(SLP) : "memory", "scc", "vcc");
}
template <class F, int... I> __device__ __forceinline__ void static_for(F f, std::integer_sequence<int, I...>) { (f(std::integral_constant<int, I>{}), ...); }
#define SFOR(n, var, ...) static_for([&](auto var##_c) AI { constexpr int var = decltype(var##_c)::value; __VA_ARGS__ }, std::make_integer_sequence<int, n>{})
template <int OFF> __device__ __forceinline__ void ld1_a(u32x4& v, __amdgpu_buffer_rsrc_t rs, int vo, int so) {
  asm volatile("buffer_load_dwordx4 %0, %1, %2, %3 offen offset:%4 sc1" : "=v"(v) : "v"(vo), "s"(rs), "s"(so), "n"(OFF) : "memory");
}
template <int N> __device__ __forceinline__ void wait1(u32x4& a) { asm volatile("s_waitcnt vmcnt(%1)" : "+v"(a) : "n"(N) : "memory"); }
template <int N> __device__ __forceinline__ void wait2(u32x4& a, u32x4& b) { asm volatile("s_waitcnt vmcnt(%2)" : "+v"(a), "+v"(b) : "n"(N) : "memory"); }

template <int NK, int NR, int NT, bool G2, int KX, int NB, int GR, class LD, class PUT, class HK>
__device__ __forceinline__ void kloop(LAS char* L, const int (&tb)[4], int jit0, u32x4 (&fr)[NR][4], u32x4 (&hv)[NB][NT],
                                      f32x4 (&acc)[4][NT], LD ld, PUT put, HK hook) {
  constexpr int NL = NK * NT;
  auto mm = [&](auto kc, auto ic, auto tc) AI {
    constexpr int k = decltype(kc)::value, i = decltype(ic)::value, t = decltype(tc)::value;
    f32x4& a = k >= KX ? acc[t & 3][i] : acc[i][t];
    const f32x4 cz = k == 0 ? Z4 : a;
    a = (G2 && k < KX) ? mfma16(hv[k % NB][t], fr[k % NR][i], cz) : mfma16(fr[k % NR][i], hv[k % NB][t], cz);
    asm volatile("" : "+v"(a));
  };
  if constexpr (C2) {
    constexpr int NM = KX < NK ? KX : NK, NR_ = NB * NT;
    SFOR(NM / 2, q,
      u32x2 lo;
      constexpr bool LAST = KX >= NK && q == NM / 2 - 1;
      SFOR(NT, t,
        constexpr int n = 8 * q + 2 * t, is = NR_ + n < NL ? NR_ + n : NL;
        wait2<is - n - 2>(hv[(n % NR_) / NT][n % NT], hv[((n + 1) % NR_) / NT][(n + 1) % NT]);
        if constexpr (t == 0) hook(std::integral_constant<int, 2 * q>{});
        SFOR(8, e,
          constexpr int k = 2 * q + (e & 1), i = e >> 1, j = n + (e & 1);
          acc[i][t] = mfma16(hv[(j % NR_) / NT][j % NT], fr[k % NR][i], k == 0 ? Z4 : acc[i][t]);
          asm volatile("" : "+v"(acc[i][t]));
          constexpr int y = (8 * t + e) >> 1, kr = 2 * q - 2 + (y >> 3);
          if constexpr (q >= 1 && (e & 1) && kr + NR < NK) {
            const LAS char* p = L + tb[(y & 7) >> 1] + ((jit0 + kr) & 3) * SS;
            if constexpr (y & 1) { const u32x2 hi = tr_read(p + 2048); fr[kr][(y & 7) >> 1] = u32x4{lo.x, lo.y, hi.x, hi.y}; }
            else lo = tr_read(p);
          }
          constexpr int d = t * 4 + i - 2;
          if constexpr (LAST && (e & 1) && d >= 0) put(std::integral_constant<int, d & 3>{}, std::integral_constant<int, d / 4>{});
          SB();
        );
        if constexpr (NR_ + n < NL) { ld(std::integral_constant<int, (NR_ + n) / NT>{}, std::integral_constant<int, (NR_ + n) % NT>{}); ld(std::integral_constant<int, (NR_ + n + 1) / NT>{}, std::integral_constant<int, (NR_ + n + 1) % NT>{}); }
        SB();
      );
    );
    if constexpr (KX >= NK) {
      put(std::integral_constant<int, 2>{}, std::integral_constant<int, NT - 1>{});
      put(std::integral_constant<int, 3>{}, std::integral_constant<int, NT - 1>{});
      return;
    }
  }
  constexpr bool COL = CE && KX < NK;
  SFOR(C2 ? 0 : COL ? KX : NK - 2, k,
    u32x2 lo;
    SFOR(NT, t,
      constexpr int n = k * NT + t, i0 = k < GR ? (NB - 1) * NT + n : k == GR ? (NB - 1) * NT + n + t : NB * NT + n, is = i0 < NL ? i0 : NL;
      wait1<is - n - 1>(hv[k % NB][t]);
      if constexpr (t == 0) hook(std::integral_constant<int, k>{});
      SFOR(4, i,
        mm(k_c, i_c, t_c);
        constexpr int e = t * 4 + i;
        if constexpr (k >= 1 && k - 1 + NR < NK && e < 8) {
          const LAS char* q = L + tb[e >> 1] + ((jit0 + k - 1) & 3) * SS;
          if constexpr (e & 1) { const u32x2 hi = tr_read(q + 2048); fr[k - 1][e >> 1] = u32x4{lo.x, lo.y, hi.x, hi.y}; }
          else lo = tr_read(q);
        }
        SB();
      );
      if constexpr (i0 < NL) ld(std::integral_constant<int, i0 / NT>{}, std::integral_constant<int, i0 % NT>{});
      if constexpr (k == GR && i0 + 1 < NL) ld(std::integral_constant<int, (i0 + 1) / NT>{}, std::integral_constant<int, (i0 + 1) % NT>{});
      SB();
    );
  );
  if constexpr (COL) {
    SFOR(NK - KX, t,
      constexpr int k = KX + t, is = (k + NB < NK ? k + NB : NK) - k - 1;
      wait4<4 * is>(hv[k % NB]);
      hook(std::integral_constant<int, k>{});
      SFOR(16, e,
        constexpr int i = e >> 2, h = e & 3, d = 4 * t + i - CD;
        acc[t][i] = mfma16(fr[(KX + h) % NR][i], hv[k % NB][h], acc[t][i]);
        asm volatile("" : "+v"(acc[t][i]));
        if constexpr (h == 3 && d >= 0) put(std::integral_constant<int, d & 3>{}, std::integral_constant<int, d / 4>{});
        SB();
      );
      if constexpr (k + NB < NK) { SFOR(NT, h, ld(std::integral_constant<int, k + NB>{}, h_c);); }
      SB();
    );
    SFOR(CD, e, put(std::integral_constant<int, 4 - CD + e>{}, std::integral_constant<int, NT - 1>{}););
    return;
  }
  SFOR(NT, t,
    wait2<NT - 1 - t>(hv[(NK - 2) % NB][t], hv[(NK - 1) % NB][t]);
    if constexpr (t == 0) hook(std::integral_constant<int, NK - 2>{});
    SFOR(8, e,
      mm(std::integral_constant<int, NK - 2 + (e & 1)>{}, std::integral_constant<int, (e >> 1)>{}, t_c);
      constexpr int d = t * 4 + (e >> 1) - 2;
      if constexpr ((e & 1) && d >= 0) put(std::integral_constant<int, d & 3>{}, std::integral_constant<int, d / 4>{});
      SB();
    );
  );
  put(std::integral_constant<int, 2>{}, std::integral_constant<int, NT - 1>{});
  put(std::integral_constant<int, 3>{}, std::integral_constant<int, NT - 1>{});
}
template <int N> __device__ __forceinline__ void gelu_pack(const f32x4 (&v)[N], unsigned (&pr)[2 * N]) {
  f32x4 z[N];
#pragma unroll
  for (int e = 0; e < N; ++e) z[e] = v[e] * (v[e] * v[e] * -0.1029432395800235f - 2.302208198144325f);
  SB();
#pragma unroll
  for (int e = 0; e < 4 * N; ++e) z[e >> 2][e & 3] = __builtin_amdgcn_exp2f(z[e >> 2][e & 3]);
  SB();
#pragma unroll
  for (int e = 0; e < N; ++e) z[e] += 1.0f;
#pragma unroll
  for (int e = 0; e < 4 * N; ++e) z[e >> 2][e & 3] = __builtin_amdgcn_rcpf(z[e >> 2][e & 3]);
  SB();
#pragma unroll
  for (int e = 0; e < N; ++e) { z[e] *= v[e]; pr[2 * e] = pack2(z[e][0], z[e][1]); pr[2 * e + 1] = pack2(z[e][2], z[e][3]); }
}
__device__ __forceinline__ float fold32(float a, float b) {
  const auto s = __builtin_amdgcn_permlane32_swap(__float_as_uint(a), __float_as_uint(b), false, false);
  return vadd(__uint_as_float(s[0]), __uint_as_float(s[1]));
}
#define LA_(a) ((LAS char*)(size_t)(unsigned)(a))
#define PINV(v) asm volatile("" : "+v"(v))
#define IC(n) std::integral_constant<int, (n)>{}
constexpr int g1buf(int nt, int t) { return ((t + 8 - nt) & 3) * SS; }
constexpr int L_B1 = 162816;
constexpr int A_W = 26624, A_WS = 24576, A_X1 = 100352, A_XS = 26624;
constexpr int A_MEAN = 153600, A_RSTD = A_MEAN + 1024, A_CS = A_RSTD + 1024;
#define FINISH() __builtin_amdgcn_endpgm()
__global__ __launch_bounds__(512, 2) void fused_block(const u16*, const u16*, const u16*, const u16*, const u16*, const u16*,
                                                      const u16*, u16*, char*) {
  extern __shared__ __attribute__((aligned(16))) char smem[];
  LAS char* L = (LAS char*)smem;
  const int lb = (int)(unsigned)(size_t)L;
  const auto kp = __builtin_amdgcn_kernarg_segment_ptr();
  u32x16 ka; u32x2 kb;
  asm volatile("s_load_dwordx16 %0, %2, 0x0\n\ts_load_dwordx2 %1, %2, 0x40" : "=&s"(ka), "=&s"(kb) : "s"(kp));
  SB();
  const int w = __builtin_amdgcn_readfirstlane((int)threadIdx.x >> 6);
  const int bid = blockIdx.x, xcd = bid & 7, idz = bid >> 3;
  const int m = w & 3;
#define ARGP(i) ((const u16*)(((u64)ka[2 * (i) + 1] << 32) | ka[2 * (i)]))
#define ARGWAIT() asm volatile("s_waitcnt lgkmcnt(0)" : "+s"(ka), "+s"(kb) : "s"(kp) : "memory")
  u64 tag;
  const int un = xcd * 16 + idz, row0 = 32 * (un >> 1) + 8 * (un & 1);
  const bool isln = idz < 16;
  u32x4 hx[4];
  u64 e1;
  auto lnask = [&]() AI {
    int lv = sel(((int)threadIdx.x & 15) < 14, (((int)threadIdx.x >> 4) & 3) * (D_ * 2) + ((int)threadIdx.x & 15) * 16, OOB);
    int ls = (row0 + 4 * (m & 1) + 8 * (m & 2)) * (D_ * 2), z = 0;
    asm volatile("" : "+v"(lv), "+v"(z));
    asm volatile("" : "+s"(ls));
    ARGWAIT();
    ldx4(hx, __builtin_amdgcn_make_buffer_rsrc((void*)ARGP(0), 0, S_ * D_ * 2, RSRC_W3), lv, ls);
    e1 = peek(__builtin_amdgcn_make_buffer_rsrc((void*)(((u64)kb[1] << 32) | kb[0]), 0, WS_END, RSRC_W3), z);
    BAR();
  };
  if (isln && w >= 4) {
    if (LP) PRIO(3);
    if (!LS) lnask();
  } else {
    u64 pc;
    unsigned co = sel(isln, 0, (((idz - 16) * 8 + w) * 8 + ((int)threadIdx.x & 7)) * 128);
    asm volatile(".Lfb_start:\n\ts_getpc_b64 %0\n\ts_mov_b32 m0, %2\n\tv_min_u32 %1, .Lfb_end-.Lfb_start-8, %1\n\t"
                 "global_load_lds_dword %1, %0\n\t.subsection 2\n.Lfb_end:\n\ts_endpgm\n\t.subsection 0" : "=&s"(pc), "+v"(co) : "s"(lb + L_DUMMY) : "memory");
    *((LAS unsigned*)(L + L_CNT) + ((int)threadIdx.x & 31)) = 0u;
  }
  SB();
  auto gettag = [&]() AI {
    const u32x2 tv = *(const LAS u32x2*)(L + L_CNT + 128);
    tag = ((u64)(unsigned)__builtin_amdgcn_readfirstlane((int)tv.y) << 32) | (unsigned)__builtin_amdgcn_readfirstlane((int)tv.x);
  };

  const int ng = 24 - 6 * (xcd & 1);
  const int k4 = idz + ng - 32, k5 = idz - 16 - 4 * (xcd & 1);
  const bool isg = k4 >= 0;
  const int lp = sel(k5 < 0, ((0x24 - 3 * ((8 >> xcd) & 1) + 0xc0) >> (k4 & 6)) & 3, k5 >> 2);
  int ft = 7 * (xcd >> 1) + 4 * (xcd & 1) + lp, dg = sel(k5 < 0, 4 + (idz & 1), idz & 3);
  int idx = sel(isg, 6 * lp + dg, ng + idz);
  asm volatile("" : "+s"(idx));
  int off1 = (OFS >> (4 * dg)) & 15;
  const int q = 22 * (xcd >> 2) + 8 * (xcd & 1) + idx - ng;
  int hp = 2 * (xcd & 2) + (q & 3), rg = q >> 2;
  int rotq = RQ1 * (q & 3) + RQ2 * rg + 70;
  rotq -= 7 * ((rotq * 147) >> 10);
  constexpr int EPB = 56;
  auto k1 = [&](int j) AI { return (j + off1) & 15; };
  int reg = (w >> 2) * L_LATE + m * 4096;
  int fv, fw, fs, fs1, role = (isg ? 0 : 2) + (w >> 2);
  {
    const int l = (int)threadIdx.x & 63, rr = l >> 3, ch = l & 7;
    const int v1 = ((m * 512 + rr) * F_ + ft * 64) * 2 + (ch ^ (2 * ((rr >> 1) & 3))) * 16;
    const int cq = ch ^ (2 * ((rr >> 1) & 1)) ^ (4 * (m & 1));
    const int vq = sel(cq < 7, (8 * m + rr) * (N3_ * 2) + hp * 112 + cq * 16, OOB);
    const int va = (192 * rg + 16 + 8 * m + rr) * (D_ * 2) + (ch ^ (4 * (m & 1) + (rr >> 1))) * 16;
    fv = sel(isg, v1, vq);
    fw = va;
    fs = sel(isg, k1(0) * 32 * (F_ * 2), rotq * (64 * N3_ * 2));
    fs1 = sel(isg, k1(1) * 32 * (F_ * 2), rotq * 128);
  }
  asm volatile("" : "+v"(fv), "+v"(fw));
  asm volatile("" : "+s"(fs), "+s"(fs1), "+s"(ft), "+s"(dg), "+s"(reg), "+s"(off1), "+s"(rotq), "+s"(role), "+s"(hp), "+s"(rg));
  const int r0 = 192 * rg;
  if (LS && isln && w >= 4) lnask();
  ARGWAIT();
  if (!HB && !(isln && w >= 4)) BAR();
  const u16 *x = ARGP(0), *wqkv = ARGP(1), *wo = ARGP(2), *w1 = ARGP(3), *b1 = ARGP(4), *w2 = ARGP(5), *b2 = ARGP(6);
  u16* out = (u16*)ARGP(7);
  char* ws = (char*)(((u64)kb[1] << 32) | kb[0]);
  const auto rs_x = __builtin_amdgcn_make_buffer_rsrc((void*)x, 0, S_ * D_ * 2, RSRC_W3);
  const auto rs_ws = __builtin_amdgcn_make_buffer_rsrc((void*)ws, 0, WS_END, RSRC_W3);
  const auto rs_w1 = __builtin_amdgcn_make_buffer_rsrc((void*)w1, 0, S_ * F_ * 2, RSRC_W3);
  const auto rs_w2 = __builtin_amdgcn_make_buffer_rsrc((void*)w2, 0, F_ * S_ * 2, RSRC_W3);
  auto putflag = [&](int off, int lane) AI {
    __builtin_amdgcn_raw_buffer_store_b128(u32x4{(unsigned)tag, (unsigned)(tag >> 32), (unsigned)tag, (unsigned)(tag >> 32)}, rs_ws,
                                           sel(lane < 8, off + lane * 16, OOB), 0, AUX_SC1);
  };
  auto mktag = [&](u64 e0) AI {
    e0 = e0 * 0x9E3779B97F4A7C15ull + 0x632BE59BD9B4E019ull;
    *(LAS u64*)(L + L_CNT + 128) = e0;
    tag = ((u64)(unsigned)__builtin_amdgcn_readfirstlane((int)(e0 >> 32)) << 32) | (unsigned)__builtin_amdgcn_readfirstlane((int)e0);
  };
  const auto rs_wq = __builtin_amdgcn_make_buffer_rsrc((void*)wqkv, 0, D_ * N3_ * 2, RSRC_W3);
  auto piece = [&](bool late, int fv, int sg, int so, int e) AI {
    if (late) {
      if (e < 6 || m < 2) dma16(rs_x, e ? fv : sel(fv >= 32 * D_ * 2, fv - 32 * D_ * 2, OOB), so + (e ? e - 1 : 0) * 32 * (D_ * 2), lb + (sg ? A_X1 + (sg - 1) * A_XS : 0) + (m + 4 * e) * 1024);
    } else {
      if (e < 6) dma16(rs_wq, fv, so + (e / 3) * 32 * (N3_ * 2) + (e % 3) * 896, lb + A_W + sg * A_WS + e * 4096 + m * 1024);
    }
  };
  if (HB) {
    if (role == 0) {
      if (isln) { BAR(); __builtin_amdgcn_s_sleep(GDL2); }
      if (GDLY) __builtin_amdgcn_s_sleep(GDLY);
      dma4(rs_w1, fv, fs, 8 * F_ * 2, lb + reg);
      dma4(rs_w1, fv, fs1, 8 * F_ * 2, lb + reg + SS);
    }
    asm volatile("" : "+s"(role));
    if (!isln || role == 2) BAR();
  }
  if (!HB && role == 0) {
    if (GDLY) __builtin_amdgcn_s_sleep(GDLY);
    if (GDL2 && isln) __builtin_amdgcn_s_sleep(GDL2);
    dma4(rs_w1, fv, fs, 8 * F_ * 2, lb + reg);
    dma4(rs_w1, fv, fs1, 8 * F_ * 2, lb + reg + SS);
  } else if (role == 2) {
    if (ADLY) __builtin_amdgcn_s_sleep(ADLY);
#pragma unroll
    for (int e = 0; e < 13; ++e) {
      if (e == AS) await(L, 7, 4u);
      const int o = AO ? (e + 6) % 13 : e;
      if (o < 6) piece(false, fv, 2, fs, o); else piece(true, fw, 2, fs1, o - 6);
    }
  }
  asm volatile("" : "+s"(role));
  if (!(role & 1)) {
  } else if (isln) {
    const int hbase = sel(isg, L_HB, A_W + A_WS);
    PRIO(3);
    LANEVARS;
    const bool act = c < 14;
    int hb = sel(act, hbase + (4 * m + g) * HB_PITCH + c * 16, L_DUMMY);
    int tp = hbase + (4 * (g & 1) + q4) * HB_PITCH + (16 * (g >> 1) + 4 * p4) * 2 + m * 64;
    int ho = WS_H + (un >> 1) * KS_ + (2 * m + (g >> 1)) * 1024 + (2 * (un & 1) + (g & 1)) * 256 + c * 16;
    int h3 = sel(m < 2, ho + 3 * 8192, sel((lane < 16) & (m == 3), WS_ST + (row0 + (c & 7) + 2 * (c & 8)) * 16, OOB));
    int sn = sel(lane == 0, lb + L_CNT, OOB), sw = lb + L_SA + (4 * m + g) * 4, sr = lb + L_SA + c * 4, cn = lb + L_CNT, h1 = ho + 8192, h2 = ho + 16384;
    unsigned one = 1u;
    hb += lb; tp += lb;
    asm volatile("" : "+v"(hb), "+v"(tp), "+v"(ho), "+v"(h3), "+v"(sn), "+v"(one), "+v"(sw), "+v"(sr), "+v"(cn), "+v"(h1), "+v"(h2));
    asm volatile("s_waitcnt vmcnt(1)" : "+v"(hx[0]), "+v"(hx[1]), "+v"(hx[2]), "+v"(hx[3])::"memory");
    const unsigned sa = ln2_rows(hx, act);
#pragma unroll
    for (int e = 0; e < 4; ++e) *(LAS u32x4*)(LA_(hb) + e * 224) = hx[e];
    *(LAS unsigned*)LA_(sw) = sa;
    SB();
    post<0>(sn, one);
    if (LT == 0) { asm volatile("s_waitcnt vmcnt(0)" : "+v"(e1)::"memory"); mktag(e1); }
    await_at(cn, 4u);
    u32x4 hs[4];
#pragma unroll
    for (int u = 0; u < 4; ++u) {
      const u32x2 a0 = tr_read(LA_(tp) + u * 256), a1 = tr_read(LA_(tp) + u * 256 + 8 * HB_PITCH);
      hs[u] = u32x4{a0.x, a0.y, a1.x, a1.y};
      SB();
    }
    const unsigned sb = *(const LAS unsigned*)LA_(sr);
    SB();
    if (LT == 1) { asm volatile("s_waitcnt vmcnt(0)" : "+v"(e1)::"memory"); mktag(e1); SB(); }
#pragma unroll
    for (int u = 0; u < 3; ++u) __builtin_amdgcn_raw_buffer_store_b128(hs[u], rs_ws, u == 0 ? ho : u == 1 ? h1 : h2, 0, AUX_SC1);
    SB();
    if (LT == 2) { asm volatile("s_waitcnt vmcnt(3)" : "+v"(e1)::"memory"); mktag(e1); }
    if (m == 3) hs[3] = u32x4{sb, (unsigned)tag, sb, (unsigned)(tag >> 32)};
    __builtin_amdgcn_raw_buffer_store_b128(hs[3], rs_ws, h3, 0, AUX_SC1);
    SB();
    post<7>(sn, one);
    if (!LDF || isg) {
      DRAIN();
      if (arrive(lb, 1) == 3u) putflag(WS_HF + un * FS, lane);
    }
  } else {
    u64 e0 = peek(rs_ws, WS_E);
    asm volatile("s_waitcnt vmcnt(0)" : "+v"(e0)::"memory");
    mktag(e0);
    sig(lb, 7);
  }
  asm volatile("" : "+s"(role));
  int idy = idx;
  asm volatile("" : "+s"(idy));
  constexpr u64 TMS[5][3] = {{0x59d28518a320820ull, 0xbdab49ca2f839acull, 0x18820ffbbcdeb38ull}, {0x7358a4a0c520820ull, 0xbcdeb38ed5a4a30ull, 0xffbbcd671bull}, {0x7358a4a0c520820ull, 0x9e1f65d4e3a4a30ull, 0xffbbcdeaf9ull}, {0x1b58a4a0c520820ull, 0xc4ed57d1679460eull, 0xffbbcdeaf9ull}, {0x7358a4a0c520820ull, 0xcbf0bb1c75a4a30ull, 0xffbbcddf53ull}};
  const u64 tm = idy < 12 ? TMS[TMP][0] : idy < 24 ? TMS[TMP][1] : TMS[TMP][2];
  const int tile = sel(xcd & 1, (int)(tm >> (5 * (idy - 12 * ((idy * 43) >> 9)))) & 31, idy);
  const int st = 4 * (xcd ^ SX) + (tile & 3), dt = tile >> 2;
  const int rotl = dt % 6;
  auto pos2 = [&](bool late, int p) AI { const int v = p + rotl; return 14 * m + (late ? 8 + (v >= 6 ? v - 6 : v) : (p + 2 * dt) & 7); };
  constexpr int SP = HW ? F_ : S_;
  const int stx = HW ? 7 * (xcd >> 1) + 4 * (xcd & 1) + min(tile & 3, 3 - (xcd & 1)) : st;
  auto ldw2 = [&](u32x4 (&a)[4], int vr, int p) AI { ld4(a, HW ? rs_w1 : rs_w2, vr, (pos2(false, p & 6) * 32 + 16 * (p & 1)) * (SP * 2), SP * 2); };
  auto w2lane = [&](int lane) AI { return ((32 * ((lane >> 3) & 1) + 4 * (lane >> 4)) * SP + stx * 64 + 8 * (lane & 7)) * 2; };
  auto gemm2 = [&](auto latec, auto nec, auto& f2, auto idc) AI {
    constexpr bool LATE = decltype(latec)::value;
    constexpr int NR2 = LATE ? 6 : 8, NK2 = LATE ? 10 : 8, N0 = decltype(nec)::value & 15, T0 = decltype(nec)::value >> 4;
    {
      constexpr int NB2 = LATE ? NB2L : NB2E;
      u32x4 uv[NB2][4];
      f32x4 acc[4][4];
      {
        LANEVARS;
        PRIO(PP2);
        const int w2E = (rr * SP + stx * 64) * 2 + ((lane & 7) ^ (2 * ((rr >> 1) & 3))) * 16;
        int tb[4];
#pragma unroll
        for (int i = 0; i < 4; ++i) tb[i] = reg + trbase(i, g, q4, p4);
        const int v0 = lane * 16;
        const int ta = 4 * dt + 3 * ((lane >> 3) & 1), dgp = sel(ta < 20, (ta * 13) >> 6, (ta >> 2) - 1);
        const int fo = WS_UF + ((7 * m + (LATE ? 4 : 0) + min(lane & 7, LATE ? 2 : 3)) * 6 + dgp) * FS;
        const int oo = WS_OF + (((st * 43) >> 7) * 8 + 2 * m + (lane & 1)) * FS;
        auto fill = [&](int p, int dst) AI { dma4(HW ? rs_w1 : rs_w2, w2E, pos2(true, p) * 32 * (SP * 2), 8 * SP * 2, lb + dst); };
        constexpr int XS = L_HB - L_LATE;
        u64 f0;
        const auto rs_wo = __builtin_amdgcn_make_buffer_rsrc((void*)wo, 0, D_ * D_ * 2, RSRC_W3);
        const int woE = (rr * D_ + dt * 64) * 2 + ((lane & 7) ^ (2 * ((rr >> 1) & 3))) * 16;
        auto fillo = [&](int e) AI { if (HO) dma4(rs_w1, (rr * F_ + (7 * (xcd >> 1) + 4 * (xcd & 1)) * 64) * 2 + (lane & 7) * 16, ((4 * m + e) * 32 + 64 * dt) * (F_ * 2), 8 * F_ * 2, lb + L_LATE + m * 4096 + e * SS); else dma4(rs_wo, woE, (4 * m + ((e + RO * st) & 3)) * 28 * (D_ * 2), 8 * D_ * 2, lb + L_LATE + m * 4096 + e * SS); };
        const int ub = WS_U + 4 * dt * 1024, ob = (OH ? WS_H + (4 * (st & 15) * 16 + 4 * m) * 1024 : WS_O + (4 * st * 16 + 4 * m) * 1024);
        u64 o0 = 0;
        auto ldu = [&](auto pc, auto tc) AI {
          constexpr int p = decltype(pc)::value, t = decltype(tc)::value;
          if constexpr (LATE && p == 6 && t == 0) { asm volatile("s_waitcnt vmcnt(%1)" : "+v"(o0) : "n"(4 * (6 - OPK)) : "memory"); settle(rs_ws, oo, o0, tag); }
          if constexpr (LATE && p == OPK && t == 0) o0 = peek(rs_ws, oo);
          if constexpr (LATE && p >= 6 && CE) ld1_a<t * 1024>(uv[p % NB2][t], rs_ws, v0, ob + (p - 6) * 16384);
          else if constexpr (LATE && p >= 6) ld1_a<0>(uv[p % NB2][t], rs_ws, v0, ob + t * 16384 + ((p - 6 + RO * st) & 3) * 1024);
          else if constexpr (C2) ld1_a<(2 * (p & 1) + (t >> 1)) * 1024>(uv[p % NB2][t], rs_ws, v0, ub + pos2(LATE, (p & ~1) + (t & 1)) * KS_);
          else ld1_a<t * 1024>(uv[p % NB2][t], rs_ws, v0, ub + pos2(LATE, p) * KS_);
        };
        auto first = [&]() AI {
          settle(rs_ws, fo, f0, tag);
          PRIO(LATE ? PL2 : PE2);
          if (PF1 && LATE) PRIO(3);
          SFOR((NB2 - ((LATE ? GK : GE) >= 0)) * 4, n, ldu(IC(n / 4), IC(n % 4)); if constexpr (LATE && n + 1 == PF1) PRIO(PL2););
          SB();
        };
        if constexpr (!LATE) {
          const int vr = w2lane(lane);
#pragma unroll
          for (int p = N0; p < 8; ++p) {
            ldw2(f2[p], vr, p);
            if (!SG2 && p == N0 + NSG - 1) { WAITVM(4 * NSG); sig(lb, 2); }
          }
          await(L, 3, 4u);
          await(L, 4, 4u);
          auto turn = [&](auto pc, auto nc) AI {
            constexpr int p = decltype(pc)::value, n = decltype(nc)::value;
            if constexpr (p < T0) return;
            wait4<n>(f2[2 * p]);
            wait4<n>(f2[2 * p + 1]);
            turn_pair(f2[2 * p], f2[2 * p + 1]);
            SB();
          };
#define TURN(p, n) turn(std::integral_constant<int, p>{}, std::integral_constant<int, n>{})
          if constexpr (FA && decltype(idc)::value == 0) {
            fill(0, reg); fill(1, reg + SS);
            await(L, 5, 4u);
            fill(2, reg + 2 * SS); fill(3, reg + 3 * SS);
            fill(4, L_HB + m * 5120); fill(5, L_LATE + reg + 3 * SS);
            WAITVM(48);
            fillo(0); fillo(1); fillo(2);
            PRIO(PA);
            if (FA == 2) { TURN(0, 60); TURN(1, 52); }
            WAITVM(12);
            sig(lb, 16 + m);
            if (FA == 1) { TURN(0, 12); TURN(1, 12); }
            WAITVM(0);
            sig(lb, 16 + m);
            TURN(2, 0);
            f0 = peek(rs_ws, fo);
            TURN(3, 1);
            asm volatile("s_waitcnt vmcnt(0)" : "+v"(f0)::"memory");
            first();
          } else {
          fill(0, reg); fill(1, reg + SS);
          if (SG2) { WAITVM(4 * (8 - N0) + 8); sig(lb, 2); }
          if (IL) TURN(0, 32);
          await(L, 5, 4u);
          fill(2, reg + 2 * SS); fill(3, reg + 3 * SS);
          if (IL) TURN(1, 32);
          fill(4, L_HB + m * 5120); fill(5, L_LATE + reg + 3 * SS);
          if (IL) TURN(2, 32);
          if (OV == 2) sig(lb, 31);
          if (OV) f0 = peek(rs_ws, fo);
          fillo(0); fillo(1); fillo(2);
          WAITVM(12 + (OV ? 1 : 0));
          sig(lb, 16 + m);
          if (OV) {
            asm volatile("s_waitcnt vmcnt(12)" : "+v"(f0)::"memory");
            first();
            TURN(3, 24);
          } else {
            f0 = peek(rs_ws, fo);
            TURN(3, 13);
            WAITVM(1);
            sig(lb, 16 + m);
            asm volatile("s_waitcnt vmcnt(0)" : "+v"(f0)::"memory");
            first();
          }
          }
        } else {
          auto take = [&](int r, int so) AI {
            slot_frags(L, tb, so, f2[r]);
            asm volatile("s_waitcnt lgkmcnt(0)" : "+v"(f2[r][0]), "+v"(f2[r][1]), "+v"(f2[r][2]), "+v"(f2[r][3])::"memory");
          };
          if (OV == 2) { await(L, 31, 4u); f0 = peek(rs_ws, fo); asm volatile("s_waitcnt vmcnt(0)" : "+v"(f0)::"memory"); first(); }
          await<S2>(L, 16 + m, 1u);
          if (OV == 1) f0 = peek(rs_ws, fo);
          take(5, 3 * SS);
          fillo(3);
          if (OV == 1) { asm volatile("s_waitcnt vmcnt(4)" : "+v"(f0)::"memory"); first(); }
          if (!OV) f0 = peek(rs_ws, fo);
#pragma unroll
          for (int r = 0; r < 4; ++r) slot_frags(L, tb, r * SS - L_LATE, f2[r]);
          slot_frags(L, tb, XS + m * 1024, f2[4]);
          sig(lb, 20 + m);
          if constexpr (DF == 2 && decltype(idc)::value == 1) { WAITVM(5); if (arrive(lb, 6) == 3u) putflag(WS_OF + (rg * 8 + hp) * FS, lane); }
          if (!OV) {
            await(L, 16 + m, 2u);
            asm volatile("s_waitcnt vmcnt(0)" : "+v"(f0)::"memory");
            if constexpr (DF == 1 && decltype(idc)::value == 1) { if (arrive(lb, 6) == 3u) putflag(WS_OF + (rg * 8 + hp) * FS, lane); }
            first();
          }
        }
        int wr[4], sa = sel(lane == 0, lb + L_CNT, OOB);
        unsigned one = 1u;
        if constexpr (LATE && CE == 2) { PINV(sa); PINV(one); }
#pragma unroll
        for (int e = 0; e < 4; ++e) wr[e] = reg + g * 256 + (LATE ? c ^ (2 * e + (g >> 1)) : (c + 8 * e + 4 * (g >> 1)) & 15) * 16;
        kloop<NK2, NR2, 4, true, LATE ? 6 : 99, NB2, LATE ? GK : GE>(L, tb, 0, f2, uv, acc, ldu,
            [&](auto ic, auto tc) AI {
              constexpr int i = LATE ? decltype(tc)::value : decltype(ic)::value, t = LATE ? decltype(ic)::value : decltype(tc)::value;
              *(LAS f32x4*)(L + wr[(t & 1) + (LATE ? 2 * (i & 1) : 0)] + i * SS + t * 1024) = acc[i][t];
              if constexpr (LATE && CE == 2 && (i & 1) && (t == 3 || (FB == 2 && t == FT)) && !(FB == 1 && i == 3)) { SB(); post<(i == 1 ? 31 : 10)>(sa, one); }
            }, [&](auto kc) AI {
              constexpr int k = decltype(kc)::value;
              if constexpr (!LATE && k == NK2 - 2) await(L, 20 + m, 1u);
              if constexpr (OV && !LATE && k == 0) sig(lb, 16 + m);
              if constexpr (OV && LATE && k == 1) await(L, 16 + m, 2u);
            });
      }
      if constexpr (LATE) {
        if constexpr (CE != 2) sig(lb, 10);
        if constexpr (FB == 1) { WAITLG(); BAR(); }
      } else if constexpr (CE == 2) {
        PRIO(PT);
        LANEVARS;
        const int rw = st * 64 + 8 * (rr & 3) + (rr & 4) + m, eo = (rw * D_ + dt * 64 + (lane & 7) * 8) * 2;
        u32x4 xr[2];
        float bs[2];
#pragma unroll
        for (int e = 0; e < 2; ++e) {
          xr[e] = __builtin_amdgcn_raw_buffer_load_b128(rs_x, eo + e * 32 * D_ * 2, 0, 0);
          bs[e] = lo_f(*(const GAS u16*)(b2 + rw + 32 * e));
        }
        int ra[2];
#pragma unroll
        for (int e = 0; e < 2; ++e) ra[e] = (m * 16 + ((lane & 7) >> 1)) * 1024 + (lane & 1) * 512 + ((4 * e + (rr & 3) + 2 * (rr & 4) + 4 * (lane & 3)) & 15) * 16;
        sig(lb, 9); await<S4>(L, 9, 4u);
        f32x4 tm[4][4], base[4];
#pragma unroll
        for (int j = 0; j < 4; ++j)
#pragma unroll
          for (int e = 0; e < 4; ++e) tm[j][e] = *(const LAS f32x4*)(L + ra[e >> 1] + j * 4096 + (e & 1) * 256);
        SB();
#pragma unroll
        for (int e = 0; e < 4; ++e) {
          const unsigned a = xr[e >> 1][2 * (e & 1)], b = xr[e >> 1][2 * (e & 1) + 1];
          const float s = bs[e >> 1];
          if (XR) base[e] = tm[0][e] + f32x4{s, lo_f(a), lo_f(b), s}; else
          base[e] = add4(add4(add4(f32x4{lo_f(a), hi_f(a), lo_f(b), hi_f(b)}, f32x4{s, s, s, s}), add4(tm[0][e], tm[1][e])), add4(tm[2][e], tm[3][e]));
          asm volatile("" : "+v"(base[e]));
        }
        int rl = L_LATE + ((rr >> 1) & 1) * SS + ((lane & 7) >> 1) * 1024 + (lane & 1) * 512 + ((8 * (rr & 1) + (rr & 4) + m) ^ ((lane & 3) + 2 * (rr & 2))) * 16;
        int e1 = eo + 32 * D_ * 2;
        asm volatile("" : "+v"(rl), "+v"(e1));
        const auto rs_out = __builtin_amdgcn_make_buffer_rsrc((void*)out, 0, S_ * D_ * 2, RSRC_W3);
        if constexpr (FLP) {
          int ca = lb + L_CNT + 4 * 31, ev = eo, it = 0;
          f32x4 b0 = base[0], b1 = base[1];
          _Pragma("unroll 1") do {
            asm volatile("" : "+v"(ca), "+v"(ev), "+v"(rl), "+v"(b0), "+v"(b1));
            if constexpr (FB == 2) {
              await_at(ca, 4u);
              take8(tm, lb + rl, ca);
            } else {
              if (FB == 1 && it) BAR(); else await_at(ca, 4u);
              SFOR(8, j, tm[j >> 1][j & 1] = *(const LAS f32x4*)(L + rl + (j >> 1) * 4096 + (j & 1) * 256););
            }
            SB();
            const f32x4 s0 = (b0 + tm[0][0]) + tm[1][0] + (tm[2][0] + tm[3][0]), s1 = (b1 + tm[0][1]) + tm[1][1] + (tm[2][1] + tm[3][1]);
            __builtin_amdgcn_raw_buffer_store_b128(u32x4{pack2(s0[0], s0[1]), pack2(s0[2], s0[3]), pack2(s1[0], s1[1]), pack2(s1[2], s1[3])}, rs_out, ev, 0, OAUX);
            SB();
            ca = lb + L_CNT + 4 * 10; ev = e1; rl += 2 * SS; b0 = base[2]; b1 = base[3];
            asm volatile("" : "+s"(it));
          } while (++it < 2);
        } else {
        SFOR(2, e,
          PRIO(e ? PZ : PH);
          await(L, e ? 10 : 31, 4u);
          SFOR(8, j, tm[j >> 1][j & 1] = *(const LAS f32x4*)(L + rl + 2 * e * SS + (j >> 1) * 4096 + (j & 1) * 256););
          SB();
          f32x4 s0, s1;
          if constexpr (e) {
            s0 = (base[2] + tm[0][0]) + tm[1][0] + (tm[2][0] + tm[3][0]);
            s1 = (base[3] + tm[0][1]) + tm[1][1] + (tm[2][1] + tm[3][1]);
          } else {
            s0 = add4(add4(add4(base[0], tm[0][0]), tm[1][0]), add4(tm[2][0], tm[3][0]));
            s1 = add4(add4(add4(base[1], tm[0][1]), tm[1][1]), add4(tm[2][1], tm[3][1]));
          }
          __builtin_amdgcn_raw_buffer_store_b128(u32x4{pack2(s0[0], s0[1]), pack2(s0[2], s0[3]), pack2(s1[0], s1[1]), pack2(s1[2], s1[3])}, rs_out, e ? e1 : eo, 0, AUX_SC1);
          SB();
        );
        }
      } else {
        LANEVARS;
        const int eo = ((st * 64 + 8 * rr + m) * D_ + dt * 64 + (lane & 7) * 8) * 2;
        u32x4 xr[2];
        float bs[2];
#pragma unroll
        for (int e = 0; e < 2; ++e) {
          xr[e] = __builtin_amdgcn_raw_buffer_load_b128(rs_x, eo + e * 4 * D_ * 2, 0, 0);
          bs[e] = lo_f(*(const GAS u16*)(b2 + st * 64 + 8 * rr + 4 * e + m));
        }
        int ra[2];
#pragma unroll
        for (int e = 0; e < 2; ++e) ra[e] = (m * 16 + ((lane & 7) >> 1)) * 1024 + (lane & 1) * 512 + ((rr + 8 * e + 4 * (lane & 3)) & 15) * 16;
        gbar4(L, lb, 9);
        f32x4 tm[4][4], base[4];
#pragma unroll
        for (int j = 0; j < 4; ++j)
#pragma unroll
          for (int e = 0; e < 4; ++e) tm[j][e] = *(const LAS f32x4*)(L + ra[e >> 1] + j * 4096 + (e & 1) * 256);
        SB();
#pragma unroll
        for (int e = 0; e < 4; ++e) {
          const unsigned a = xr[e >> 1][2 * (e & 1)], b = xr[e >> 1][2 * (e & 1) + 1];
          const float s = bs[e >> 1];
          base[e] = add4(add4(add4(f32x4{lo_f(a), hi_f(a), lo_f(b), hi_f(b)}, f32x4{s, s, s, s}), add4(tm[0][e], tm[1][e])), add4(tm[2][e], tm[3][e]));
          asm volatile("" : "+v"(base[e]));
        }
#pragma unroll
        for (int e = 0; e < 2; ++e) {
          ra[e] = L_LATE + (rr >> 1) * SS + ((lane & 7) >> 1) * 1024 + (lane & 1) * 512 + ((8 * (rr & 1) + 4 * e + m) ^ ((lane & 3) + 2 * (rr & 2))) * 16;
          asm volatile("" : "+v"(ra[e]));
        }
        PRIO(3);
        await(L, 10, 4u);
#pragma unroll
        for (int j = 0; j < 4; ++j)
#pragma unroll
          for (int e = 0; e < 4; ++e) tm[j][e] = *(const LAS f32x4*)(L + ra[e >> 1] + j * 4096 + (e & 1) * 256);
        SB();
        const auto rs_out = __builtin_amdgcn_make_buffer_rsrc((void*)out, 0, S_ * D_ * 2, RSRC_W3);
#pragma unroll
        for (int e = 0; e < 2; ++e) {
          const f32x4 s0 = (base[2 * e] + tm[0][2 * e]) + tm[1][2 * e] + (tm[2][2 * e] + tm[3][2 * e]);
          const f32x4 s1 = (base[2 * e + 1] + tm[0][2 * e + 1]) + tm[1][2 * e + 1] + (tm[2][2 * e + 1] + tm[3][2 * e + 1]);
          __builtin_amdgcn_raw_buffer_store_b128(u32x4{pack2(s0[0], s0[1]), pack2(s0[2], s0[3]), pack2(s1[0], s1[1]), pack2(s1[2], s1[3])},
                                                 rs_out, eo + e * 4 * D_ * 2, 0, AUX_SC1);
        }
      }
    }
    asm volatile("s_nop %0" ::"n"(decltype(idc)::value));
    FINISH();
  };
  auto utile = [&](auto latec) AI {
    constexpr bool LATE = decltype(latec)::value;
      auto gemm1 = [&](auto ntc) AI {
        constexpr int NT = decltype(ntc)::value, NL = 8 * NT, NB = NB1, TPN = TP * 4;
        const int t0 = NT == 5 ? 5 * dg : 4 * dg + 4;
        u32x4 fr[8][4], hv[NB], f2[LATE ? 6 : 8][4];
        LANEVARS;
        PRIO(LATE ? PLP : PEP);
        const int v0 = lane * 16, v4 = v0 + 4096, wr = reg + v0;
        const int tr = ((NT == 5 ? TR5 : TR4) >> (4 * (ft - 7 * (xcd >> 1) - 4 * (xcd & 1)))) & 7;
        auto tl = [&](int t) AI { return TR ? t + tr - sel(t + tr >= NT, NT, 0) : t; };
        int ho[8], vt;
        auto ld = [&](auto nc) AI {
          constexpr int n = decltype(nc)::value, t = n >> 3;
          if constexpr (TR) {
            if constexpr ((n & 7) == 0) { vt = v0 + tl(t) * 1024; PINV(vt); }
            ld1_a<0>(hv[n % NB], rs_ws, vt, ho[n & 7]);
          } else ld1_a<(t & 3) * 1024>(hv[n % NB], rs_ws, t < 4 ? v0 : v4, ho[n & 7]);
        };
        auto start = [&](int fo, u64 f0) AI {
          settle(rs_ws, fo, f0, tag);
          PRIO(LATE ? PL1 : PE1);
#pragma unroll
          for (int p = 0; p < 8; ++p) ho[p] = WS_H + (m * 16 + k1((LATE ? 8 : 0) + p)) * KS_ + t0 * 1024;
          SFOR(NB, n, ld(n_c););
          SB();
        };
        {
          const int w1E = ((m * 512 + rr) * F_ + ft * 64) * 2 + ((lane & 7) ^ (2 * ((rr >> 1) & 3))) * 16;
          auto fill = [&](int j) AI { dma4(rs_w1, w1E, k1(j) * 32 * (F_ * 2), 8 * F_ * 2, lb + reg + ((j >> 2) & 1) * L_LATE + (j & 3) * SS); };
          int tb[2][4];
#pragma unroll
          for (int i = 0; i < 8; ++i) tb[i >> 2][i & 3] = (i >> 2) * L_LATE + m * 4096 + trbase(i & 3, g, q4, p4);
          auto take = [&](int j) AI { slot_frags(L, tb[j >> 2], (j & 3) * SS, fr[j]); };
          auto taken = [&](int j) AI { asm volatile("s_waitcnt lgkmcnt(0)" : "+v"(fr[j][0]), "+v"(fr[j][1]), "+v"(fr[j][2]), "+v"(fr[j][3])::"memory"); };
          const int fo = WS_HF + (m * 32 + 2 * (((LATE ? 8 : 0) + ((lane >> 1) & 7) + off1) & 15) + (lane & 1)) * FS;
          u64 f0;
          if constexpr (!LATE) {
            if constexpr (NT == 4) { WAITVM(W0); if (GS) await(L, 7, 4u); } else WAITVM(W5);
            if constexpr (W6 < 12) { fill(2); WAITVM(W6); }
#pragma unroll
            for (int j = W6 < 12 ? 3 : 2; j < 8; ++j) fill(j);
            await(L, 7, 4u);
            gettag();
            SFOR(8, j, WAITVM(28 + (j > 8 - PKE)); take(j); taken(j); if constexpr (j == 8 - PKE) f0 = peek(rs_ws, fo); fill(8 + j););
            WAITVM(17); sig(lb, 12 + m);
            asm volatile("s_waitcnt vmcnt(%1)" : "+v"(f0) : "n"(4 * PKE) : "memory");
            start(fo, f0);
            WAITVM(NB); sig(lb, 12 + m);
          } else {
            if (m == 0) {
              const auto rs_b1 = __builtin_amdgcn_make_buffer_rsrc((void*)b1, 0, F_ * 2, RSRC_W3);
              await(L, 7, 4u);
              touch(rs_b1, sel(lane < 32, lane * 4, OOB), ft * 128, lb + L_B1);
            }
            await<S1>(L, 12 + m, 1u);
            f0 = peek(rs_ws, fo);
#pragma unroll
            for (int j = 0; j < 4; ++j) take(j);
            asm volatile("s_waitcnt vmcnt(0)" : "+v"(f0)::"memory");
            if (m == 0) sig(lb, 11);
            start(fo, f0);
            await(L, 12 + m, 2u);
#pragma unroll
            for (int j = 4; j < 8; ++j) take(j);
#pragma unroll
            for (int j = 0; j < 8; ++j) taken(j);
            sig(lb, 30);
          }
        }
        const int ra = (lane >> 5) * L_LATE + ((LATE ? 2 : 0) + (g & 1)) * 1024 + m * 256 + c * 16;
        const int ba = L_B1 + (16 * ((LATE ? 2 : 0) + (g & 1)) + 4 * m + 2 * (lane >> 5)) * 2;
        const int ub = WS_U + (2 * ft + (LATE ? 1 : 0)) * KS_ + t0 * 1024 + m * 256 + c * 16, uo = ub + tl(g) * 1024;
        const int vr = w2lane(lane);
        int sa = sel(lane == 0, lb + L_CNT, OOB);
        unsigned one = 1u;
        PINV(sa); PINV(one);
        f32x4 acc[2][4], pt[4];
        float bq[2], v[2], z[2];
        unsigned pr[6], cv, cr;
        auto rd = [&](int t, int o) AI {
#pragma unroll
          for (int e = 0; e < 4; ++e) pt[e] = *(const LAS f32x4*)(L + ra + o + g1buf(NT, t) + e * 4096);
        };
        auto step = [&](auto sc, int t) AI {
          constexpr int s = decltype(sc)::value;
          if constexpr (s == 0) { pt[0] = add4(pt[0], pt[1]); PINV(pt[0]); }
          if constexpr (s == 1) { pt[2] = add4(pt[2], pt[3]); PINV(pt[2]); }
          if constexpr (s == 2) { pt[0] = add4(pt[0], pt[2]); PINV(pt[0]); }
          if constexpr (s == 3) {
#pragma unroll
            for (int e = 0; e < 2; ++e) { v[e] = vadd(fold32(pt[0][e], pt[0][2 + e]), bq[e]); PINV(v[e]); }
          }
          if constexpr (s == 4) {
#pragma unroll
            for (int e = 0; e < 2; ++e) {
              const float p = solo(__builtin_fmaf(vmul(v[e], v[e]), -0.1029432395800235f, -2.302208198144325f));
              z[e] = vmul(v[e], p); PINV(z[e]);
            }
          }
          if constexpr (s == 5) {
#pragma unroll
            for (int e = 0; e < 2; ++e) { z[e] = vadd(__builtin_amdgcn_exp2f(z[e]), 1.0f); PINV(z[e]); }
          }
          if constexpr (s == 6) {
            pr[t] = pack2(vmul(__builtin_amdgcn_rcpf(z[0]), v[0]), vmul(__builtin_amdgcn_rcpf(z[1]), v[1]));
            PINV(pr[t]);
          }
        };
        unsigned sw[4];
        auto fine = [&](auto sc, int t) AI {
          constexpr int s = decltype(sc)::value;
          if constexpr (s < 6) {
            constexpr int d = s < 2 || s >= 4 ? 0 : 2, o = s < 2 ? 1 : s < 4 ? 3 : 2, e = 2 * (s & 1);
            pt[d][e] = vadd(pt[d][e], pt[o][e]); pt[d][e + 1] = vadd(pt[d][e + 1], pt[o][e + 1]); PINV(pt[d]);
          }
          if constexpr (s == 6) {
#pragma unroll
            for (int e = 0; e < 2; ++e) {
              const auto r = __builtin_amdgcn_permlane32_swap(__float_as_uint(pt[0][e]), __float_as_uint(pt[0][2 + e]), false, false);
              sw[2 * e] = r[0]; sw[2 * e + 1] = r[1]; PINV(sw[2 * e]); PINV(sw[2 * e + 1]);
            }
          }
          auto two = [&](float (&d)[2], auto f) AI { d[0] = f(0); d[1] = f(1); PINV(d[0]); PINV(d[1]); };
          if constexpr (s == 7) two(v, [&](int e) AI { return vadd(__uint_as_float(sw[2 * e]), __uint_as_float(sw[2 * e + 1])); });
          if constexpr (s == 8) two(v, [&](int e) AI { return vadd(v[e], bq[e]); });
          if constexpr (s == 9) two(z, [&](int e) AI { return vmul(v[e], v[e]); });
          if constexpr (s == 10) two(z, [&](int e) AI { return solo(__builtin_fmaf(z[e], -0.1029432395800235f, -2.302208198144325f)); });
          if constexpr (s == 11) two(z, [&](int e) AI { return vmul(v[e], z[e]); });
          if constexpr (s == 12 || s == 13) { z[s - 12] = __builtin_amdgcn_exp2f(z[s - 12]); PINV(z[s - 12]); }
          if constexpr (s == 14) two(z, [&](int e) AI { return vadd(z[e], 1.0f); });
          if constexpr (s == 15 || s == 16) { z[s - 15] = __builtin_amdgcn_rcpf(z[s - 15]); PINV(z[s - 15]); }
          if constexpr (s == 17) two(z, [&](int e) AI { return vmul(z[e], v[e]); });
          if constexpr (s == 18) { pr[t] = pack2(z[0], z[1]); PINV(pr[t]); }
        };
        auto put = [&](unsigned a, unsigned b, unsigned cc, unsigned d, int off) AI {
          const auto s0 = __builtin_amdgcn_permlane32_swap(a, cc, false, false), s1 = __builtin_amdgcn_permlane32_swap(b, d, false, false);
          const auto r0 = __builtin_amdgcn_permlane16_swap(s0[0], s1[0], false, false), r1 = __builtin_amdgcn_permlane16_swap(s0[1], s1[1], false, false);
          __builtin_amdgcn_raw_buffer_store_b128(u32x4{r0[0], r1[0], r0[1], r1[1]}, rs_ws, off, 0, AUX_SC1);
          SB();
        };
        auto bias = [&](int o) AI { const unsigned b = *(const LAS unsigned*)(L + ba + o); bq[0] = lo_f(b); bq[1] = hi_f(b); };
        SFOR(NL, n,
          constexpr int t = n >> 3, p = n & 7, nw = !LATE && t == NT - 1 ? 4 * (p < NE ? p : NE) : 0;
          if constexpr (n == 9 && !LATE) check(L, cr, 30, 4u);
          if constexpr (n == 13) { check(L, cv, 11, 1u); bias(0); }
          if constexpr (t >= 2 && p == RP) { check(L, cv, 24 + t - 2, 8u); rd(t - 2, 0); if constexpr (NT == 5 && t == 2) post<29>(sa, one); }
          if constexpr (!LATE && n == NL - TPN) PRIO(3);
          if constexpr (t >= 2 && p == RP - 1) cv = look(L, 24 + t - 2);
          if constexpr (n == 7 && !LATE) cr = look(L, 30);
          if constexpr (n == 11) cv = look(L, 11);
          if constexpr (n == NL - 2) { cr = look(L, 29); cv = look(L, 24 + NT - 2); }
          SB();
          if constexpr (!CH) wait1<(n + NB < NL ? n + NB : NL) - 1 - n + nw>(hv[n % NB]);
          else if constexpr (!(n & 1)) wait2<(n + NB < NL ? n + NB : NL) - 2 - n + nw>(hv[n % NB], hv[(n + 1) % NB]);
          SFOR(4, i,
            constexpr int ci = CH ? 2 * (p & 1) + (i >> 1) : i, cn = CH ? (n & ~1) + (i & 1) : n;
            mfma_ip<(cn & 7) == 0>(acc[t & 1][ci], fr[cn & 7][ci], hv[cn % NB]);
            if constexpr (t >= 1 && (p == 1 || p == 2) && (i & 1)) *(LAS f32x4*)(L + wr + g1buf(NT, t - 1) + (2 * p - 2 + (i >> 1)) * 1024) = acc[(t - 1) & 1][2 * p - 2 + (i >> 1)];
            constexpr int se = 4 * (p - RP - 1) + i - 1;
            if constexpr (!MSP && t >= 2 && se >= 0 && se < 7) step(IC(se), t - 2);
            if constexpr (MSP && t >= 2 && se >= 0) SFOR(MSP, k, if constexpr (se * MSP + k < 19) fine(IC(se * MSP + k), t - 2););
            SB();
          );
          if constexpr (t >= 1 && p == 2) post<24 + t - 1>(sa, one);
          if constexpr (CH && (n & 1) && n + NB - 1 < NL) ld(IC(n + NB - 1));
          if constexpr ((!CH || (n & 1)) && n + NB < NL) ld(IC(n + NB));
          if constexpr (CH && !LATE && t == NT - 1 && (p & 1) && p - 1 < NE) ldw2(f2[p - 1], vr, p - 1);
          if constexpr ((!CH || (p & 1)) && !LATE && t == NT - 1 && p < NE) ldw2(f2[p], vr, p);
          SB();
        );
        PRIO(LATE ? PG : PGE);
        if constexpr (NT == 5) check(L, cr, 29, 8u);
        asm volatile("s_nop 7" ::: "memory");
#pragma unroll
        for (int i = 0; i < 4; ++i) *(LAS f32x4*)(L + wr + g1buf(NT, NT - 1) + i * 1024) = acc[(NT - 1) & 1][i];
        post<24 + NT - 1>(sa, one);
        if constexpr (LATE) post<4>(sa, one);
        check(L, cv, 24 + NT - 2, 8u);
        rd(NT - 2, 0);
        if constexpr (!LATE) { post<3>(sa, one); PRIO(PGF); }
        SFOR(7, s, step(s_c, NT - 2););
        if constexpr (NT == 5) put(pr[0], pr[1], pr[2], pr[3], uo);
        else if constexpr (!LATE) put(pr[0], pr[1], pr[2], pr[2], sel(lane < 48, uo, OOB));
        if constexpr (LATE) {
          const unsigned b2q = *(const LAS unsigned*)(L + ba - 64);
          await<S3>(L, 24 + NT - 1, 8u);
          if (PG != 3) PRIO(3);
          {
            f32x4 qa[8];
#pragma unroll
            for (int e = 0; e < 8; ++e) qa[e] = *(const LAS f32x4*)(L + ra - (e >> 2) * 2048 + g1buf(NT, NT - 1) + (e & 3) * 4096);
            post<5>(sa, one);
            const f32x4 s0 = (qa[0] + qa[1]) + (qa[2] + qa[3]), s1 = (qa[4] + qa[5]) + (qa[6] + qa[7]);
            f32x4 y[1] = {f32x4{fold32(s0[0], s0[2]), fold32(s0[1], s0[3]), fold32(s1[0], s1[2]), fold32(s1[1], s1[3])} + f32x4{bq[0], bq[1], lo_f(b2q), hi_f(b2q)}};
            unsigned o2[2];
            gelu_pack<1>(y, o2);
            pr[NT - 1] = o2[0]; pr[5] = o2[1];
          }
          if constexpr (NT == 4) put(pr[0], pr[1], pr[2], pr[3], uo);
          if constexpr (NT == 5) put(pr[4], pr[5], pr[4], pr[5], sel(lane < 32, ub + tl(4) * 1024 - g * KS_, OOB));
          else put(pr[5], pr[5], pr[5], pr[5], sel(lane < 16, ub + tl(3) * 1024 - KS_, OOB));
          DRAIN();
          if (arrive(lb, 6) == 3u) { await(L, 2, 4u); putflag(WS_UF + (ft * 6 + dg) * FS, lane); }
        }
        gemm2(latec, IC(LATE ? 0 : NE), f2, IC(2 * NT - 6 + LATE));
      };
      if (dg < 4) gemm1(IC(5));
      gemm1(IC(4));
  };
  if (role == 0) utile(std::false_type{});
  if (role == 1) utile(std::true_type{});
  if (role == 2) {
    u32x4 f2[8][4];
    constexpr int NA = (7 - EW) * EG < 8 ? (7 - EW) * EG : 8;
    auto na = [](int n) constexpr { return n < EW ? 0 : (n + 1 - EW) * EG < 8 ? (n + 1 - EW) * EG : 8; };
    auto nt = [](int n) constexpr { return n < TW ? 0 : n + 1 - TW < 4 ? n + 1 - TW : 4; };
    {
      LANEVARS;
      const int vr = w2lane(lane);
        const int cq = (lane & 7) ^ (2 * ((rr >> 1) & 1)) ^ (4 * (m & 1));
        const int vq = sel(cq < 7, (8 * m + rr) * (N3_ * 2) + hp * 112 + cq * 16, OOB);
        const int va = (r0 + 16 + 8 * m + rr) * (D_ * 2) + ((lane & 7) ^ (4 * (m & 1) + (rr >> 1))) * 16;
        const int so_ = WS_ST + min(max(r0 - 16 + min(64 * m + lane, 207), 0), S_ - 1) * 16;
        auto pcs = [&](int pp, int from, int to) AI {
          const bool wr = rotq >= 7 - pp;
#pragma unroll
          for (int n = from; n < to; ++n) {
            if (n < 6) piece(false, vq, (pp + 2) % 3, fs + sel(wr, (pp - 7) * 64 * N3_ * 2, pp * 64 * N3_ * 2), n);
            else piece(true, va, (pp + 2) % 3, rotq * 128 + sel(wr, (pp - 7) * 128, pp * 128), n - 6);
          }
        };
        u64 e0 = peek(rs_ws, WS_E);
        if (AV < 14) WAITVM(AV);
        pcs(1, 0, LA0);
        asm volatile("s_waitcnt vmcnt(%1)" : "+v"(e0) : "n"(LA0) : "memory");
        mktag(e0);
        WAITLG();
        BAR();
        auto warm = [&](int e) AI {
          const auto rs_wo = __builtin_amdgcn_make_buffer_rsrc((void*)wo, 0, D_ * D_ * 2, RSRC_W3);
          const int j = idx - 28, o = WORD ? (e + 7) % 11 : e;
          if (o < 3) touch(rs_w2, (lane >> 2) * (S_ * 2) + (lane & 3) * 128, (448 * m + 256 + 48 * j + 16 * o) * (S_ * 2) + (xcd ^ SX) * 512, lb + L_DUMMY);
          else if (o < 7) touch(rs_wo, lane * 128, ((o - 3) * 16 + j * 4 + m) * 8192, lb + L_DUMMY);
          else touch(rs_w2, (lane >> 2) * (S_ * 2) + (lane & 3) * 128, (448 * m + 64 * j + 16 * (o - 7)) * (S_ * 2) + (xcd ^ SX) * 512, lb + L_DUMMY);
        };
        u32x4 sg;
        SFOR(6, n0,
          constexpr int n = n0 + 1, lb_ = n == 5 ? LA6 : n < 3 ? LA0 : LA1, la = lb_ < 12 ? lb_ : 12, lp_ = n == 6 ? LA6 : n < 4 ? LA0 : LA1;
          pcs(n, lp_, 13);
          if constexpr (n == 3) asm volatile("buffer_load_dwordx4 %0, %1, %2, 0 offen sc1" : "=v"(sg) : "v"(so_), "s"(rs_ws) : "memory");
          if constexpr (n < 6) pcs(n + 1, 0, lb_);
          if constexpr (n == 4) {
            asm volatile("" : "+v"(sg));
            _Pragma("unroll 1")
            for (unsigned spins = 0; spins < SPIN_CAP; ++spins) {
              if (__all((sg.y == (unsigned)tag) & (sg.w == (unsigned)(tag >> 32)) & (sg.x == sg.z))) break;
              __builtin_amdgcn_s_sleep(4);
              asm volatile("buffer_load_dwordx4 %0, %1, %2, 0 offen sc1\n\ts_waitcnt vmcnt(0)" : "=v"(sg) : "v"(so_), "s"(rs_ws) : "memory");
            }
            *(LAS float*)(L + A_MEAN + (64 * m + lane) * 4) = -lo_f(sg.x);
            *(LAS float*)(L + A_RSTD + (64 * m + lane) * 4) = __builtin_amdgcn_rcpf(hi_f(sg.x));
            WAITLG();
          }
          constexpr int ge = na(n) - na(n - 1);
          SFOR(ge, e, ldw2(f2[na(n - 1) + e], vr, na(n - 1) + e););
          if constexpr (nt(n) > nt(n - 1)) {
            static_assert(2 * nt(n) <= na(n - 2), "");
            wait4<63>(f2[2 * nt(n) - 2]); wait4<63>(f2[2 * nt(n) - 1]);
            turn_pair(f2[2 * nt(n) - 2], f2[2 * nt(n) - 1]);
            SB();
          }
          if (n >= WST && idx >= 28) {
            SFOR(WPI, e, warm((n - WST) * WPI + e););
            WAITVM((n < 6 ? la : 0) + WPI + 4 * ge);
          } else {
            WAITVM((n < 6 ? la : 0) + 4 * ge);
          }
          BAR();
        );
        sig(lb, 3);
        if (WRM && idx >= 28) {
          auto code = [&]() AI {
            u64 pc;
            unsigned co = (((idx - 28) * 4 + m) * 64 + lane) * 128;
            unsigned dl;
            asm volatile("s_getpc_b64 %0\n.Lct%=:\n\ts_mov_b32 %1, .Lct%=-.Lfb_start" : "=s"(pc), "=s"(dl));
            pc -= dl;
            asm volatile("s_mov_b32 m0, %2\n\tv_min_u32 %0, .Lfb_end-.Lfb_start-8, %0\n\tglobal_load_lds_dword %0, %1" : "+v"(co) : "s"(pc), "s"(lb + L_DUMMY) : "memory");
          };
          if (CT == 1) code();
          if (WSL) touch(rs_w2, lane * (S_ * 2), (448 * m + 64 * (idx - 28)) * (S_ * 2) + (xcd ^ SX) * 512 + 384, lb + L_DUMMY);
#pragma unroll
          for (int e = (7 - WST) * WPI; e < WN; ++e) { if (WT < 9) WAITVM(WT < 9 ? WT : 0); if (e == WP) await(L, 6, 4u); warm(e); if (CT == 2 && e == 2) code(); }
          if (CT == 3) code();
          DRAIN();
          SFOR(NA, p, wait4<0>(f2[p]););
        }
    }
    if (idx < 28) gemm2(std::false_type{}, IC(NA + 16 * nt(6)), f2, IC(0));
    FINISH();
  } else {
        auto att = [&](auto jc) AI {
          constexpr int J = decltype(jc)::value, NH = J ? 2 : 4, H0 = J ? 0 : 2;
          const int H = m & 1;
          LANEVARS;
          f32x4 qa[6][6], qh[NH];
          int wa[2], xa[2][2];
#pragma unroll
          for (int i = 0; i < 2; ++i) {
            const int by = sel((i == 1) & (p4 == 3), 112, 56 * H + 32 * i + 8 * p4);
            wa[i] = A_W + (8 * g + q4) * 128 + (((by >> 4) ^ (2 * ((q4 >> 1) | ((g & 1) << 1)))) << 4) + (by & 8);
          }
          int wb[2] = {wa[0] + A_WS + 12288, wa[1] + A_WS + 12288};
#pragma unroll
          for (int i = 0; i < 4; ++i) xa[i >> 1][i & 1] = (i >> 1) * A_X1 + c * 128 + (((4 * (i & 1) + g) ^ ((c >> 1) & 7)) << 4);
          asm volatile("" : "+v"(wa[0]), "+v"(wa[1]), "+v"(wb[0]), "+v"(wb[1]), "+v"(xa[0][0]), "+v"(xa[0][1]), "+v"(xa[1][0]), "+v"(xa[1][1]));
          u32x4 wf[6], xf[7];
          u32x2 lo;
          auto rdx = [&](int k, int t) AI {
            const int sg = ((k >> 1) + 2) % 3;
            xf[t] = *(const LAS u32x4*)(L + xa[sg ? 1 : 0][k & 1] + (sg == 2 ? A_XS : 0) + (t == 6 ? 0 : (1 + 6 * J + t) * 2048));
          };
          auto rdw = [&](int k, int i, int hf) AI {
            const int o = (((k >> 1) + 2) % 3) * 2 + (k & 1);
            const LAS char* p = L + (o < 3 ? wa[i & 1] + o * 12288 : wb[i & 1] + (o - 3) * 12288) + (i >> 1) * 4096;
            if (hf) { const u32x2 hi = tr_read(p + 512); wf[i] = u32x4{lo.x, lo.y, hi.x, hi.y}; }
            else lo = tr_read(p);
          };
          const bool c0 = c == 0;
          int exa = lb + A_X1 + H * 4096 + lane * 16;
          int ta = lb + A_MEAN + (96 * J + c) * 4, tk = lb + A_RSTD + (96 * J + 4 * g + (c & 1)) * 4, tc = lb + A_CS + (H * 96 + 4 * g) * 4, tv = lb + A_CS + (H * 96 + 64 + c) * 4;
          int sa = sel(lane == 0, lb + L_CNT, OOB);
          unsigned one = 1u;
          f32x2 mk[2][2];
          {
            const int d0 = 16 + (c & 14) - 4 * g, hi = min(r0 + c, 14);
#pragma unroll
            for (int e = 0; e < 4; ++e) {
              const int dk = d0 - 16 * (e >> 1) - 2 * (e & 1);
              mk[1][e >> 1][e & 1] = sel((unsigned)dk <= 14u, 0.f, -3.0e38f);
              mk[0][e >> 1][e & 1] = sel((unsigned)dk <= (unsigned)hi, 0.f, -3.0e38f);
            }
          }
          PINV(exa); PINV(ta); PINV(tk); PINV(tc); PINV(tv); PINV(sa); PINV(one); PINV(mk[1][0]); PINV(mk[1][1]);
          if constexpr (J) { mk[0][0] = mk[1][0]; mk[0][1] = mk[1][1]; } else { PINV(mk[0][0]); PINV(mk[0][1]); }
          float mc[7], rc[6];
          f32x2 rk[7];
          auto tab = [&](int e) AI {
            const int t = e / 3;
            if (e % 3 == 0) mc[t] = *(const LAS float*)(LA_(ta) + 64 * t);
            if (e % 3 == 1) { const LAS float* p = (const LAS float*)(LA_(tk) + 64 * t); rk[t] = f32x2{p[0], p[2]}; }
            if (e % 3 == 2) rc[t] = *(const LAS float*)(LA_(ta) + A_RSTD - A_MEAN + 64 * (t + 1));
          };
          if (LDF) {
            DRAIN();
            if (arrive(lb, 1) == 3u) putflag(WS_HF + un * FS, lane);
          }
          BAR();
          PRIO(2);
#pragma unroll
          for (int t = 0; t < 7; ++t) rdx(0, t);
#pragma unroll
          for (int i = 0; i < 10; ++i) rdw(0, i >> 1, i & 1);
          gettag();
          SB();
          SFOR(14, k,
            SFOR(6, ci,
              constexpr int i = (k == 13 && J == 0) ? (ci + 2) % 6 : ci;
              SFOR(7, t,
                if constexpr (SKA && k > 0 && (J ? (t == 6 || (SKA > 1 && t == 4 && i < 4)) : (t == 4 && i < 4) || (SKA > 1 && t == 3 && i < 4))) {
                } else if constexpr (t < 6) {
                  if constexpr (i < 4) mfma_ip<k == 0>(qa[t][i], wf[i], xf[t]); else mfma_ip<k == 0>(qa[t][i], xf[t], wf[i]);
                } else if constexpr (i >= H0 && i < H0 + NH) {
                  if constexpr (i < 4) mfma_ip<k == 0>(qh[i - H0], wf[i], xf[6]); else mfma_ip<k == 0>(qh[i - H0], xf[6], wf[i]);
                  if constexpr (k < 13 && i == H0 + NH - 1 && J == 0) rdx(k + 1, 6);
                }
                if constexpr (ci == 0 && t < 2) rdw(k, 5, t);
                if constexpr (ci == (k == 13 ? 0 : H0) && t == 2) { SFOR(4, j, xf[6][j] = sel(c0, 0x3f803f80u, xf[6][j]);); asm volatile("" : "+v"(xf[6])); }
                if constexpr ((k & 1) && k < 13 && ci == 1 && t == 1) { WAITLG(); BAR(); }
                if constexpr (k < 13 && ci >= 1 && (t == 2 || t == 3)) rdw(k + 1, ci - 1, t - 2);
                if constexpr (k < 13 && ci == 5 && t < 6) rdx(k + 1, t);
                if constexpr (k < 13 && J == 1 && ci == 2 && t == 0) rdx(k + 1, 6);
                if constexpr (k == 13 && t == 2 && ci >= 2 && ci < NH + 2) {
                  constexpr int ip = H0 + ci - 2;
                  if constexpr (ip < 4) *(LAS f32x4*)(LA_(sel(c0, tc, lb + L_DUMMY)) + 64 * ip) = qh[ci - 2];
                  else *(LAS float*)(LA_(sel(g == 0, tv, lb + L_DUMMY)) + 64 * (ip - 4)) = qh[ci - 2][0];
                }
                if constexpr (k == 13 && J == 0 && t == 4 && ci >= 2) *(LAS f32x4*)(LA_(exa) + (ci - 2) * 1024) = qa[5][ci];
                if constexpr (k == 13 && (J ? ci == 3 && t == 2 : ci == 5 && t == 4)) { SB(); post<4>(sa, one); }
                if constexpr (k == 13 && ci >= 2 && t < 5) tab(5 * (ci - 2) + t);
                SB();
              );
            );
          );
          await(L, 4, 4u);
          f32x4 c4[4], ex[4];
          float cv[2];
#pragma unroll
          for (int i = 0; i < 4; ++i) c4[(i + 2) & 3] = *(const LAS f32x4*)(LA_(tc) + 64 * ((i + 2) & 3));
#pragma unroll
          for (int i = 0; i < 4; ++i) { if constexpr (J) ex[i] = *(const LAS f32x4*)(LA_(exa) + i * 1024); else ex[i] = qh[i & (NH - 1)]; }
#pragma unroll
          for (int i = 0; i < 2; ++i) cv[i] = *(const LAS float*)(LA_(tv) + 64 * i);
          SB();
          post<5>(sa, one);
          const bool odd = ta & 4;
          f32x2 lg[6][2];
#pragma unroll
          for (int u = 0; u < 6; ++u) {
            const float sc = rc[u] * (0.18898223650461362f * 1.4426950408889634f);
            lg[u][0] = rk[u] * sc; lg[u][1] = rk[u + 1] * sc;
            PINV(lg[u][0]); PINV(lg[u][1]);
          }
          int tm = (tk & ~4) + A_MEAN - A_RSTD;
          int ov = exa - lb + WS_O - A_X1 + ((12 * rg + 6 * J) * 16 + 2 * hp - 3 * H) * 1024;
          unsigned p0 = sel(odd, 0x01000c0cu, 0x0c0c0100u), p1 = sel(odd, 0x03020c0cu, 0x0c0c0302u);
          PINV(tm); PINV(ov); PINV(p0); PINV(p1);
          SB();
          auto pk4 = [&](f32x4 v) AI { return u32x2{pack2(v[0], v[1]), pack2(v[2], v[3])}; };
          auto frag = [&](f32x4 a, f32x4 b, float mu, int i) AI {
            const u32x2 l = pk4(c4[i] * mu + a), h = pk4(c4[i + 1] * mu + b);
            return u32x4{l.x, l.y, h.x, h.y};
          };
          f32x4 s0[6], s1[6], m4[7];
          u32x4 kf[7], qf[6];
          kf[0] = frag(ex[0], ex[1], mc[0], 2);
          qf[0] = frag(qa[0][0], qa[0][1], mc[1], 0);
#pragma unroll
          for (int u = 0; u < 6; ++u) {
            kf[u + 1] = frag(qa[u][2], qa[u][3], mc[u + 1], 2);
            SB();
            s0[u] = mfma16(kf[u], qf[u], Z4);
            m4[u] = *(const LAS f32x4*)(LA_(tm) + 64 * u);
            SB();
            if (u < 5) qf[u + 1] = frag(qa[u + 1][0], qa[u + 1][1], mc[u + 2], 0); else m4[6] = *(const LAS f32x4*)(LA_(tm) + 64 * 6);
            SB();
            s1[u] = mfma16(kf[u + 1], qf[u], Z4);
            SB();
          }
          float mx[6], inv[6];
#pragma unroll
          for (int u = 0; u < 6; ++u) {
#pragma unroll
            for (int e = 0; e < 2; ++e) {
              const f32x4 s = e ? s1[u] : s0[u];
              lg[u][e] = __builtin_elementwise_fma(f32x2{sel(odd, s[1], s[0]), sel(odd, s[3], s[2])}, lg[u][e], mk[u ? 1 : 0][e]);
            }
            mx[u] = max2(max2(lg[u][0][0], lg[u][0][1]), max2(lg[u][1][0], lg[u][1][1]));
          }
          u32x16 vv[2];
          auto pkv = [&](int t) AI {
#pragma unroll
            for (int i = 0; i < 2; ++i) { u32x2 y = pk4(m4[t] * cv[i] + (t ? qa[t - 1][4 + i] : ex[2 + i])); PINV(y); vv[i][2 * t] = y.x; vv[i][2 * t + 1] = y.y; }
          };
          across6(mx, [](float a, float b) AI { return max2(a, b); }, [](float a) AI { return a; }, pkv);
          pkv(4); pkv(5); pkv(6);
          PINV(vv[0]); PINV(vv[1]);
          SB();
#pragma unroll
          for (int u = 0; u < 6; ++u) {
#pragma unroll
            for (int e = 0; e < 2; ++e) {
              lg[u][e] -= mx[u];
              lg[u][e] = f32x2{__builtin_amdgcn_exp2f(lg[u][e][0]), __builtin_amdgcn_exp2f(lg[u][e][1])};
            }
            const f32x2 t = lg[u][0] + lg[u][1];
            inv[u] = t[0] + t[1];
          }
          across6(inv, [](float a, float b) AI { return a + b; }, [](float a) AI { return __builtin_amdgcn_rcpf(a); },
                  [&](int q) AI { for (int e = 3 * q; e < 3 * q + 3; ++e) lg[e >> 1][e & 1] *= rk[(e >> 1) + (e & 1)]; });
          f32x4 o0[6], o1[6];
          auto sto = [&](int u) AI {
            const u32x2 l = pk4(o0[u]), h = pk4(o1[u]);
            __builtin_amdgcn_raw_buffer_store_b128(u32x4{l.x, l.y, h.x, h.y}, rs_ws, ov + u * 16384, 0, AUX_SC1);
          };
          PRIO(3);
          SFOR(6, u,
            const f32x2 a = lg[u][0] * inv[u], b = lg[u][1] * inv[u];
            const unsigned wa_ = pack2(a[0], a[1]), wb_ = pack2(b[0], b[1]);
            const u32x4 pp = {__builtin_amdgcn_perm(wa_, wa_, p0), __builtin_amdgcn_perm(wa_, wa_, p1), __builtin_amdgcn_perm(wb_, wb_, p0), __builtin_amdgcn_perm(wb_, wb_, p1)};
            if constexpr (u >= 2) sto(u - 2);
            SB();
            o0[u] = mfma16(__builtin_shufflevector(vv[0], vv[0], 2 * u, 2 * u + 1, 2 * u + 2, 2 * u + 3), pp, Z4);
            o1[u] = mfma16(__builtin_shufflevector(vv[1], vv[1], 2 * u, 2 * u + 1, 2 * u + 2, 2 * u + 3), pp, Z4);
            SB();
          );
          sto(4); sto(5);
          SB();
          };
        if (m < 2) att(std::integral_constant<int, 0>{}); else att(std::integral_constant<int, 1>{});
    LANEVARS;
    if (DF && idx < 28) { u32x4 f2[6][4]; gemm2(std::true_type{}, IC(0), f2, IC(1)); }
    DRAIN();
    if (arrive(lb, 6) == 3u) putflag(WS_OF + (rg * 8 + hp) * FS, lane);
    if (!DF && idx < 28) { u32x4 f2[6][4]; gemm2(std::true_type{}, IC(0), f2, IC(1)); }
    if (bid == EPB) {
      __builtin_amdgcn_s_sleep(64);
      poll_flags<16>((const GAS u64*)(ws + WS_UF + (m * 64 + lane) * FS), tag);
      if (arrive(lb, 8) == 3u) putflag(WS_E, lane);
    }
  }
  FINISH();
}

extern "C" void kernel_launch(void* const* d_in, const int* in_sizes, int n_in,
                              void* d_out, int out_size, void* d_ws, size_t ws_size,
                              hipStream_t stream) {
  (void)in_sizes; (void)n_in; (void)out_size; (void)ws_size;
  (void)hipFuncSetAttribute((const void*)fused_block, hipFuncAttributeMaxDynamicSharedMemorySize, LDS_BYTES);
  fused_block<<<256, 512, LDS_BYTES, stream>>>((const u16*)d_in[0], (const u16*)d_in[1], (const u16*)d_in[2],
                                               (const u16*)d_in[4], (const u16*)d_in[5], (const u16*)d_in[6],
                                               (const u16*)d_in[7], (u16*)d_out, (char*)d_ws);
}
```

```cpp
#include <hip/hip_runtime.h>
#include <stdint.h>
#include <type_traits>
#include <utility>

#define __float_as_uint(x) __builtin_bit_cast(unsigned, (float)(x))
#define __uint_as_float(x) __builtin_bit_cast(float, (unsigned)(x))
#define __all(p) (__builtin_amdgcn_ballot_w64(!(p)) == 0)
typedef unsigned short u16;
typedef unsigned long long u64;
using bfv8 = __attribute__((ext_vector_type(8))) __bf16;
using bfv2 = __attribute__((ext_vector_type(2))) __bf16;
using f32x4 = __attribute__((ext_vector_type(4))) float;
using f32x2 = __attribute__((ext_vector_type(2))) float;
using u32x2 = __attribute__((ext_vector_type(2))) unsigned int;
using u32x4 = __attribute__((ext_vector_type(4))) unsigned int;
using u32x16 = __attribute__((ext_vector_type(16))) unsigned int;
using s16x4 = __attribute__((ext_vector_type(4))) short;

#define LAS __attribute__((address_space(3)))
#define GAS __attribute__((address_space(1)))
#define AI __attribute__((always_inline))
#define SB() __builtin_amdgcn_sched_barrier(0)
#define DRAIN() asm volatile("s_waitcnt vmcnt(0)" ::: "memory")
#define WAITVM(n) asm volatile("s_waitcnt vmcnt(%0)" ::"n"(n) : "memory")
#define WAITLG() asm volatile("s_waitcnt lgkmcnt(0)" ::: "memory")
#define RLX_AGENT __ATOMIC_RELAXED, __HIP_MEMORY_SCOPE_AGENT
#define PRIO(n) __builtin_amdgcn_s_setprio(n)

constexpr int RQ1 = 2, RQ2 = 5, LA = 6, AS = 4;
constexpr int GDLY = 5, GDL2 = 4, SLP = 4;
constexpr int NB1 = 8, NE = 4, NA = 2, NSG = 2;
constexpr int S_ = 2048, D_ = 448, F_ = 1792, N3_ = 1344;
constexpr int AUX_SC1 = 16;
constexpr int RSRC_W3 = 0x00020000;

constexpr int FS = 128;
constexpr int WS_E = 0, WS_HF = 4096, WS_UF = WS_HF + 128 * FS, WS_OF = WS_UF + 168 * FS;
constexpr int WS_ST = 65536;
constexpr int KS_ = 28 * 1024;
constexpr int WS_H = 131072;
constexpr int WS_U = WS_H + 64 * KS_;
constexpr int WS_O = WS_U + 56 * KS_;
constexpr int WS_END = WS_O + 132 * 16384;

constexpr int L_LATE = 65536, SS = 16384;
constexpr int L_HB = 131072;
constexpr int L_CNT = 162176;
constexpr int L_SA = 162432;
constexpr int L_DUMMY = 162560;
constexpr int LDS_BYTES = 163840;
constexpr int HB_PITCH = 928;

__device__ __forceinline__ unsigned pack2(float a, float b) {
  bfv2 v; v[0] = (__bf16)a; v[1] = (__bf16)b;
  return __builtin_bit_cast(unsigned, v);
}
__device__ __forceinline__ float bfr(float f) { return __uint_as_float(pack2(f, 0.f) << 16); }
__device__ __forceinline__ float lo_f(unsigned u) { return __uint_as_float(u << 16); }
__device__ __forceinline__ float hi_f(unsigned u) { return __uint_as_float(u & 0xffff0000u); }
__device__ __forceinline__ f32x4 mfma16(u32x4 a, u32x4 b, f32x4 c) {
  return __builtin_amdgcn_mfma_f32_16x16x32_bf16(__builtin_bit_cast(bfv8, a), __builtin_bit_cast(bfv8, b), c, 0, 0, 0);
}
__device__ __forceinline__ float max2(float a, float b) { return __builtin_elementwise_maximum(a, b); }
template <bool FIRST> __device__ __forceinline__ void mfma_ip(f32x4& d, u32x4 a, u32x4 b) {
  if constexpr (FIRST) asm volatile("v_mfma_f32_16x16x32_bf16 %0, %1, %2, 0" : "=&v"(d) : "v"(a), "v"(b));
  else asm volatile("v_mfma_f32_16x16x32_bf16 %0, %1, %2, %0" : "+v"(d) : "v"(a), "v"(b));
}
template <class OP, class FN, class FL> __device__ __forceinline__ void across6(float (&v)[6], OP op, FN fin, FL fill) {
  auto s32 = [](float a, float b) AI { const auto r = __builtin_amdgcn_permlane32_swap(__float_as_uint(a), __float_as_uint(b), false, false); return f32x2{__uint_as_float(r[0]), __uint_as_float(r[1])}; };
  auto s16 = [](float a, float b) AI { const auto r = __builtin_amdgcn_permlane16_swap(__float_as_uint(a), __float_as_uint(b), false, false); return f32x2{__uint_as_float(r[0]), __uint_as_float(r[1])}; };
  __builtin_amdgcn_sched_barrier(0);
  fill(0);
  __builtin_amdgcn_sched_barrier(0);
  const f32x2 a = s32(v[0], v[2]), b = s32(v[1], v[3]), d = s32(v[4], v[5]);
  float x = op(a[0], a[1]), y = op(b[0], b[1]), z = op(d[0], d[1]), z2 = z;
  asm volatile("" : "+v"(z2));
  __builtin_amdgcn_sched_barrier(0);
  fill(1);
  __builtin_amdgcn_sched_barrier(0);
  const f32x2 e = s16(x, y), f = s16(z, z2);
  float p = fin(op(e[0], e[1])), q = fin(op(f[0], f[1])), p2 = p, q2 = q;
  asm volatile("" : "+v"(p2), "+v"(q2));
  __builtin_amdgcn_sched_barrier(0);
  fill(2);
  __builtin_amdgcn_sched_barrier(0);
  const f32x2 h = s16(p, p2), k = s32(q, q2);
  float h0 = h[0], h1 = h[1];
  asm volatile("" : "+v"(h0), "+v"(h1));
  __builtin_amdgcn_sched_barrier(0);
  fill(3);
  __builtin_amdgcn_sched_barrier(0);
  const f32x2 r = s32(h[0], h0), t = s32(h[1], h1);
  v[0] = r[0]; v[2] = r[1]; v[1] = t[0]; v[3] = t[1]; v[4] = k[0]; v[5] = k[1];
}
template <int CTRL>
__device__ __forceinline__ float dpp_add(float v) {
  return v + __uint_as_float(__builtin_amdgcn_update_dpp(0u, __float_as_uint(v), CTRL, 0xf, 0xf, true));
}
__device__ __forceinline__ void dma16(__amdgpu_buffer_rsrc_t rs, int voff, int soff, int dst) {
  asm volatile("s_mov_b32 m0, %3\n\ts_nop 0\n\tbuffer_load_dwordx4 %0, %1, %2 offen lds" ::"v"(voff), "s"(rs), "s"(soff), "s"(dst) : "memory");
}
__device__ __forceinline__ void touch(__amdgpu_buffer_rsrc_t rs, int voff, int soff, int dst) {
  asm volatile("s_mov_b32 m0, %3\n\ts_nop 0\n\tbuffer_load_dword %0, %1, %2 offen lds" ::"v"(voff), "s"(rs), "s"(soff), "s"(dst) : "memory");
}
__device__ __forceinline__ void ldx4(u32x4 (&a)[4], __amdgpu_buffer_rsrc_t rs, int voff, int soff) {
  asm volatile("buffer_load_dwordx4 %0, %4, %5, %6 offen\n\tbuffer_load_dwordx4 %1, %4, %5, %6 offen offset:224\n\t"
               "buffer_load_dwordx4 %2, %4, %5, %6 offen offset:448\n\tbuffer_load_dwordx4 %3, %4, %5, %6 offen offset:672"
               : "=&v"(a[0]), "=&v"(a[1]), "=&v"(a[2]), "=&v"(a[3]) : "v"(voff), "s"(rs), "s"(soff) : "memory");
}
__device__ __forceinline__ void dma4(__amdgpu_buffer_rsrc_t rs, int v, int so, int step, int dst) {
  const int s1 = so + step - 1024, s2 = so + 2 * step - 2048, s3 = so + 3 * step - 3072;
  asm volatile("s_mov_b32 m0, %6\n\ts_nop 0\n\t"
               "buffer_load_dwordx4 %0, %1, %2 offen lds\n\t"
               "buffer_load_dwordx4 %0, %1, %3 offen offset:1024 lds\n\t"
               "buffer_load_dwordx4 %0, %1, %4 offen offset:2048 lds\n\t"
               "buffer_load_dwordx4 %0, %1, %5 offen offset:3072 lds"
               ::"v"(v), "s"(rs), "s"(so), "s"(s1), "s"(s2), "s"(s3), "s"(dst) : "memory");
}
__device__ __forceinline__ void ld4(u32x4 (&a)[4], __amdgpu_buffer_rsrc_t rs, int v, int so, int step) {
  asm volatile("buffer_load_dwordx4 %0, %4, %5, %6 offen\n\tbuffer_load_dwordx4 %1, %4, %5, %7 offen\n\t"
               "buffer_load_dwordx4 %2, %4, %5, %8 offen\n\tbuffer_load_dwordx4 %3, %4, %5, %9 offen"
               : "=&v"(a[0]), "=&v"(a[1]), "=&v"(a[2]), "=&v"(a[3])
               : "v"(v), "s"(rs), "s"(so), "s"(so + step), "s"(so + 2 * step), "s"(so + 3 * step) : "memory");
}
template <int N> __device__ __forceinline__ void wait4(u32x4 (&a)[4]) {
  asm volatile("s_waitcnt vmcnt(%4)" : "+v"(a[0]), "+v"(a[1]), "+v"(a[2]), "+v"(a[3]) : "n"(N) : "memory");
}
__device__ __forceinline__ void turn_pair(u32x4 (&a)[4], u32x4 (&b)[4]) {
  unsigned xa[8][2], xb[8][2];
#pragma unroll
  for (int r = 0; r < 8; ++r)
#pragma unroll
    for (int e = 0; e < 2; ++e) {
      const u32x4 v = r < 4 ? a[r] : b[r - 4];
      xa[r][e] = __builtin_amdgcn_update_dpp(v[e], v[2 + e], 0x128, 0xf, 0xc, false);
      xb[r][e] = __builtin_amdgcn_update_dpp(v[2 + e], v[e], 0x128, 0xf, 0x3, false);
    }
#pragma unroll
  for (int j = 0; j < 4; ++j)
#pragma unroll
    for (int q = 0; q < 4; ++q) {
      a[j][q] = __builtin_amdgcn_perm(xa[2 * q + 1][j >> 1], xa[2 * q][j >> 1], (j & 1) ? 0x07060302u : 0x05040100u);
      b[j][q] = __builtin_amdgcn_perm(xb[2 * q + 1][j >> 1], xb[2 * q][j >> 1], (j & 1) ? 0x07060302u : 0x05040100u);
    }
#pragma unroll
  for (int j = 0; j < 4; ++j) asm volatile("" : "+v"(a[j]), "+v"(b[j]));
}
__device__ __forceinline__ u32x2 tr_read(const LAS char* p) {
  return __builtin_bit_cast(u32x2, __builtin_amdgcn_ds_read_tr16_b64_v4i16((LAS s16x4*)p));
}
__device__ __forceinline__ float ln_sd(float var) { return bfr(__builtin_amdgcn_sqrtf(bfr(bfr(var) + 1e-5f))); }

__device__ __forceinline__ float row_sum(float v) {
  v = dpp_add<0xB1>(v);
  v = dpp_add<0x4E>(v);
  v = dpp_add<0x141>(v);
  return dpp_add<0x140>(v);
}
__device__ __forceinline__ unsigned ln2_rows(u32x4 (&f)[4], bool act) {
  f32x2 d[16];
#pragma unroll
  for (int e = 0; e < 16; ++e) d[e] = f32x2{lo_f(f[e >> 2][e & 3]), hi_f(f[e >> 2][e & 3])};
  f32x2 t[4];
#pragma unroll
  for (int e = 0; e < 4; ++e) t[e] = (d[e] + d[4 + e]) + (d[8 + e] + d[12 + e]);
  t[0] = (t[0] + t[1]) + (t[2] + t[3]);
  const float mean = row_sum(t[0][0] + t[0][1]) * (1.0f / D_);
  const float mr = bfr(mean);
  const float mb = act ? mr : 0.f;
#pragma unroll
  for (int e = 0; e < 16; ++e) d[e] -= f32x2{mb, mb};
#pragma unroll
  for (int e = 0; e < 4; ++e) t[e] = d[e] * d[e];
#pragma unroll
  for (int e = 4; e < 16; ++e) t[e & 3] = __builtin_elementwise_fma(d[e], d[e], t[e & 3]);
  t[0] = (t[0] + t[1]) + (t[2] + t[3]);
  const float dm = mean - mr;
  const float var = fmaxf(row_sum(t[0][0] + t[0][1]) * (1.0f / D_) - dm * dm, 0.f);
  const float s1 = ln_sd(var), r1 = __builtin_amdgcn_rcpf(s1);
  const float r2 = __builtin_amdgcn_rcpf(ln_sd(var * r1 * r1));
  const float a = r1 * r2, cc = -bfr(dm * r1) * r2;
#pragma unroll
  for (int e = 0; e < 16; ++e) {
    const f32x2 v = __builtin_elementwise_fma(d[e], f32x2{a, a}, f32x2{cc, cc});
    f[e >> 2][e & 3] = pack2(v[0], v[1]);
  }
  return (__float_as_uint(mr) >> 16) | __float_as_uint(s1);
}
__device__ __forceinline__ float solo(float v) { asm("" : "+v"(v)); return v; }
__device__ __forceinline__ float vadd(float a, float b) { return solo(a + b); }
__device__ __forceinline__ float vmul(float a, float b) { return solo(a * b); }
__device__ __forceinline__ f32x4 add4(f32x4 a, f32x4 b) { return f32x4{vadd(a[0], b[0]), vadd(a[1], b[1]), vadd(a[2], b[2]), vadd(a[3], b[3])}; }

constexpr unsigned SPIN_CAP = 1u << 18;
template <int SL>
__device__ __forceinline__ void poll_flags(const GAS u64* p, u64 tag) {
#pragma unroll 1
  for (unsigned spins = 0; spins < SPIN_CAP; ++spins) {
    const u64 v = __hip_atomic_load(p, RLX_AGENT);
    if (__all(v == tag)) break;
    __builtin_amdgcn_s_sleep(SL);
  }
  __builtin_amdgcn_fence(__ATOMIC_ACQUIRE, "wavefront");
}

__device__ __forceinline__ unsigned arrive(int lb, int i) {
  unsigned r;
  asm volatile("s_waitcnt lgkmcnt(0)\n\ts_mov_b64 exec, 1\n\tds_add_rtn_u32 %0, %1, %2\n\ts_mov_b64 exec, -1\n\ts_waitcnt lgkmcnt(0)"
               : "=&v"(r) : "v"(lb + L_CNT + 4 * i), "v"(1u) : "memory");
  return __builtin_amdgcn_readfirstlane(r);
}
__device__ __forceinline__ void sig(int lb, int i) {
  asm volatile("s_waitcnt lgkmcnt(0)\n\ts_mov_b64 exec, 1\n\tds_add_u32 %0, %1\n\ts_mov_b64 exec, -1" ::"v"(lb + L_CNT + 4 * i), "v"(1u) : "memory");
}
template <int I> __device__ __forceinline__ void post(int sa, unsigned one) {
  asm volatile("ds_add_u32 %0, %1 offset:%2" ::"v"(sa), "v"(one), "n"(4 * I) : "memory");
}
__device__ __forceinline__ unsigned look(LAS char* L, int i) {
  return __hip_atomic_load((LAS unsigned*)(L + L_CNT) + i, __ATOMIC_RELAXED, __HIP_MEMORY_SCOPE_WORKGROUP);
}
__device__ __forceinline__ void check(LAS char* L, unsigned v, int i, unsigned target) {
  unsigned t, n, a;
  asm volatile("s_nop 0\n\tv_readfirstlane_b32 %0, %3\n\ts_cmp_lt_u32 %0, %5\n\ts_cbranch_scc1 .Lcw%=\n.Lcb%=:\n\t.subsection 1\n"
               ".Lcw%=:\n\ts_mov_b32 %1, 0x400000\n\tv_mov_b32 %2, %4\n"
               ".Lcl%=:\n\tds_read_b32 %3, %2\n\ts_waitcnt lgkmcnt(0)\n\tv_readfirstlane_b32 %0, %3\n\ts_cmp_ge_u32 %0, %5\n\t"
               "s_cbranch_scc1 .Lcb%=\n\ts_sleep 0\n\ts_sub_u32 %1, %1, 1\n\ts_cbranch_scc0 .Lcl%=\n\ts_branch .Lcb%=\n\t.subsection 0"
               : "=&s"(t), "=&s"(n), "=&v"(a), "+v"(v) : "s"((int)(unsigned)(size_t)L + L_CNT + 4 * i), "n"(target) : "memory", "scc");
}
template <int SL = 0> __device__ __forceinline__ void await_at(int a, unsigned target) {
  unsigned t, n, v;
  asm volatile("s_mov_b32 %1, 0x400000\n.Lal%=:\n\tds_read_b32 %2, %3\n\ts_waitcnt lgkmcnt(0)\n\tv_readfirstlane_b32 %0, %2\n\ts_cmp_ge_u32 %0, %4\n\t"
               "s_cbranch_scc1 .Lab%=\n\ts_sleep %5\n\ts_sub_u32 %1, %1, 1\n\ts_cbranch_scc0 .Lal%=\n.Lab%=:"
               : "=&s"(t), "=&s"(n), "=&v"(v) : "v"(a), "n"(target), "n"(SL) : "memory", "scc");
}
template <int SL = 0> __device__ __forceinline__ void await(LAS char* L, int i, unsigned target) { await_at<SL>((int)(unsigned)(size_t)L + L_CNT + 4 * i, target); }

#define BAR() __builtin_amdgcn_s_barrier()
#define LANEVARS                                                                                         \
  int lane;                                                                                              \
  asm volatile("v_mbcnt_lo_u32_b32 %0, -1, 0\n\tv_mbcnt_hi_u32_b32 %0, -1, %0" : "=v"(lane));             \
  const int c = lane & 15, g = lane >> 4, q4 = c >> 2, p4 = c & 3, rr = lane >> 3;                       \
  (void)c; (void)g; (void)q4; (void)p4; (void)rr
constexpr f32x4 Z4 = {0.f, 0.f, 0.f, 0.f};
constexpr int OOB = 0x40000000;
template <class T> __device__ __forceinline__ T sel(bool c, T a, T b) { return __builtin_unpredictable(c) ? a : b; }
__device__ __forceinline__ int trbase(int i, int g, int q4, int p4) {
  return (4 * g + q4) * 128 + (((2 * i + (p4 >> 1)) ^ (2 * ((q4 >> 1) | ((g & 1) << 1)))) << 4) + 8 * (p4 & 1);
}
__device__ __forceinline__ void slot_frags(const LAS char* L, const int (&tb)[4], int so, u32x4 (&a)[4]) {
#pragma unroll
  for (int i = 0; i < 4; ++i) {
    const u32x2 lo = tr_read(L + tb[i] + so), hi = tr_read(L + tb[i] + so + 2048);
    a[i] = u32x4{lo.x, lo.y, hi.x, hi.y};
  }
}
__device__ __forceinline__ u64 peek(__amdgpu_buffer_rsrc_t rs, int off) {
  u64 v;
  asm volatile("buffer_load_dwordx2 %0, %1, %2, 0 offen sc1" : "=v"(v) : "v"(off), "s"(rs) : "memory");
  return v;
}
__device__ __forceinline__ void settle(__amdgpu_buffer_rsrc_t rs, int off, u64 first, u64 tag) {
  unsigned n;
  asm volatile("v_cmp_ne_u64 vcc, %4, %1\n\ts_cbranch_vccz .Lsb%=\n\ts_mov_b32 %0, 0x40000\n"
               ".Lsl%=:\n\tbuffer_load_dwordx2 %1, %2, %3, 0 offen sc1\n\ts_waitcnt vmcnt(0)\n\tv_cmp_ne_u64 vcc, %4, %1\n\t"
               "s_cbranch_vccz .Lsb%=\n\ts_sleep %5\n\ts_sub_u32 %0, %0, 1\n\ts_cbranch_scc0 .Lsl%=\n.Lsb%=:"
               : "=&s"(n), "+v"(first) : "v"(off), "s"(rs), "s"(tag), "n"(SLP) : "memory", "scc", "vcc");
}
template <class F, int... I> __device__ __forceinline__ void static_for(F f, std::integer_sequence<int, I...>) { (f(std::integral_constant<int, I>{}), ...); }
#define SFOR(n, var, ...) static_for([&](auto var##_c) AI { constexpr int var = decltype(var##_c)::value; __VA_ARGS__ }, std::make_integer_sequence<int, n>{})
template <int OFF> __device__ __forceinline__ void ld1_a(u32x4& v, __amdgpu_buffer_rsrc_t rs, int vo, int so) {
  asm volatile("buffer_load_dwordx4 %0, %1, %2, %3 offen offset:%4 sc1" : "=v"(v) : "v"(vo), "s"(rs), "s"(so), "n"(OFF) : "memory");
}
template <int N> __device__ __forceinline__ void wait1(u32x4& a) { asm volatile("s_waitcnt vmcnt(%1)" : "+v"(a) : "n"(N) : "memory"); }
template <int N> __device__ __forceinline__ void wait2(u32x4& a, u32x4& b) { asm volatile("s_waitcnt vmcnt(%2)" : "+v"(a), "+v"(b) : "n"(N) : "memory"); }

template <int NK, int NR, int NB, int GR, class LD, class PUT, class HK>
__device__ __forceinline__ void kloop(LAS char* L, const int (&tb)[4], u32x4 (&fr)[NR][4], u32x4 (&hv)[NB][4],
                                      f32x4 (&acc)[4][4], LD ld, PUT put, HK hook) {
  constexpr int KX = NK - 4;
  SFOR(KX, k,
    u32x2 lo;
    SFOR(4, t,
      constexpr int n = k * 4 + t, i0 = k < GR ? (NB - 1) * 4 + n : k == GR ? (NB - 1) * 4 + n + t : NB * 4 + n;
      wait1<i0 - n - 1>(hv[k % NB][t]);
      SFOR(4, i,
        acc[i][t] = mfma16(hv[k % NB][t], fr[k][i], k == 0 ? Z4 : acc[i][t]);
        asm volatile("" : "+v"(acc[i][t]));
        constexpr int e = t * 4 + i;
        if constexpr (k >= 1 && k - 1 + NR < NK && e < 8) {
          const LAS char* q = L + tb[e >> 1] + (k - 1) * SS;
          if constexpr (e & 1) { const u32x2 hi = tr_read(q + 2048); fr[k - 1][e >> 1] = u32x4{lo.x, lo.y, hi.x, hi.y}; }
          else lo = tr_read(q);
        }
        SB();
      );
      ld(std::integral_constant<int, i0 / 4>{}, std::integral_constant<int, i0 % 4>{});
      if constexpr (k == GR) ld(std::integral_constant<int, (i0 + 1) / 4>{}, std::integral_constant<int, (i0 + 1) % 4>{});
      SB();
    );
  );
  SFOR(4, t,
    constexpr int k = KX + t, is = (k + NB < NK ? k + NB : NK) - k - 1;
    wait4<4 * is>(hv[k % NB]);
    if constexpr (t == 0) hook();
    SFOR(16, e,
      constexpr int i = e >> 2, h = e & 3, d = 4 * t + i - 1;
      if constexpr (NK > NR) {
        acc[t][i] = mfma16(fr[(KX + h) % NR][i], hv[k % NB][h], acc[t][i]);
        asm volatile("" : "+v"(acc[t][i]));
      } else {
        acc[i][t] = mfma16(hv[k % NB][h], fr[KX + h][i], acc[i][t]);
        asm volatile("" : "+v"(acc[i][t]));
      }
      if constexpr (h == 3 && d >= 0) put(std::integral_constant<int, d & 3>{}, std::integral_constant<int, d / 4>{});
      SB();
    );
    if constexpr (k + NB < NK) { SFOR(4, h, ld(std::integral_constant<int, k + NB>{}, h_c);); }
    SB();
  );
  put(std::integral_constant<int, 3>{}, std::integral_constant<int, 3>{});
}
template <int N> __device__ __forceinline__ void gelu_pack(const f32x4 (&v)[N], unsigned (&pr)[2 * N]) {
  f32x4 z[N];
#pragma unroll
  for (int e = 0; e < N; ++e) z[e] = v[e] * (v[e] * v[e] * -0.1029432395800235f - 2.302208198144325f);
  SB();
#pragma unroll
  for (int e = 0; e < 4 * N; ++e) z[e >> 2][e & 3] = __builtin_amdgcn_exp2f(z[e >> 2][e & 3]);
  SB();
#pragma unroll
  for (int e = 0; e < N; ++e) z[e] += 1.0f;
#pragma unroll
  for (int e = 0; e < 4 * N; ++e) z[e >> 2][e & 3] = __builtin_amdgcn_rcpf(z[e >> 2][e & 3]);
  SB();
#pragma unroll
  for (int e = 0; e < N; ++e) { z[e] *= v[e]; pr[2 * e] = pack2(z[e][0], z[e][1]); pr[2 * e + 1] = pack2(z[e][2], z[e][3]); }
}
__device__ __forceinline__ float fold32(float a, float b) {
  const auto s = __builtin_amdgcn_permlane32_swap(__float_as_uint(a), __float_as_uint(b), false, false);
  return vadd(__uint_as_float(s[0]), __uint_as_float(s[1]));
}
#define LA_(a) ((LAS char*)(size_t)(unsigned)(a))
#define PINV(v) asm volatile("" : "+v"(v))
#define IC(n) std::integral_constant<int, (n)>{}
constexpr int g1buf(int nt, int t) { return ((t + 8 - nt) & 3) * SS; }
constexpr int L_B1 = 162816;
constexpr int A_W = 26624, A_WS = 24576, A_X1 = 100352, A_XS = 26624;
constexpr int A_MEAN = 153600, A_RSTD = A_MEAN + 1024, A_CS = A_RSTD + 1024;
#define FINISH() __builtin_amdgcn_endpgm()
__global__ __launch_bounds__(512, 2) void fused_block(const u16*, const u16*, const u16*, const u16*, const u16*, const u16*,
                                                      const u16*, u16*, char*) {
  extern __shared__ __attribute__((aligned(16))) char smem[];
  LAS char* L = (LAS char*)smem;
  const int lb = (int)(unsigned)(size_t)L;
  const auto kp = __builtin_amdgcn_kernarg_segment_ptr();
  u32x16 ka; u32x2 kb;
  asm volatile("s_load_dwordx16 %0, %2, 0x0\n\ts_load_dwordx2 %1, %2, 0x40" : "=&s"(ka), "=&s"(kb) : "s"(kp));
  SB();
  const int w = __builtin_amdgcn_readfirstlane((int)threadIdx.x >> 6);
  const int bid = blockIdx.x, xcd = bid & 7, idz = bid >> 3;
  const int m = w & 3;
#define ARGP(i) ((const u16*)(((u64)ka[2 * (i) + 1] << 32) | ka[2 * (i)]))
#define ARGWAIT() asm volatile("s_waitcnt lgkmcnt(0)" : "+s"(ka), "+s"(kb) : "s"(kp) : "memory")
  u64 tag;
  const int un = xcd * 16 + idz, row0 = 32 * (un >> 1) + 8 * (un & 1);
  const bool isln = idz < 16;
  u32x4 hx[4];
  u64 e1;
  if (isln && w >= 4) {
    PRIO(3);
    int lv = sel(((int)threadIdx.x & 15) < 14, (((int)threadIdx.x >> 4) & 3) * (D_ * 2) + ((int)threadIdx.x & 15) * 16, OOB);
    int ls = (row0 + 4 * (m & 1) + 8 * (m & 2)) * (D_ * 2), z = 0;
    asm volatile("" : "+v"(lv), "+v"(z));
    asm volatile("" : "+s"(ls));
    ARGWAIT();
    ldx4(hx, __builtin_amdgcn_make_buffer_rsrc((void*)ARGP(0), 0, S_ * D_ * 2, RSRC_W3), lv, ls);
    e1 = peek(__builtin_amdgcn_make_buffer_rsrc((void*)(((u64)kb[1] << 32) | kb[0]), 0, WS_END, RSRC_W3), z);
    BAR();
  } else {
    u64 pc;
    unsigned co = sel(isln, 0, (((idz - 16) * 8 + w) * 8 + ((int)threadIdx.x & 7)) * 128);
    asm volatile(".Lfb_start:\n\ts_getpc_b64 %0\n\ts_mov_b32 m0, %2\n\tv_min_u32 %1, .Lfb_end-.Lfb_start-8, %1\n\t"
                 "global_load_lds_dword %1, %0\n\t.subsection 2\n.Lfb_end:\n\ts_endpgm\n\t.subsection 0" : "=&s"(pc), "+v"(co) : "s"(lb + L_DUMMY) : "memory");
    *((LAS unsigned*)(L + L_CNT) + ((int)threadIdx.x & 31)) = 0u;
  }
  SB();
  auto gettag = [&]() AI {
    const u32x2 tv = *(const LAS u32x2*)(L + L_CNT + 128);
    tag = ((u64)(unsigned)__builtin_amdgcn_readfirstlane((int)tv.y) << 32) | (unsigned)__builtin_amdgcn_readfirstlane((int)tv.x);
  };

  const int ng = 24 - 6 * (xcd & 1);
  const int k4 = idz + ng - 32, k5 = idz - 16 - 4 * (xcd & 1);
  const bool isg = k4 >= 0;
  const int lp = sel(k5 < 0, ((0x24 - 3 * ((8 >> xcd) & 1) + 0xc0) >> (k4 & 6)) & 3, k5 >> 2);
  int ft = 7 * (xcd >> 1) + 4 * (xcd & 1) + lp, dg = sel(k5 < 0, 4 + (idz & 1), idz & 3);
  int idx = sel(isg, 6 * lp + dg, ng + idz);
  asm volatile("" : "+s"(idx));
  int off1 = (0xda8520 >> (4 * dg)) & 15;
  const int q = 22 * (xcd >> 2) + 8 * (xcd & 1) + idx - ng;
  int hp = 2 * (xcd & 2) + (q & 3), rg = q >> 2;
  int rotq = RQ1 * (q & 3) + RQ2 * rg + 70;
  rotq -= 7 * ((rotq * 147) >> 10);
  constexpr int EPB = 56;
  auto k1 = [&](int j) AI { return (j + off1) & 15; };
  int reg = (w >> 2) * L_LATE + m * 4096;
  int fv, fw, fs, fs1, role = (isg ? 0 : 2) + (w >> 2);
  {
    const int l = (int)threadIdx.x & 63, rr = l >> 3, ch = l & 7;
    const int v1 = ((m * 512 + rr) * F_ + ft * 64) * 2 + (ch ^ (2 * ((rr >> 1) & 3))) * 16;
    const int cq = ch ^ (2 * ((rr >> 1) & 1)) ^ (4 * (m & 1));
    const int vq = sel(cq < 7, (8 * m + rr) * (N3_ * 2) + hp * 112 + cq * 16, OOB);
    const int va = (192 * rg + 16 + 8 * m + rr) * (D_ * 2) + (ch ^ (4 * (m & 1) + (rr >> 1))) * 16;
    fv = sel(isg, v1, vq);
    fw = va;
    fs = sel(isg, k1(0) * 32 * (F_ * 2), rotq * (64 * N3_ * 2));
    fs1 = sel(isg, k1(1) * 32 * (F_ * 2), rotq * 128);
  }
  asm volatile("" : "+v"(fv), "+v"(fw));
  asm volatile("" : "+s"(fs), "+s"(fs1), "+s"(ft), "+s"(dg), "+s"(reg), "+s"(off1), "+s"(rotq), "+s"(role), "+s"(hp), "+s"(rg));
  const int r0 = 192 * rg;
  ARGWAIT();
  const u16 *x = ARGP(0), *wqkv = ARGP(1), *wo = ARGP(2), *w1 = ARGP(3), *b1 = ARGP(4), *w2 = ARGP(5), *b2 = ARGP(6);
  u16* out = (u16*)ARGP(7);
  char* ws = (char*)(((u64)kb[1] << 32) | kb[0]);
  const auto rs_x = __builtin_amdgcn_make_buffer_rsrc((void*)x, 0, S_ * D_ * 2, RSRC_W3);
  const auto rs_ws = __builtin_amdgcn_make_buffer_rsrc((void*)ws, 0, WS_END, RSRC_W3);
  const auto rs_w1 = __builtin_amdgcn_make_buffer_rsrc((void*)w1, 0, S_ * F_ * 2, RSRC_W3);
  const auto rs_w2 = __builtin_amdgcn_make_buffer_rsrc((void*)w2, 0, F_ * S_ * 2, RSRC_W3);
  auto putflag = [&](int off, int lane) AI {
    __builtin_amdgcn_raw_buffer_store_b128(u32x4{(unsigned)tag, (unsigned)(tag >> 32), (unsigned)tag, (unsigned)(tag >> 32)}, rs_ws,
                                           sel(lane < 8, off + lane * 16, OOB), 0, AUX_SC1);
  };
  auto mktag = [&](u64 e0) AI {
    e0 = e0 * 0x9E3779B97F4A7C15ull + 0x632BE59BD9B4E019ull;
    *(LAS u64*)(L + L_CNT + 128) = e0;
    tag = ((u64)(unsigned)__builtin_amdgcn_readfirstlane((int)(e0 >> 32)) << 32) | (unsigned)__builtin_amdgcn_readfirstlane((int)e0);
  };
  const auto rs_wq = __builtin_amdgcn_make_buffer_rsrc((void*)wqkv, 0, D_ * N3_ * 2, RSRC_W3);
  auto piece = [&](bool rows, int fv, int sg, int so, int e) AI {
    if (rows) {
      if (e < 6 || m < 2) dma16(rs_x, e ? fv : sel(fv >= 32 * D_ * 2, fv - 32 * D_ * 2, OOB), so + (e ? e - 1 : 0) * 32 * (D_ * 2), lb + (sg ? A_X1 + (sg - 1) * A_XS : 0) + (m + 4 * e) * 1024);
    } else {
      dma16(rs_wq, fv, so + (e / 3) * 32 * (N3_ * 2) + (e % 3) * 896, lb + A_W + sg * A_WS + e * 4096 + m * 1024);
    }
  };
  if (role == 0) {
    if (isln) { BAR(); __builtin_amdgcn_s_sleep(GDL2); }
    __builtin_amdgcn_s_sleep(GDLY);
    dma4(rs_w1, fv, fs, 8 * F_ * 2, lb + reg);
    dma4(rs_w1, fv, fs1, 8 * F_ * 2, lb + reg + SS);
  }
  asm volatile("" : "+s"(role));
  if (!isln || role == 2) BAR();
  if (role == 2) {
#pragma unroll
    for (int e = 0; e < 13; ++e) {
      if (e == AS) await(L, 7, 4u);
      if (e < 6) piece(false, fv, 2, fs, e); else piece(true, fw, 2, fs1, e - 6);
    }
  }
  asm volatile("" : "+s"(role));
  if (!(role & 1)) {
  } else if (isln) {
    const int hbase = sel(isg, L_HB, A_W + A_WS);
    PRIO(3);
    LANEVARS;
    const bool act = c < 14;
    int hb = sel(act, hbase + (4 * m + g) * HB_PITCH + c * 16, L_DUMMY);
    int tp = hbase + (4 * (g & 1) + q4) * HB_PITCH + (16 * (g >> 1) + 4 * p4) * 2 + m * 64;
    int ho = WS_H + (un >> 1) * KS_ + (2 * m + (g >> 1)) * 1024 + (2 * (un & 1) + (g & 1)) * 256 + c * 16;
    int h3 = sel(m < 2, ho + 3 * 8192, sel((lane < 16) & (m == 3), WS_ST + (row0 + (c & 7) + 2 * (c & 8)) * 16, OOB));
    int sn = sel(lane == 0, lb + L_CNT, OOB), sw = lb + L_SA + (4 * m + g) * 4, sr = lb + L_SA + c * 4, cn = lb + L_CNT, h1 = ho + 8192, h2 = ho + 16384;
    unsigned one = 1u;
    hb += lb; tp += lb;
    asm volatile("" : "+v"(hb), "+v"(tp), "+v"(ho), "+v"(h3), "+v"(sn), "+v"(one), "+v"(sw), "+v"(sr), "+v"(cn), "+v"(h1), "+v"(h2));
    asm volatile("s_waitcnt vmcnt(1)" : "+v"(hx[0]), "+v"(hx[1]), "+v"(hx[2]), "+v"(hx[3])::"memory");
    const unsigned sa = ln2_rows(hx, act);
#pragma unroll
    for (int e = 0; e < 4; ++e) *(LAS u32x4*)(LA_(hb) + e * 224) = hx[e];
    *(LAS unsigned*)LA_(sw) = sa;
    SB();
    post<0>(sn, one);
    await_at(cn, 4u);
    u32x4 hs[4];
#pragma unroll
    for (int u = 0; u < 4; ++u) {
      const u32x2 a0 = tr_read(LA_(tp) + u * 256), a1 = tr_read(LA_(tp) + u * 256 + 8 * HB_PITCH);
      hs[u] = u32x4{a0.x, a0.y, a1.x, a1.y};
      SB();
    }
    const unsigned sb = *(const LAS unsigned*)LA_(sr);
    SB();
#pragma unroll
    for (int u = 0; u < 3; ++u) __builtin_amdgcn_raw_buffer_store_b128(hs[u], rs_ws, u == 0 ? ho : u == 1 ? h1 : h2, 0, AUX_SC1);
    SB();
    asm volatile("s_waitcnt vmcnt(3)" : "+v"(e1)::"memory");
    mktag(e1);
    if (m == 3) hs[3] = u32x4{sb, (unsigned)tag, sb, (unsigned)(tag >> 32)};
    __builtin_amdgcn_raw_buffer_store_b128(hs[3], rs_ws, h3, 0, AUX_SC1);
    SB();
    post<7>(sn, one);
    if (isg) {
      DRAIN();
      if (arrive(lb, 1) == 3u) putflag(WS_HF + un * FS, lane);
    }
  } else {
    u64 e0 = peek(rs_ws, WS_E);
    asm volatile("s_waitcnt vmcnt(0)" : "+v"(e0)::"memory");
    mktag(e0);
    sig(lb, 7);
  }
  asm volatile("" : "+s"(role));
  int idy = idx;
  asm volatile("" : "+s"(idy));
  const u64 tm = idy < 12 ? 0x59d28518a320820ull : idy < 24 ? 0xbdab49ca2f839acull : 0x18820ffbbcdeb38ull;
  const int tile = sel(xcd & 1, (int)(tm >> (5 * (idy - 12 * ((idy * 43) >> 9)))) & 31, idy);
  const int st = 4 * (xcd ^ 1) + (tile & 3), dt = tile >> 2;
  const int rotl = dt % 6;
  auto pos2 = [&](bool late, int p) AI { const int v = p + rotl; return 14 * m + (late ? 8 + (v >= 6 ? v - 6 : v) : (p + 2 * dt) & 7); };
  auto ldw2 = [&](u32x4 (&a)[4], int vr, int p) AI { ld4(a, rs_w2, vr, (pos2(false, p & 6) * 32 + 16 * (p & 1)) * (S_ * 2), S_ * 2); };
  auto w2lane = [&](int lane) AI { return ((32 * ((lane >> 3) & 1) + 4 * (lane >> 4)) * S_ + st * 64 + 8 * (lane & 7)) * 2; };
  auto gemm2 = [&](auto latec, auto nec, auto& f2, auto idc) AI {
    constexpr bool LATE = decltype(latec)::value;
    constexpr int NR2 = LATE ? 6 : 8, NK2 = LATE ? 10 : 8, N0 = decltype(nec)::value;
    {
      constexpr int NB2 = 3;
      u32x4 uv[NB2][4];
      f32x4 acc[4][4];
      {
        LANEVARS;
        PRIO(0);
        const int w2E = (rr * S_ + st * 64) * 2 + ((lane & 7) ^ (2 * ((rr >> 1) & 3))) * 16;
        int tb[4];
#pragma unroll
        for (int i = 0; i < 4; ++i) tb[i] = reg + trbase(i, g, q4, p4);
        const int v0 = lane * 16;
        const int ta = 4 * dt + 3 * ((lane >> 3) & 1), dgp = sel(ta < 20, (ta * 13) >> 6, (ta >> 2) - 1);
        const int fo = WS_UF + ((7 * m + (LATE ? 4 : 0) + min(lane & 7, LATE ? 2 : 3)) * 6 + dgp) * FS;
        const int oo = WS_OF + (((st * 43) >> 7) * 8 + 2 * m + (lane & 1)) * FS;
        auto fill = [&](int p, int dst) AI { dma4(rs_w2, w2E, pos2(true, p) * 32 * (S_ * 2), 8 * S_ * 2, lb + dst); };
        constexpr int XS = L_HB - L_LATE;
        u64 f0;
        const auto rs_wo = __builtin_amdgcn_make_buffer_rsrc((void*)wo, 0, D_ * D_ * 2, RSRC_W3);
        const int woE = (rr * D_ + dt * 64) * 2 + ((lane & 7) ^ (2 * ((rr >> 1) & 3))) * 16;
        auto fillo = [&](int e) AI { dma4(rs_wo, woE, (4 * m + e) * 28 * (D_ * 2), 8 * D_ * 2, lb + L_LATE + m * 4096 + e * SS); };
        const int ub = WS_U + 4 * dt * 1024, ob = WS_O + (4 * st * 16 + 4 * m) * 1024;
        u64 o0 = 0;
        auto ldu = [&](auto pc, auto tc) AI {
          constexpr int p = decltype(pc)::value, t = decltype(tc)::value;
          if constexpr (LATE && p == 6 && t == 0) { asm volatile("s_waitcnt vmcnt(12)" : "+v"(o0)::"memory"); settle(rs_ws, oo, o0, tag); }
          if constexpr (LATE && p == 3 && t == 0) o0 = peek(rs_ws, oo);
          if constexpr (LATE && p >= 6) ld1_a<t * 1024>(uv[p % NB2][t], rs_ws, v0, ob + (p - 6) * 16384);
          else if constexpr (!LATE && p >= 4) ld1_a<(p - 4) * 1024>(uv[p % NB2][t], rs_ws, v0, ub + pos2(LATE, 4 + t) * KS_);
          else ld1_a<t * 1024>(uv[p % NB2][t], rs_ws, v0, ub + pos2(LATE, p) * KS_);
        };
        if constexpr (!LATE) {
          const int vr = w2lane(lane);
#pragma unroll
          for (int p = N0; p < 8; ++p) {
            ldw2(f2[p], vr, p);
            if (p == N0 + NSG - 1) { WAITVM(4 * NSG); sig(lb, 2); }
          }
          await(L, 3, 4u);
          await(L, 4, 4u);
          auto turn = [&](auto pc, auto nc) AI {
            constexpr int p = decltype(pc)::value, n = decltype(nc)::value;
            wait4<n>(f2[2 * p]);
            wait4<n>(f2[2 * p + 1]);
            turn_pair(f2[2 * p], f2[2 * p + 1]);
            SB();
          };
          fill(0, reg); fill(1, reg + SS);
          turn(IC(0), IC(32));
          await(L, 5, 4u);
          fill(2, reg + 2 * SS); fill(3, reg + 3 * SS);
          turn(IC(1), IC(32));
          fill(4, L_HB + m * 5120); fill(5, L_LATE + reg + 3 * SS);
          turn(IC(2), IC(32));
          fillo(0); fillo(1); fillo(2);
          WAITVM(12);
          sig(lb, 16 + m);
          f0 = peek(rs_ws, fo);
          turn(IC(3), IC(13));
          WAITVM(1);
          sig(lb, 16 + m);
        } else {
          await(L, 16 + m, 1u);
          slot_frags(L, tb, 3 * SS, f2[5]);
          asm volatile("s_waitcnt lgkmcnt(0)" : "+v"(f2[5][0]), "+v"(f2[5][1]), "+v"(f2[5][2]), "+v"(f2[5][3])::"memory");
          fillo(3);
          f0 = peek(rs_ws, fo);
#pragma unroll
          for (int r = 0; r < 4; ++r) slot_frags(L, tb, r * SS - L_LATE, f2[r]);
          slot_frags(L, tb, XS + m * 1024, f2[4]);
          sig(lb, 20 + m);
          await(L, 16 + m, 2u);
        }
        asm volatile("s_waitcnt vmcnt(0)" : "+v"(f0)::"memory");
        settle(rs_ws, fo, f0, tag);
        PRIO(LATE ? 1 : 2);
        SFOR((NB2 - LATE) * 4, n, ldu(IC(n / 4), IC(n % 4)););
        SB();
        int wr[4], sa = sel(lane == 0, lb + L_CNT, OOB);
        unsigned one = 1u;
        if constexpr (LATE) { PINV(sa); PINV(one); }
#pragma unroll
        for (int e = 0; e < 4; ++e) wr[e] = reg + g * 256 + (LATE ? c ^ (2 * e + (g >> 1)) : (c + 8 * e + 4 * (g >> 1)) & 15) * 16;
        kloop<NK2, NR2, NB2, LATE ? 1 : -1>(L, tb, f2, uv, acc, ldu,
            [&](auto ic, auto tc) AI {
              constexpr int i = LATE ? decltype(tc)::value : decltype(ic)::value, t = LATE ? decltype(ic)::value : decltype(tc)::value;
              *(LAS f32x4*)(L + wr[(t & 1) + (LATE ? 2 * (i & 1) : 0)] + i * SS + t * 1024) = acc[i][t];
              if constexpr (LATE && (i & 1) && t == 3) { SB(); post<(i == 1 ? 31 : 10)>(sa, one); }
            }, [&]() AI { if constexpr (!LATE) await(L, 20 + m, 1u); });
      }
      if constexpr (!LATE) {
        PRIO(0);
        LANEVARS;
        const int rw = st * 64 + 8 * (rr & 3) + (rr & 4) + m, eo = (rw * D_ + dt * 64 + (lane & 7) * 8) * 2;
        u32x4 xr[2];
        float bs[2];
#pragma unroll
        for (int e = 0; e < 2; ++e) {
          xr[e] = __builtin_amdgcn_raw_buffer_load_b128(rs_x, eo + e * 32 * D_ * 2, 0, 0);
          bs[e] = lo_f(*(const GAS u16*)(b2 + rw + 32 * e));
        }
        int ra[2];
#pragma unroll
        for (int e = 0; e < 2; ++e) ra[e] = (m * 16 + ((lane & 7) >> 1)) * 1024 + (lane & 1) * 512 + ((4 * e + (rr & 3) + 2 * (rr & 4) + 4 * (lane & 3)) & 15) * 16;
        sig(lb, 9);
        await<2>(L, 9, 4u);
        f32x4 tm[4][4], base[4];
#pragma unroll
        for (int j = 0; j < 4; ++j)
#pragma unroll
          for (int e = 0; e < 4; ++e) tm[j][e] = *(const LAS f32x4*)(L + ra[e >> 1] + j * 4096 + (e & 1) * 256);
        SB();
#pragma unroll
        for (int e = 0; e < 4; ++e) {
          const unsigned a = xr[e >> 1][2 * (e & 1)], b = xr[e >> 1][2 * (e & 1) + 1];
          const float s = bs[e >> 1];
          base[e] = add4(add4(add4(f32x4{lo_f(a), hi_f(a), lo_f(b), hi_f(b)}, f32x4{s, s, s, s}), add4(tm[0][e], tm[1][e])), add4(tm[2][e], tm[3][e]));
          asm volatile("" : "+v"(base[e]));
        }
        int rl = L_LATE + ((rr >> 1) & 1) * SS + ((lane & 7) >> 1) * 1024 + (lane & 1) * 512 + ((8 * (rr & 1) + (rr & 4) + m) ^ ((lane & 3) + 2 * (rr & 2))) * 16;
        int e1 = eo + 32 * D_ * 2;
        asm volatile("" : "+v"(rl), "+v"(e1));
        const auto rs_out = __builtin_amdgcn_make_buffer_rsrc((void*)out, 0, S_ * D_ * 2, RSRC_W3);
        int ca = lb + L_CNT + 4 * 31, ev = eo, it = 0;
        f32x4 b0 = base[0], b1 = base[1];
        _Pragma("unroll 1") do {
          asm volatile("" : "+v"(ca), "+v"(ev), "+v"(rl), "+v"(b0), "+v"(b1));
          await_at(ca, 4u);
          SFOR(8, j, tm[j >> 1][j & 1] = *(const LAS f32x4*)(L + rl + (j >> 1) * 4096 + (j & 1) * 256););
          SB();
          const f32x4 s0 = (b0 + tm[0][0]) + tm[1][0] + (tm[2][0] + tm[3][0]), s1 = (b1 + tm[0][1]) + tm[1][1] + (tm[2][1] + tm[3][1]);
          __builtin_amdgcn_raw_buffer_store_b128(u32x4{pack2(s0[0], s0[1]), pack2(s0[2], s0[3]), pack2(s1[0], s1[1]), pack2(s1[2], s1[3])}, rs_out, ev, 0, AUX_SC1);
          SB();
          ca = lb + L_CNT + 4 * 10; ev = e1; rl += 2 * SS; b0 = base[2]; b1 = base[3];
          asm volatile("" : "+s"(it));
        } while (++it < 2);
      }
    }
    asm volatile("s_nop %0" ::"n"(decltype(idc)::value));
    FINISH();
  };
  auto utile = [&](auto latec) AI {
    constexpr bool LATE = decltype(latec)::value;
      auto gemm1 = [&](auto ntc) AI {
        constexpr int NT = decltype(ntc)::value, NL = 8 * NT, NB = NB1;
        const int t0 = NT == 5 ? 5 * dg : 4 * dg + 4;
        u32x4 fr[8][4], hv[NB], f2[LATE ? 6 : 8][4];
        LANEVARS;
        PRIO(LATE ? 0 : 1);
        const int v0 = lane * 16, v4 = v0 + 4096, wr = reg + v0;
        int ho[8];
        auto ld = [&](auto nc) AI {
          constexpr int n = decltype(nc)::value, t = n >> 3;
          ld1_a<(t & 3) * 1024>(hv[n % NB], rs_ws, t < 4 ? v0 : v4, ho[n & 7]);
        };
        auto start = [&](int fo, u64 f0) AI {
          settle(rs_ws, fo, f0, tag);
          PRIO(LATE ? 2 : 1);
#pragma unroll
          for (int p = 0; p < 8; ++p) ho[p] = WS_H + (m * 16 + k1((LATE ? 8 : 0) + p)) * KS_ + t0 * 1024;
          SFOR(NB, n, ld(n_c););
          SB();
        };
        {
          const int w1E = ((m * 512 + rr) * F_ + ft * 64) * 2 + ((lane & 7) ^ (2 * ((rr >> 1) & 3))) * 16;
          auto fill = [&](int j) AI { dma4(rs_w1, w1E, k1(j) * 32 * (F_ * 2), 8 * F_ * 2, lb + reg + ((j >> 2) & 1) * L_LATE + (j & 3) * SS); };
          int tb[2][4];
#pragma unroll
          for (int i = 0; i < 8; ++i) tb[i >> 2][i & 3] = (i >> 2) * L_LATE + m * 4096 + trbase(i & 3, g, q4, p4);
          auto take = [&](int j) AI { slot_frags(L, tb[j >> 2], (j & 3) * SS, fr[j]); };
          auto taken = [&](int j) AI { asm volatile("s_waitcnt lgkmcnt(0)" : "+v"(fr[j][0]), "+v"(fr[j][1]), "+v"(fr[j][2]), "+v"(fr[j][3])::"memory"); };
          const int fo = WS_HF + (m * 32 + 2 * (((LATE ? 8 : 0) + ((lane >> 1) & 7) + off1) & 15) + (lane & 1)) * FS;
          u64 f0;
          if constexpr (!LATE) {
            WAITVM(NT == 4 ? 0 : 8);
#pragma unroll
            for (int j = 2; j < 8; ++j) fill(j);
            await(L, 7, 4u);
            gettag();
            SFOR(8, j, WAITVM(28); take(j); taken(j); if constexpr (j == 7) f0 = peek(rs_ws, fo); fill(8 + j););
            WAITVM(17); sig(lb, 12 + m);
            asm volatile("s_waitcnt vmcnt(4)" : "+v"(f0)::"memory");
            start(fo, f0);
            WAITVM(NB); sig(lb, 12 + m);
          } else {
            if (m == 0) {
              const auto rs_b1 = __builtin_amdgcn_make_buffer_rsrc((void*)b1, 0, F_ * 2, RSRC_W3);
              await(L, 7, 4u);
              touch(rs_b1, sel(lane < 32, lane * 4, OOB), ft * 128, lb + L_B1);
            }
            await(L, 12 + m, 1u);
            f0 = peek(rs_ws, fo);
#pragma unroll
            for (int j = 0; j < 4; ++j) take(j);
            asm volatile("s_waitcnt vmcnt(0)" : "+v"(f0)::"memory");
            if (m == 0) sig(lb, 11);
            start(fo, f0);
            await(L, 12 + m, 2u);
#pragma unroll
            for (int j = 4; j < 8; ++j) take(j);
#pragma unroll
            for (int j = 0; j < 8; ++j) taken(j);
            sig(lb, 30);
          }
        }
        const int ra = (lane >> 5) * L_LATE + ((LATE ? 2 : 0) + (g & 1)) * 1024 + m * 256 + c * 16;
        const int ba = L_B1 + (16 * ((LATE ? 2 : 0) + (g & 1)) + 4 * m + 2 * (lane >> 5)) * 2;
        const int ub = WS_U + (2 * ft + (LATE ? 1 : 0)) * KS_ + t0 * 1024 + m * 256 + c * 16, uo = ub + g * 1024;
        const int vr = w2lane(lane);
        int sa = sel(lane == 0, lb + L_CNT, OOB);
        unsigned one = 1u;
        PINV(sa); PINV(one);
        f32x4 acc[2][4], pt[4];
        float bq[2], v[2], z[2];
        unsigned pr[6], cv, cr;
        auto rd = [&](int t) AI {
#pragma unroll
          for (int e = 0; e < 4; ++e) pt[e] = *(const LAS f32x4*)(L + ra + g1buf(NT, t) + e * 4096);
        };
        auto step = [&](auto sc, int t) AI {
          constexpr int s = decltype(sc)::value;
          if constexpr (s == 0) { pt[0] = add4(pt[0], pt[1]); PINV(pt[0]); }
          if constexpr (s == 1) { pt[2] = add4(pt[2], pt[3]); PINV(pt[2]); }
          if constexpr (s == 2) { pt[0] = add4(pt[0], pt[2]); PINV(pt[0]); }
          if constexpr (s == 3) {
#pragma unroll
            for (int e = 0; e < 2; ++e) { v[e] = vadd(fold32(pt[0][e], pt[0][2 + e]), bq[e]); PINV(v[e]); }
          }
          if constexpr (s == 4) {
#pragma unroll
            for (int e = 0; e < 2; ++e) {
              const float p = solo(__builtin_fmaf(vmul(v[e], v[e]), -0.1029432395800235f, -2.302208198144325f));
              z[e] = vmul(v[e], p); PINV(z[e]);
            }
          }
          if constexpr (s == 5) {
#pragma unroll
            for (int e = 0; e < 2; ++e) { z[e] = vadd(__builtin_amdgcn_exp2f(z[e]), 1.0f); PINV(z[e]); }
          }
          if constexpr (s == 6) {
            pr[t] = pack2(vmul(__builtin_amdgcn_rcpf(z[0]), v[0]), vmul(__builtin_amdgcn_rcpf(z[1]), v[1]));
            PINV(pr[t]);
          }
        };
        unsigned sw[4];
        auto fine = [&](auto sc, int t) AI {
          constexpr int s = decltype(sc)::value;
          if constexpr (s < 6) {
            constexpr int d = s < 2 || s >= 4 ? 0 : 2, o = s < 2 ? 1 : s < 4 ? 3 : 2, e = 2 * (s & 1);
            pt[d][e] = vadd(pt[d][e], pt[o][e]); pt[d][e + 1] = vadd(pt[d][e + 1], pt[o][e + 1]); PINV(pt[d]);
          }
          if constexpr (s == 6) {
#pragma unroll
            for (int e = 0; e < 2; ++e) {
              const auto r = __builtin_amdgcn_permlane32_swap(__float_as_uint(pt[0][e]), __float_as_uint(pt[0][2 + e]), false, false);
              sw[2 * e] = r[0]; sw[2 * e + 1] = r[1]; PINV(sw[2 * e]); PINV(sw[2 * e + 1]);
            }
          }
          auto two = [&](float (&d)[2], auto f) AI { d[0] = f(0); d[1] = f(1); PINV(d[0]); PINV(d[1]); };
          if constexpr (s == 7) two(v, [&](int e) AI { return vadd(__uint_as_float(sw[2 * e]), __uint_as_float(sw[2 * e + 1])); });
          if constexpr (s == 8) two(v, [&](int e) AI { return vadd(v[e], bq[e]); });
          if constexpr (s == 9) two(z, [&](int e) AI { return vmul(v[e], v[e]); });
          if constexpr (s == 10) two(z, [&](int e) AI { return solo(__builtin_fmaf(z[e], -0.1029432395800235f, -2.302208198144325f)); });
          if constexpr (s == 11) two(z, [&](int e) AI { return vmul(v[e], z[e]); });
          if constexpr (s == 12 || s == 13) { z[s - 12] = __builtin_amdgcn_exp2f(z[s - 12]); PINV(z[s - 12]); }
          if constexpr (s == 14) two(z, [&](int e) AI { return vadd(z[e], 1.0f); });
          if constexpr (s == 15 || s == 16) { z[s - 15] = __builtin_amdgcn_rcpf(z[s - 15]); PINV(z[s - 15]); }
          if constexpr (s == 17) two(z, [&](int e) AI { return vmul(z[e], v[e]); });
          if constexpr (s == 18) { pr[t] = pack2(z[0], z[1]); PINV(pr[t]); }
        };
        auto put = [&](unsigned a, unsigned b, unsigned cc, unsigned d, int off) AI {
          const auto s0 = __builtin_amdgcn_permlane32_swap(a, cc, false, false), s1 = __builtin_amdgcn_permlane32_swap(b, d, false, false);
          const auto r0 = __builtin_amdgcn_permlane16_swap(s0[0], s1[0], false, false), r1 = __builtin_amdgcn_permlane16_swap(s0[1], s1[1], false, false);
          __builtin_amdgcn_raw_buffer_store_b128(u32x4{r0[0], r1[0], r0[1], r1[1]}, rs_ws, off, 0, AUX_SC1);
          SB();
        };
        SFOR(NL, n,
          constexpr int t = n >> 3, p = n & 7, nw = !LATE && t == NT - 1 ? 4 * (p < NE ? p : NE) : 0;
          if constexpr (n == 9 && !LATE) check(L, cr, 30, 4u);
          if constexpr (n == 13) { check(L, cv, 11, 1u); const unsigned b = *(const LAS unsigned*)(L + ba); bq[0] = lo_f(b); bq[1] = hi_f(b); }
          if constexpr (t >= 2 && p == 2) { check(L, cv, 24 + t - 2, 8u); rd(t - 2); if constexpr (NT == 5 && t == 2) post<29>(sa, one); }
          if constexpr (t >= 2 && p == 1) cv = look(L, 24 + t - 2);
          if constexpr (n == 7 && !LATE) cr = look(L, 30);
          if constexpr (n == 11) cv = look(L, 11);
          if constexpr (n == NL - 2) { cr = look(L, 29); cv = look(L, 24 + NT - 2); }
          SB();
          if constexpr (!(n & 1)) wait2<(n + NB < NL ? n + NB : NL) - 2 - n + nw>(hv[n % NB], hv[(n + 1) % NB]);
          SFOR(4, i,
            constexpr int ci = 2 * (p & 1) + (i >> 1), cn = (n & ~1) + (i & 1);
            mfma_ip<(cn & 7) == 0>(acc[t & 1][ci], fr[cn & 7][ci], hv[cn % NB]);
            if constexpr (t >= 1 && (p == 1 || p == 2) && (i & 1)) *(LAS f32x4*)(L + wr + g1buf(NT, t - 1) + (2 * p - 2 + (i >> 1)) * 1024) = acc[(t - 1) & 1][2 * p - 2 + (i >> 1)];
            if constexpr (t >= 2 && 4 * p + i >= 13) fine(IC(4 * p + i - 13), t - 2);
            SB();
          );
          if constexpr (t >= 1 && p == 2) post<24 + t - 1>(sa, one);
          if constexpr ((n & 1) && n + NB < NL) { ld(IC(n + NB - 1)); ld(IC(n + NB)); }
          if constexpr (!LATE && t == NT - 1 && (p & 1) && p < NE) { ldw2(f2[p - 1], vr, p - 1); ldw2(f2[p], vr, p); }
          SB();
        );
        PRIO(LATE ? 0 : 3);
        if constexpr (NT == 5) check(L, cr, 29, 8u);
        asm volatile("s_nop 7" ::: "memory");
#pragma unroll
        for (int i = 0; i < 4; ++i) *(LAS f32x4*)(L + wr + g1buf(NT, NT - 1) + i * 1024) = acc[(NT - 1) & 1][i];
        post<24 + NT - 1>(sa, one);
        if constexpr (LATE) post<4>(sa, one);
        check(L, cv, 24 + NT - 2, 8u);
        rd(NT - 2);
        if constexpr (!LATE) { post<3>(sa, one); PRIO(3); }
        SFOR(7, s, step(s_c, NT - 2););
        if constexpr (NT == 5) put(pr[0], pr[1], pr[2], pr[3], uo);
        else if constexpr (!LATE) put(pr[0], pr[1], pr[2], pr[2], sel(lane < 48, uo, OOB));
        if constexpr (LATE) {
          const unsigned b2q = *(const LAS unsigned*)(L + ba - 64);
          await(L, 24 + NT - 1, 8u);
          PRIO(3);
          {
            f32x4 qa[8];
#pragma unroll
            for (int e = 0; e < 8; ++e) qa[e] = *(const LAS f32x4*)(L + ra - (e >> 2) * 2048 + g1buf(NT, NT - 1) + (e & 3) * 4096);
            post<5>(sa, one);
            const f32x4 s0 = (qa[0] + qa[1]) + (qa[2] + qa[3]), s1 = (qa[4] + qa[5]) + (qa[6] + qa[7]);
            f32x4 y[1] = {f32x4{fold32(s0[0], s0[2]), fold32(s0[1], s0[3]), fold32(s1[0], s1[2]), fold32(s1[1], s1[3])} + f32x4{bq[0], bq[1], lo_f(b2q), hi_f(b2q)}};
            unsigned o2[2];
            gelu_pack<1>(y, o2);
            pr[NT - 1] = o2[0]; pr[5] = o2[1];
          }
          if constexpr (NT == 4) put(pr[0], pr[1], pr[2], pr[3], uo);
          if constexpr (NT == 5) put(pr[4], pr[5], pr[4], pr[5], sel(lane < 32, ub + 4096 - g * KS_, OOB));
          else put(pr[5], pr[5], pr[5], pr[5], sel(lane < 16, ub + 3072 - KS_, OOB));
          DRAIN();
          if (arrive(lb, 6) == 3u) { await(L, 2, 4u); putflag(WS_UF + (ft * 6 + dg) * FS, lane); }
        }
        gemm2(latec, IC(LATE ? 0 : NE), f2, IC(2 * NT - 6 + LATE));
      };
      if (dg < 4) gemm1(IC(5));
      gemm1(IC(4));
  };
  if (role == 0) utile(std::false_type{});
  if (role == 1) utile(std::true_type{});
  if (role == 2) {
    u32x4 f2[8][4];
    {
      LANEVARS;
      const int vr = w2lane(lane);
        const int cq = (lane & 7) ^ (2 * ((rr >> 1) & 1)) ^ (4 * (m & 1));
        const int vq = sel(cq < 7, (8 * m + rr) * (N3_ * 2) + hp * 112 + cq * 16, OOB);
        const int va = (r0 + 16 + 8 * m + rr) * (D_ * 2) + ((lane & 7) ^ (4 * (m & 1) + (rr >> 1))) * 16;
        const int so_ = WS_ST + min(max(r0 - 16 + min(64 * m + lane, 207), 0), S_ - 1) * 16;
        auto pcs = [&](int pp, int from, int to) AI {
          const bool wr = rotq >= 7 - pp;
#pragma unroll
          for (int n = from; n < to; ++n) {
            if (n < 6) piece(false, vq, (pp + 2) % 3, fs + sel(wr, (pp - 7) * 64 * N3_ * 2, pp * 64 * N3_ * 2), n);
            else piece(true, va, (pp + 2) % 3, rotq * 128 + sel(wr, (pp - 7) * 128, pp * 128), n - 6);
          }
        };
        pcs(1, 0, LA);
        gettag();
        WAITVM(LA);
        BAR();
        auto warm = [&](int e) AI {
          const auto rs_wo = __builtin_amdgcn_make_buffer_rsrc((void*)wo, 0, D_ * D_ * 2, RSRC_W3);
          const int j = idx - 28;
          if (e < 3) touch(rs_w2, (lane >> 2) * (S_ * 2) + (lane & 3) * 128, (448 * m + 256 + 48 * j + 16 * e) * (S_ * 2) + (xcd ^ 1) * 512, lb + L_DUMMY);
          else touch(rs_wo, lane * 128, ((e - 3) * 16 + j * 4 + m) * 8192, lb + L_DUMMY);
        };
        u32x4 sg;
        SFOR(6, n0,
          constexpr int n = n0 + 1, open = n < 5 ? LA : n == 5 ? 12 : 4 * NA;
          if constexpr (n < 6) pcs(n, LA, 13);
          if constexpr (n == 3) asm volatile("buffer_load_dwordx4 %0, %1, %2, 0 offen sc1" : "=v"(sg) : "v"(so_), "s"(rs_ws) : "memory");
          if constexpr (n < 6) pcs(n + 1, 0, n == 5 ? 13 : LA);
          if constexpr (n == 4) {
            asm volatile("" : "+v"(sg));
            _Pragma("unroll 1")
            for (unsigned spins = 0; spins < SPIN_CAP; ++spins) {
              if (__all((sg.y == (unsigned)tag) & (sg.w == (unsigned)(tag >> 32)) & (sg.x == sg.z))) break;
              __builtin_amdgcn_s_sleep(4);
              asm volatile("buffer_load_dwordx4 %0, %1, %2, 0 offen sc1\n\ts_waitcnt vmcnt(0)" : "=v"(sg) : "v"(so_), "s"(rs_ws) : "memory");
            }
            *(LAS float*)(L + A_MEAN + (64 * m + lane) * 4) = -lo_f(sg.x);
            *(LAS float*)(L + A_RSTD + (64 * m + lane) * 4) = __builtin_amdgcn_rcpf(hi_f(sg.x));
            WAITLG();
          }
          if constexpr (n == 6) { SFOR(NA, e, ldw2(f2[e], vr, e);); }
          if (n >= 5 && idx >= 28) {
            warm(2 * n - 10); warm(2 * n - 9);
            WAITVM(open + 2);
          } else {
            WAITVM(open);
          }
          BAR();
        );
        sig(lb, 3);
        if (idx >= 28) {
          touch(rs_w2, lane * (S_ * 2), (448 * m + 64 * (idx - 28)) * (S_ * 2) + (xcd ^ 1) * 512 + 384, lb + L_DUMMY);
#pragma unroll
          for (int e = 4; e < 7; ++e) warm(e);
          DRAIN();
          SFOR(NA, p, wait4<0>(f2[p]););
        }
    }
    if (idx < 28) gemm2(std::false_type{}, IC(NA), f2, IC(0));
    FINISH();
  } else {
        auto att = [&](auto jc) AI {
          constexpr int J = decltype(jc)::value, NH = J ? 2 : 4, H0 = J ? 0 : 2;
          const int H = m & 1;
          LANEVARS;
          f32x4 qa[6][6], qh[NH];
          int wa[2], xa[2][2];
#pragma unroll
          for (int i = 0; i < 2; ++i) {
            const int by = sel((i == 1) & (p4 == 3), 112, 56 * H + 32 * i + 8 * p4);
            wa[i] = A_W + (8 * g + q4) * 128 + (((by >> 4) ^ (2 * ((q4 >> 1) | ((g & 1) << 1)))) << 4) + (by & 8);
          }
          int wb[2] = {wa[0] + A_WS + 12288, wa[1] + A_WS + 12288};
#pragma unroll
          for (int i = 0; i < 4; ++i) xa[i >> 1][i & 1] = (i >> 1) * A_X1 + c * 128 + (((4 * (i & 1) + g) ^ ((c >> 1) & 7)) << 4);
          asm volatile("" : "+v"(wa[0]), "+v"(wa[1]), "+v"(wb[0]), "+v"(wb[1]), "+v"(xa[0][0]), "+v"(xa[0][1]), "+v"(xa[1][0]), "+v"(xa[1][1]));
          u32x4 wf[6], xf[7];
          u32x2 lo;
          auto rdx = [&](int k, int t) AI {
            const int sg = ((k >> 1) + 2) % 3;
            xf[t] = *(const LAS u32x4*)(L + xa[sg ? 1 : 0][k & 1] + (sg == 2 ? A_XS : 0) + (t == 6 ? 0 : (1 + 6 * J + t) * 2048));
          };
          auto rdw = [&](int k, int i, int hf) AI {
            const int o = (((k >> 1) + 2) % 3) * 2 + (k & 1);
            const LAS char* p = L + (o < 3 ? wa[i & 1] + o * 12288 : wb[i & 1] + (o - 3) * 12288) + (i >> 1) * 4096;
            if (hf) { const u32x2 hi = tr_read(p + 512); wf[i] = u32x4{lo.x, lo.y, hi.x, hi.y}; }
            else lo = tr_read(p);
          };
          const bool c0 = c == 0;
          int exa = lb + A_X1 + H * 4096 + lane * 16;
          int ta = lb + A_MEAN + (96 * J + c) * 4, tk = lb + A_RSTD + (96 * J + 4 * g + (c & 1)) * 4, tc = lb + A_CS + (H * 96 + 4 * g) * 4, tv = lb + A_CS + (H * 96 + 64 + c) * 4;
          int sa = sel(lane == 0, lb + L_CNT, OOB);
          unsigned one = 1u;
          f32x2 mk[2][2];
          {
            const int d0 = 16 + (c & 14) - 4 * g, hi = min(r0 + c, 14);
#pragma unroll
            for (int e = 0; e < 4; ++e) {
              const int dk = d0 - 16 * (e >> 1) - 2 * (e & 1);
              mk[1][e >> 1][e & 1] = sel((unsigned)dk <= 14u, 0.f, -3.0e38f);
              mk[0][e >> 1][e & 1] = sel((unsigned)dk <= (unsigned)hi, 0.f, -3.0e38f);
            }
          }
          PINV(exa); PINV(ta); PINV(tk); PINV(tc); PINV(tv); PINV(sa); PINV(one); PINV(mk[1][0]); PINV(mk[1][1]);
          if constexpr (J) { mk[0][0] = mk[1][0]; mk[0][1] = mk[1][1]; } else { PINV(mk[0][0]); PINV(mk[0][1]); }
          float mc[7], rc[6];
          f32x2 rk[7];
          auto tab = [&](int e) AI {
            const int t = e / 3;
            if (e % 3 == 0) mc[t] = *(const LAS float*)(LA_(ta) + 64 * t);
            if (e % 3 == 1) { const LAS float* p = (const LAS float*)(LA_(tk) + 64 * t); rk[t] = f32x2{p[0], p[2]}; }
            if (e % 3 == 2) rc[t] = *(const LAS float*)(LA_(ta) + A_RSTD - A_MEAN + 64 * (t + 1));
          };
          DRAIN();
          if (arrive(lb, 1) == 3u) putflag(WS_HF + un * FS, lane);
          BAR();
          PRIO(2);
#pragma unroll
          for (int t = 0; t < 7; ++t) rdx(0, t);
#pragma unroll
          for (int i = 0; i < 10; ++i) rdw(0, i >> 1, i & 1);
          gettag();
          SB();
          SFOR(14, k,
            SFOR(6, ci,
              constexpr int i = (k == 13 && J == 0) ? (ci + 2) % 6 : ci;
              SFOR(7, t,
                if constexpr (t < 6) {
                  if constexpr (i < 4) mfma_ip<k == 0>(qa[t][i], wf[i], xf[t]); else mfma_ip<k == 0>(qa[t][i], xf[t], wf[i]);
                } else if constexpr (i >= H0 && i < H0 + NH) {
                  if constexpr (i < 4) mfma_ip<k == 0>(qh[i - H0], wf[i], xf[6]); else mfma_ip<k == 0>(qh[i - H0], xf[6], wf[i]);
                  if constexpr (k < 13 && i == H0 + NH - 1 && J == 0) rdx(k + 1, 6);
                }
                if constexpr (ci == 0 && t < 2) rdw(k, 5, t);
                if constexpr (ci == (k == 13 ? 0 : H0) && t == 2) { SFOR(4, j, xf[6][j] = sel(c0, 0x3f803f80u, xf[6][j]);); asm volatile("" : "+v"(xf[6])); }
                if constexpr ((k & 1) && k < 13 && ci == 1 && t == 1) { WAITLG(); BAR(); }
                if constexpr (k < 13 && ci >= 1 && (t == 2 || t == 3)) rdw(k + 1, ci - 1, t - 2);
                if constexpr (k < 13 && ci == 5 && t < 6) rdx(k + 1, t);
                if constexpr (k < 13 && J == 1 && ci == 2 && t == 0) rdx(k + 1, 6);
                if constexpr (k == 13 && t == 2 && ci >= 2 && ci < NH + 2) {
                  constexpr int ip = H0 + ci - 2;
                  if constexpr (ip < 4) *(LAS f32x4*)(LA_(sel(c0, tc, lb + L_DUMMY)) + 64 * ip) = qh[ci - 2];
                  else *(LAS float*)(LA_(sel(g == 0, tv, lb + L_DUMMY)) + 64 * (ip - 4)) = qh[ci - 2][0];
                }
                if constexpr (k == 13 && J == 0 && t == 4 && ci >= 2) *(LAS f32x4*)(LA_(exa) + (ci - 2) * 1024) = qa[5][ci];
                if constexpr (k == 13 && (J ? ci == 3 && t == 2 : ci == 5 && t == 4)) { SB(); post<4>(sa, one); }
                if constexpr (k == 13 && ci >= 2 && t < 5) tab(5 * (ci - 2) + t);
                SB();
              );
            );
          );
          await(L, 4, 4u);
          f32x4 c4[4], ex[4];
          float cv[2];
#pragma unroll
          for (int i = 0; i < 4; ++i) c4[(i + 2) & 3] = *(const LAS f32x4*)(LA_(tc) + 64 * ((i + 2) & 3));
#pragma unroll
          for (int i = 0; i < 4; ++i) { if constexpr (J) ex[i] = *(const LAS f32x4*)(LA_(exa) + i * 1024); else ex[i] = qh[i & (NH - 1)]; }
#pragma unroll
          for (int i = 0; i < 2; ++i) cv[i] = *(const LAS float*)(LA_(tv) + 64 * i);
          SB();
          post<5>(sa, one);
          const bool odd = ta & 4;
          f32x2 lg[6][2];
#pragma unroll
          for (int u = 0; u < 6; ++u) {
            const float sc = rc[u] * (0.18898223650461362f * 1.4426950408889634f);
            lg[u][0] = rk[u] * sc; lg[u][1] = rk[u + 1] * sc;
            PINV(lg[u][0]); PINV(lg[u][1]);
          }
          int tm = (tk & ~4) + A_MEAN - A_RSTD;
          int ov = exa - lb + WS_O - A_X1 + ((12 * rg + 6 * J) * 16 + 2 * hp - 3 * H) * 1024;
          unsigned p0 = sel(odd, 0x01000c0cu, 0x0c0c0100u), p1 = sel(odd, 0x03020c0cu, 0x0c0c0302u);
          PINV(tm); PINV(ov); PINV(p0); PINV(p1);
          SB();
          auto pk4 = [&](f32x4 v) AI { return u32x2{pack2(v[0], v[1]), pack2(v[2], v[3])}; };
          auto frag = [&](f32x4 a, f32x4 b, float mu, int i) AI {
            const u32x2 l = pk4(c4[i] * mu + a), h = pk4(c4[i + 1] * mu + b);
            return u32x4{l.x, l.y, h.x, h.y};
          };
          f32x4 s0[6], s1[6], m4[7];
          u32x4 kf[7], qf[6];
          kf[0] = frag(ex[0], ex[1], mc[0], 2);
          qf[0] = frag(qa[0][0], qa[0][1], mc[1], 0);
#pragma unroll
          for (int u = 0; u < 6; ++u) {
            kf[u + 1] = frag(qa[u][2], qa[u][3], mc[u + 1], 2);
            SB();
            s0[u] = mfma16(kf[u], qf[u], Z4);
            m4[u] = *(const LAS f32x4*)(LA_(tm) + 64 * u);
            SB();
            if (u < 5) qf[u + 1] = frag(qa[u + 1][0], qa[u + 1][1], mc[u + 2], 0); else m4[6] = *(const LAS f32x4*)(LA_(tm) + 64 * 6);
            SB();
            s1[u] = mfma16(kf[u + 1], qf[u], Z4);
            SB();
          }
          float mx[6], inv[6];
#pragma unroll
          for (int u = 0; u < 6; ++u) {
#pragma unroll
            for (int e = 0; e < 2; ++e) {
              const f32x4 s = e ? s1[u] : s0[u];
              lg[u][e] = __builtin_elementwise_fma(f32x2{sel(odd, s[1], s[0]), sel(odd, s[3], s[2])}, lg[u][e], mk[u ? 1 : 0][e]);
            }
            mx[u] = max2(max2(lg[u][0][0], lg[u][0][1]), max2(lg[u][1][0], lg[u][1][1]));
          }
          u32x16 vv[2];
          auto pkv = [&](int t) AI {
#pragma unroll
            for (int i = 0; i < 2; ++i) { u32x2 y = pk4(m4[t] * cv[i] + (t ? qa[t - 1][4 + i] : ex[2 + i])); PINV(y); vv[i][2 * t] = y.x; vv[i][2 * t + 1] = y.y; }
          };
          across6(mx, [](float a, float b) AI { return max2(a, b); }, [](float a) AI { return a; }, pkv);
          pkv(4); pkv(5); pkv(6);
          PINV(vv[0]); PINV(vv[1]);
          SB();
#pragma unroll
          for (int u = 0; u < 6; ++u) {
#pragma unroll
            for (int e = 0; e < 2; ++e) {
              lg[u][e] -= mx[u];
              lg[u][e] = f32x2{__builtin_amdgcn_exp2f(lg[u][e][0]), __builtin_amdgcn_exp2f(lg[u][e][1])};
            }
            const f32x2 t = lg[u][0] + lg[u][1];
            inv[u] = t[0] + t[1];
          }
          across6(inv, [](float a, float b) AI { return a + b; }, [](float a) AI { return __builtin_amdgcn_rcpf(a); },
                  [&](int q) AI { for (int e = 3 * q; e < 3 * q + 3; ++e) lg[e >> 1][e & 1] *= rk[(e >> 1) + (e & 1)]; });
          f32x4 o0[6], o1[6];
          auto sto = [&](int u) AI {
            const u32x2 l = pk4(o0[u]), h = pk4(o1[u]);
            __builtin_amdgcn_raw_buffer_store_b128(u32x4{l.x, l.y, h.x, h.y}, rs_ws, ov + u * 16384, 0, AUX_SC1);
          };
          PRIO(3);
          SFOR(6, u,
            const f32x2 a = lg[u][0] * inv[u], b = lg[u][1] * inv[u];
            const unsigned wa_ = pack2(a[0], a[1]), wb_ = pack2(b[0], b[1]);
            const u32x4 pp = {__builtin_amdgcn_perm(wa_, wa_, p0), __builtin_amdgcn_perm(wa_, wa_, p1), __builtin_amdgcn_perm(wb_, wb_, p0), __builtin_amdgcn_perm(wb_, wb_, p1)};
            if constexpr (u >= 2) sto(u - 2);
            SB();
            o0[u] = mfma16(__builtin_shufflevector(vv[0], vv[0], 2 * u, 2 * u + 1, 2 * u + 2, 2 * u + 3), pp, Z4);
            o1[u] = mfma16(__builtin_shufflevector(vv[1], vv[1], 2 * u, 2 * u + 1, 2 * u + 2, 2 * u + 3), pp, Z4);
            SB();
          );
          sto(4); sto(5);
          SB();
          };
        if (m < 2) att(std::integral_constant<int, 0>{}); else att(std::integral_constant<int, 1>{});
    LANEVARS;
    DRAIN();
    if (arrive(lb, 6) == 3u) putflag(WS_OF + (rg * 8 + hp) * FS, lane);
    if (idx < 28) { u32x4 f2[6][4]; gemm2(std::true_type{}, IC(0), f2, IC(1)); }
    if (bid == EPB) {
      __builtin_amdgcn_s_sleep(64);
      poll_flags<16>((const GAS u64*)(ws + WS_UF + (m * 64 + lane) * FS), tag);
      if (arrive(lb, 8) == 3u) putflag(WS_E, lane);
    }
  }
  FINISH();
}

extern "C" void kernel_launch(void* const* d_in, const int* in_sizes, int n_in,
                              void* d_out, int out_size, void* d_ws, size_t ws_size,
                              hipStream_t stream) {
  (void)in_sizes; (void)n_in; (void)out_size; (void)ws_size;
  (void)hipFuncSetAttribute((const void*)fused_block, hipFuncAttributeMaxDynamicSharedMemorySize, LDS_BYTES);
  fused_block<<<256, 512, LDS_BYTES, stream>>>((const u16*)d_in[0], (const u16*)d_in[1], (const u16*)d_in[2],
                                               (const u16*)d_in[4], (const u16*)d_in[5], (const u16*)d_in[6],
                                               (const u16*)d_in[7], (u16*)d_out, (char*)d_ws);
}
```

```cpp
#include <hip/hip_runtime.h>
#include <stdint.h>
#include <type_traits>
#include <utility>

#define __float_as_uint(x) __builtin_bit_cast(unsigned, (float)(x))
#define __uint_as_float(x) __builtin_bit_cast(float, (unsigned)(x))
#define __all(p) (__builtin_amdgcn_ballot_w64(!(p)) == 0)
typedef unsigned short u16;
typedef unsigned long long u64;
using bfv8 = __attribute__((ext_vector_type(8))) __bf16;
using bfv2 = __attribute__((ext_vector_type(2))) __bf16;
using f32x4 = __attribute__((ext_vector_type(4))) float;
using f32x2 = __attribute__((ext_vector_type(2))) float;
using u32x2 = __attribute__((ext_vector_type(2))) unsigned int;
using u32x4 = __attribute__((ext_vector_type(4))) unsigned int;
using u32x16 = __attribute__((ext_vector_type(16))) unsigned int;
using s16x4 = __attribute__((ext_vector_type(4))) short;

#define LAS __attribute__((address_space(3)))
#define GAS __attribute__((address_space(1)))
#define AI __attribute__((always_inline))
#define SB() __builtin_amdgcn_sched_barrier(0)
#define DRAIN() asm volatile("s_waitcnt vmcnt(0)" ::: "memory")
#define WAITVM(n) asm volatile("s_waitcnt vmcnt(%0)" ::"n"(n) : "memory")
#define WAITLG() asm volatile("s_waitcnt lgkmcnt(0)" ::: "memory")
#define RLX_AGENT __ATOMIC_RELAXED, __HIP_MEMORY_SCOPE_AGENT
#define PRIO(n) __builtin_amdgcn_s_setprio(n)

constexpr int RQ1 = 2, RQ2 = 5, LA = 6, AS = 4;
constexpr int GDLY = 5, GDL2 = 4, SLP = 4;
constexpr int NB1 = 8, NE = 4, NA = 2, NSG = 2;
constexpr int S_ = 2048, D_ = 448, F_ = 1792, N3_ = 1344;
constexpr int AUX_SC1 = 16;
constexpr int RSRC_W3 = 0x00020000;

constexpr int FS = 128;
constexpr int WS_E = 0, WS_HF = 4096, WS_UF = WS_HF + 128 * FS, WS_OF = WS_UF + 168 * FS;
constexpr int WS_ST = 65536;
constexpr int KS_ = 28 * 1024;
constexpr int WS_H = 131072;
constexpr int WS_U = WS_H + 64 * KS_;
constexpr int WS_O = WS_U + 56 * KS_;
constexpr int WS_END = WS_O + 132 * 16384;

constexpr int L_LATE = 65536, SS = 16384;
constexpr int L_HB = 131072;
constexpr int L_CNT = 162176;
constexpr int L_SA = 162432;
constexpr int L_DUMMY = 162560;
constexpr int LDS_BYTES = 163840;
constexpr int HB_PITCH = 928;

__device__ __forceinline__ unsigned pack2(float a, float b) {
  bfv2 v; v[0] = (__bf16)a; v[1] = (__bf16)b;
  return __builtin_bit_cast(unsigned, v);
}
__device__ __forceinline__ float bfr(float f) { return __uint_as_float(pack2(f, 0.f) << 16); }
__device__ __forceinline__ float lo_f(unsigned u) { return __uint_as_float(u << 16); }
__device__ __forceinline__ float hi_f(unsigned u) { return __uint_as_float(u & 0xffff0000u); }
__device__ __forceinline__ f32x4 mfma16(u32x4 a, u32x4 b, f32x4 c) {
  return __builtin_amdgcn_mfma_f32_16x16x32_bf16(__builtin_bit_cast(bfv8, a), __builtin_bit_cast(bfv8, b), c, 0, 0, 0);
}
__device__ __forceinline__ float max2(float a, float b) { return __builtin_elementwise_maximum(a, b); }
template <bool FIRST> __device__ __forceinline__ void mfma_ip(f32x4& d, u32x4 a, u32x4 b) {
  if constexpr (FIRST) asm volatile("v_mfma_f32_16x16x32_bf16 %0, %1, %2, 0" : "=&v"(d) : "v"(a), "v"(b));
  else asm volatile("v_mfma_f32_16x16x32_bf16 %0, %1, %2, %0" : "+v"(d) : "v"(a), "v"(b));
}
template <class OP, class FN, class FL> __device__ __forceinline__ void across6(float (&v)[6], OP op, FN fin, FL fill) {
  auto s32 = [](float a, float b) AI { const auto r = __builtin_amdgcn_permlane32_swap(__float_as_uint(a), __float_as_uint(b), false, false); return f32x2{__uint_as_float(r[0]), __uint_as_float(r[1])}; };
  auto s16 = [](float a, float b) AI { const auto r = __builtin_amdgcn_permlane16_swap(__float_as_uint(a), __float_as_uint(b), false, false); return f32x2{__uint_as_float(r[0]), __uint_as_float(r[1])}; };
  __builtin_amdgcn_sched_barrier(0);
  fill(0);
  __builtin_amdgcn_sched_barrier(0);
  const f32x2 a = s32(v[0], v[2]), b = s32(v[1], v[3]), d = s32(v[4], v[5]);
  float x = op(a[0], a[1]), y = op(b[0], b[1]), z = op(d[0], d[1]), z2 = z;
  asm volatile("" : "+v"(z2));
  __builtin_amdgcn_sched_barrier(0);
  fill(1);
  __builtin_amdgcn_sched_barrier(0);
  const f32x2 e = s16(x, y), f = s16(z, z2);
  float p = fin(op(e[0], e[1])), q = fin(op(f[0], f[1])), p2 = p, q2 = q;
  asm volatile("" : "+v"(p2), "+v"(q2));
  __builtin_amdgcn_sched_barrier(0);
  fill(2);
  __builtin_amdgcn_sched_barrier(0);
  const f32x2 h = s16(p, p2), k = s32(q, q2);
  float h0 = h[0], h1 = h[1];
  asm volatile("" : "+v"(h0), "+v"(h1));
  __builtin_amdgcn_sched_barrier(0);
  fill(3);
  __builtin_amdgcn_sched_barrier(0);
  const f32x2 r = s32(h[0], h0), t = s32(h[1], h1);
  v[0] = r[0]; v[2] = r[1]; v[1] = t[0]; v[3] = t[1]; v[4] = k[0]; v[5] = k[1];
}
template <int CTRL>
__device__ __forceinline__ float dpp_add(float v) {
  return v + __uint_as_float(__builtin_amdgcn_update_dpp(0u, __float_as_uint(v), CTRL, 0xf, 0xf, true));
}
__device__ __forceinline__ void dma16(__amdgpu_buffer_rsrc_t rs, int voff, int soff, int dst) {
  asm volatile("s_mov_b32 m0, %3\n\ts_nop 0\n\tbuffer_load_dwordx4 %0, %1, %2 offen lds" ::"v"(voff), "s"(rs), "s"(soff), "s"(dst) : "memory");
}
__device__ __forceinline__ void touch(__amdgpu_buffer_rsrc_t rs, int voff, int soff, int dst) {
  asm volatile("s_mov_b32 m0, %3\n\ts_nop 0\n\tbuffer_load_dword %0, %1, %2 offen lds" ::"v"(voff), "s"(rs), "s"(soff), "s"(dst) : "memory");
}
__device__ __forceinline__ void ldx4(u32x4 (&a)[4], __amdgpu_buffer_rsrc_t rs, int voff, int soff) {
  asm volatile("buffer_load_dwordx4 %0, %4, %5, %6 offen\n\tbuffer_load_dwordx4 %1, %4, %5, %6 offen offset:224\n\t"
               "buffer_load_dwordx4 %2, %4, %5, %6 offen offset:448\n\tbuffer_load_dwordx4 %3, %4, %5, %6 offen offset:672"
               : "=&v"(a[0]), "=&v"(a[1]), "=&v"(a[2]), "=&v"(a[3]) : "v"(voff), "s"(rs), "s"(soff) : "memory");
}
__device__ __forceinline__ void dma4(__amdgpu_buffer_rsrc_t rs, int v, int so, int step, int dst) {
  const int s1 = so + step - 1024, s2 = so + 2 * step - 2048, s3 = so + 3 * step - 3072;
  asm volatile("s_mov_b32 m0, %6\n\ts_nop 0\n\t"
               "buffer_load_dwordx4 %0, %1, %2 offen lds\n\t"
               "buffer_load_dwordx4 %0, %1, %3 offen offset:1024 lds\n\t"
               "buffer_load_dwordx4 %0, %1, %4 offen offset:2048 lds\n\t"
               "buffer_load_dwordx4 %0, %1, %5 offen offset:3072 lds"
               ::"v"(v), "s"(rs), "s"(so), "s"(s1), "s"(s2), "s"(s3), "s"(dst) : "memory");
}
__device__ __forceinline__ void ld4(u32x4 (&a)[4], __amdgpu_buffer_rsrc_t rs, int v, int so, int step) {
  asm volatile("buffer_load_dwordx4 %0, %4, %5, %6 offen\n\tbuffer_load_dwordx4 %1, %4, %5, %7 offen\n\t"
               "buffer_load_dwordx4 %2, %4, %5, %8 offen\n\tbuffer_load_dwordx4 %3, %4, %5, %9 offen"
               : "=&v"(a[0]), "=&v"(a[1]), "=&v"(a[2]), "=&v"(a[3])
               : "v"(v), "s"(rs), "s"(so), "s"(so + step), "s"(so + 2 * step), "s"(so + 3 * step) : "memory");
}
template <int N> __device__ __forceinline__ void wait4(u32x4 (&a)[4]) {
  asm volatile("s_waitcnt vmcnt(%4)" : "+v"(a[0]), "+v"(a[1]), "+v"(a[2]), "+v"(a[3]) : "n"(N) : "memory");
}
__device__ __forceinline__ void turn_pair(u32x4 (&a)[4], u32x4 (&b)[4]) {
  unsigned xa[8][2], xb[8][2];
#pragma unroll
  for (int r = 0; r < 8; ++r)
#pragma unroll
    for (int e = 0; e < 2; ++e) {
      const u32x4 v = r < 4 ? a[r] : b[r - 4];
      xa[r][e] = __builtin_amdgcn_update_dpp(v[e], v[2 + e], 0x128, 0xf, 0xc, false);
      xb[r][e] = __builtin_amdgcn_update_dpp(v[2 + e], v[e], 0x128, 0xf, 0x3, false);
    }
#pragma unroll
  for (int j = 0; j < 4; ++j)
#pragma unroll
    for (int q = 0; q < 4; ++q) {
      a[j][q] = __builtin_amdgcn_perm(xa[2 * q + 1][j >> 1], xa[2 * q][j >> 1], (j & 1) ? 0x07060302u : 0x05040100u);
      b[j][q] = __builtin_amdgcn_perm(xb[2 * q + 1][j >> 1], xb[2 * q][j >> 1], (j & 1) ? 0x07060302u : 0x05040100u);
    }
#pragma unroll
  for (int j = 0; j < 4; ++j) asm volatile("" : "+v"(a[j]), "+v"(b[j]));
}
__device__ __forceinline__ u32x2 tr_read(const LAS char* p) {
  return __builtin_bit_cast(u32x2, __builtin_amdgcn_ds_read_tr16_b64_v4i16((LAS s16x4*)p));
}
__device__ __forceinline__ float ln_sd(float var) { return bfr(__builtin_amdgcn_sqrtf(bfr(bfr(var) + 1e-5f))); }

__device__ __forceinline__ float row_sum(float v) {
  v = dpp_add<0xB1>(v);
  v = dpp_add<0x4E>(v);
  v = dpp_add<0x141>(v);
  return dpp_add<0x140>(v);
}
__device__ __forceinline__ unsigned ln2_rows(u32x4 (&f)[4], bool act) {
  f32x2 d[16];
#pragma unroll
  for (int e = 0; e < 16; ++e) d[e] = f32x2{lo_f(f[e >> 2][e & 3]), hi_f(f[e >> 2][e & 3])};
  f32x2 t[4];
#pragma unroll
  for (int e = 0; e < 4; ++e) t[e] = (d[e] + d[4 + e]) + (d[8 + e] + d[12 + e]);
  t[0] = (t[0] + t[1]) + (t[2] + t[3]);
  const float mean = row_sum(t[0][0] + t[0][1]) * (1.0f / D_);
  const float mr = bfr(mean);
  const float mb = act ? mr : 0.f;
#pragma unroll
  for (int e = 0; e < 16; ++e) d[e] -= f32x2{mb, mb};
#pragma unroll
  for (int e = 0; e < 4; ++e) t[e] = d[e] * d[e];
#pragma unroll
  for (int e = 4; e < 16; ++e) t[e & 3] = __builtin_elementwise_fma(d[e], d[e], t[e & 3]);
  t[0] = (t[0] + t[1]) + (t[2] + t[3]);
  const float dm = mean - mr;
  const float var = fmaxf(row_sum(t[0][0] + t[0][1]) * (1.0f / D_) - dm * dm, 0.f);
  const float s1 = ln_sd(var), r1 = __builtin_amdgcn_rcpf(s1);
  const float r2 = __builtin_amdgcn_rcpf(ln_sd(var * r1 * r1));
  const float a = r1 * r2, cc = -bfr(dm * r1) * r2;
#pragma unroll
  for (int e = 0; e < 16; ++e) {
    const f32x2 v = __builtin_elementwise_fma(d[e], f32x2{a, a}, f32x2{cc, cc});
    f[e >> 2][e & 3] = pack2(v[0], v[1]);
  }
  return (__float_as_uint(mr) >> 16) | __float_as_uint(s1);
}
__device__ __forceinline__ float solo(float v) { asm("" : "+v"(v)); return v; }
__device__ __forceinline__ float vadd(float a, float b) { return solo(a + b); }
__device__ __forceinline__ float vmul(float a, float b) { return solo(a * b); }
__device__ __forceinline__ f32x4 add4(f32x4 a, f32x4 b) { return f32x4{vadd(a[0], b[0]), vadd(a[1], b[1]), vadd(a[2], b[2]), vadd(a[3], b[3])}; }

constexpr unsigned SPIN_CAP = 1u << 18;
template <int SL>
__device__ __forceinline__ void poll_flags(const GAS u64* p, u64 tag) {
#pragma unroll 1
  for (unsigned spins = 0; spins < SPIN_CAP; ++spins) {
    const u64 v = __hip_atomic_load(p, RLX_AGENT);
    if (__all(v == tag)) break;
    __builtin_amdgcn_s_sleep(SL);
  }
  __builtin_amdgcn_fence(__ATOMIC_ACQUIRE, "wavefront");
}

__device__ __forceinline__ unsigned arrive(int lb, int i) {
  unsigned r;
  asm volatile("s_waitcnt lgkmcnt(0)\n\ts_mov_b64 exec, 1\n\tds_add_rtn_u32 %0, %1, %2\n\ts_mov_b64 exec, -1\n\ts_waitcnt lgkmcnt(0)"
               : "=&v"(r) : "v"(lb + L_CNT + 4 * i), "v"(1u) : "memory");
  return __builtin_amdgcn_readfirstlane(r);
}
__device__ __forceinline__ void sig(int lb, int i) {
  asm volatile("s_waitcnt lgkmcnt(0)\n\ts_mov_b64 exec, 1\n\tds_add_u32 %0, %1\n\ts_mov_b64 exec, -1" ::"v"(lb + L_CNT + 4 * i), "v"(1u) : "memory");
}
template <int I> __device__ __forceinline__ void post(int sa, unsigned one) {
  asm volatile("ds_add_u32 %0, %1 offset:%2" ::"v"(sa), "v"(one), "n"(4 * I) : "memory");
}
__device__ __forceinline__ unsigned look(LAS char* L, int i) {
  return __hip_atomic_load((LAS unsigned*)(L + L_CNT) + i, __ATOMIC_RELAXED, __HIP_MEMORY_SCOPE_WORKGROUP);
}
__device__ __forceinline__ void check(LAS char* L, unsigned v, int i, unsigned target) {
  unsigned t, n, a;
  asm volatile("s_nop 0\n\tv_readfirstlane_b32 %0, %3\n\ts_cmp_lt_u32 %0, %5\n\ts_cbranch_scc1 .Lcw%=\n.Lcb%=:\n\t.subsection 1\n"
               ".Lcw%=:\n\ts_mov_b32 %1, 0x400000\n\tv_mov_b32 %2, %4\n"
               ".Lcl%=:\n\tds_read_b32 %3, %2\n\ts_waitcnt lgkmcnt(0)\n\tv_readfirstlane_b32 %0, %3\n\ts_cmp_ge_u32 %0, %5\n\t"
               "s_cbranch_scc1 .Lcb%=\n\ts_sleep 0\n\ts_sub_u32 %1, %1, 1\n\ts_cbranch_scc0 .Lcl%=\n\ts_branch .Lcb%=\n\t.subsection 0"
               : "=&s"(t), "=&s"(n), "=&v"(a), "+v"(v) : "s"((int)(unsigned)(size_t)L + L_CNT + 4 * i), "n"(target) : "memory", "scc");
}
template <int SL = 0> __device__ __forceinline__ void await_at(int a, unsigned target) {
  unsigned t, n, v;
  asm volatile("s_mov_b32 %1, 0x400000\n.Lal%=:\n\tds_read_b32 %2, %3\n\ts_waitcnt lgkmcnt(0)\n\tv_readfirstlane_b32 %0, %2\n\ts_cmp_ge_u32 %0, %4\n\t"
               "s_cbranch_scc1 .Lab%=\n\ts_sleep %5\n\ts_sub_u32 %1, %1, 1\n\ts_cbranch_scc0 .Lal%=\n.Lab%=:"
               : "=&s"(t), "=&s"(n), "=&v"(v) : "v"(a), "n"(target), "n"(SL) : "memory", "scc");
}
template <int SL = 0> __device__ __forceinline__ void await(LAS char* L, int i, unsigned target) { await_at<SL>((int)(unsigned)(size_t)L + L_CNT + 4 * i, target); }

#define BAR() __builtin_amdgcn_s_barrier()
#define LANEVARS                                                                                         \
  int lane;                                                                                              \
  asm volatile("v_mbcnt_lo_u32_b32 %0, -1, 0\n\tv_mbcnt_hi_u32_b32 %0, -1, %0" : "=v"(lane));             \
  const int c = lane & 15, g = lane >> 4, q4 = c >> 2, p4 = c & 3, rr = lane >> 3;                       \
  (void)c; (void)g; (void)q4; (void)p4; (void)rr
constexpr f32x4 Z4 = {0.f, 0.f, 0.f, 0.f};
constexpr int OOB = 0x40000000;
template <class T> __device__ __forceinline__ T sel(bool c, T a, T b) { return __builtin_unpredictable(c) ? a : b; }
__device__ __forceinline__ int trbase(int i, int g, int q4, int p4) {
  return (4 * g + q4) * 128 + (((2 * i + (p4 >> 1)) ^ (2 * ((q4 >> 1) | ((g & 1) << 1)))) << 4) + 8 * (p4 & 1);
}
__device__ __forceinline__ void slot_frags(const LAS char* L, const int (&tb)[4], int so, u32x4 (&a)[4]) {
#pragma unroll
  for (int i = 0; i < 4; ++i) {
    const u32x2 lo = tr_read(L + tb[i] + so), hi = tr_read(L + tb[i] + so + 2048);
    a[i] = u32x4{lo.x, lo.y, hi.x, hi.y};
  }
}
__device__ __forceinline__ u64 peek(__amdgpu_buffer_rsrc_t rs, int off) {
  u64 v;
  asm volatile("buffer_load_dwordx2 %0, %1, %2, 0 offen sc1" : "=v"(v) : "v"(off), "s"(rs) : "memory");
  return v;
}
__device__ __forceinline__ void settle(__amdgpu_buffer_rsrc_t rs, int off, u64 first, u64 tag) {
  unsigned n;
  asm volatile("v_cmp_ne_u64 vcc, %4, %1\n\ts_cbranch_vccz .Lsb%=\n\ts_mov_b32 %0, 0x40000\n"
               ".Lsl%=:\n\tbuffer_load_dwordx2 %1, %2, %3, 0 offen sc1\n\ts_waitcnt vmcnt(0)\n\tv_cmp_ne_u64 vcc, %4, %1\n\t"
               "s_cbranch_vccz .Lsb%=\n\ts_sleep %5\n\ts_sub_u32 %0, %0, 1\n\ts_cbranch_scc0 .Lsl%=\n.Lsb%=:"
               : "=&s"(n), "+v"(first) : "v"(off), "s"(rs), "s"(tag), "n"(SLP) : "memory", "scc", "vcc");
}
template <class F, int... I> __device__ __forceinline__ void static_for(F f, std::integer_sequence<int, I...>) { (f(std::integral_constant<int, I>{}), ...); }
#define SFOR(n, var, ...) static_for([&](auto var##_c) AI { constexpr int var = decltype(var##_c)::value; __VA_ARGS__ }, std::make_integer_sequence<int, n>{})
template <int OFF> __device__ __forceinline__ void ld1_a(u32x4& v, __amdgpu_buffer_rsrc_t rs, int vo, int so) {
  asm volatile("buffer_load_dwordx4 %0, %1, %2, %3 offen offset:%4 sc1" : "=v"(v) : "v"(vo), "s"(rs), "s"(so), "n"(OFF) : "memory");
}
template <int N> __device__ __forceinline__ void wait1(u32x4& a) { asm volatile("s_waitcnt vmcnt(%1)" : "+v"(a) : "n"(N) : "memory"); }
template <int N> __device__ __forceinline__ void wait2(u32x4& a, u32x4& b) { asm volatile("s_waitcnt vmcnt(%2)" : "+v"(a), "+v"(b) : "n"(N) : "memory"); }

template <int NK, int NR, int NB, int GR, class LD, class PUT, class HK>
__device__ __forceinline__ void kloop(LAS char* L, const int (&tb)[4], u32x4 (&fr)[NR][4], u32x4 (&hv)[NB][4],
                                      f32x4 (&acc)[4][4], LD ld, PUT put, HK hook) {
  constexpr int KX = NK - 4;
  SFOR(KX, k,
    u32x2 lo;
    SFOR(4, t,
      constexpr int n = k * 4 + t, i0 = k < GR ? (NB - 1) * 4 + n : k == GR ? (NB - 1) * 4 + n + t : NB * 4 + n;
      wait1<i0 - n - 1>(hv[k % NB][t]);
      SFOR(4, i,
        acc[i][t] = mfma16(hv[k % NB][t], fr[k][i], k == 0 ? Z4 : acc[i][t]);
        asm volatile("" : "+v"(acc[i][t]));
        constexpr int e = t * 4 + i;
        if constexpr (k >= 1 && k - 1 + NR < NK && e < 8) {
          const LAS char* q = L + tb[e >> 1] + (k - 1) * SS;
          if constexpr (e & 1) { const u32x2 hi = tr_read(q + 2048); fr[k - 1][e >> 1] = u32x4{lo.x, lo.y, hi.x, hi.y}; }
          else lo = tr_read(q);
        }
        SB();
      );
      ld(std::integral_constant<int, i0 / 4>{}, std::integral_constant<int, i0 % 4>{});
      if constexpr (k == GR) ld(std::integral_constant<int, (i0 + 1) / 4>{}, std::integral_constant<int, (i0 + 1) % 4>{});
      SB();
    );
  );
  SFOR(4, t,
    constexpr int k = KX + t, is = (k + NB < NK ? k + NB : NK) - k - 1;
    wait4<4 * is>(hv[k % NB]);
    if constexpr (t == 0) hook();
    SFOR(16, e,
      constexpr int i = e >> 2, h = e & 3, d = 4 * t + i - 1;
      if constexpr (NK > NR) {
        acc[t][i] = mfma16(fr[(KX + h) % NR][i], hv[k % NB][h], acc[t][i]);
        asm volatile("" : "+v"(acc[t][i]));
      } else {
        acc[i][t] = mfma16(hv[k % NB][h], fr[KX + h][i], acc[i][t]);
        asm volatile("" : "+v"(acc[i][t]));
      }
      if constexpr (h == 3 && d >= 0) put(std::integral_constant<int, d & 3>{}, std::integral_constant<int, d / 4>{});
      SB();
    );
    if constexpr (k + NB < NK) { SFOR(4, h, ld(std::integral_constant<int, k + NB>{}, h_c);); }
    SB();
  );
  put(std::integral_constant<int, 3>{}, std::integral_constant<int, 3>{});
}
template <int N> __device__ __forceinline__ void gelu_pack(const f32x4 (&v)[N], unsigned (&pr)[2 * N]) {
  f32x4 z[N];
#pragma unroll
  for (int e = 0; e < N; ++e) z[e] = v[e] * (v[e] * v[e] * -0.1029432395800235f - 2.302208198144325f);
  SB();
#pragma unroll
  for (int e = 0; e < 4 * N; ++e) z[e >> 2][e & 3] = __builtin_amdgcn_exp2f(z[e >> 2][e & 3]);
  SB();
#pragma unroll
  for (int e = 0; e < N; ++e) z[e] += 1.0f;
#pragma unroll
  for (int e = 0; e < 4 * N; ++e) z[e >> 2][e & 3] = __builtin_amdgcn_rcpf(z[e >> 2][e & 3]);
  SB();
#pragma unroll
  for (int e = 0; e < N; ++e) { z[e] *= v[e]; pr[2 * e] = pack2(z[e][0], z[e][1]); pr[2 * e + 1] = pack2(z[e][2], z[e][3]); }
}
__device__ __forceinline__ float fold32(float a, float b) {
  const auto s = __builtin_amdgcn_permlane32_swap(__float_as_uint(a), __float_as_uint(b), false, false);
  return vadd(__uint_as_float(s[0]), __uint_as_float(s[1]));
}
#define LA_(a) ((LAS char*)(size_t)(unsigned)(a))
#define PINV(v) asm volatile("" : "+v"(v))
#define IC(n) std::integral_constant<int, (n)>{}
constexpr int g1buf(int nt, int t) { return ((t + 8 - nt) & 3) * SS; }
constexpr int L_B1 = 162816;
constexpr int A_W = 26624, A_WS = 24576, A_X1 = 100352, A_XS = 26624;
constexpr int A_MEAN = 153600, A_RSTD = A_MEAN + 1024, A_CS = A_RSTD + 1024;
#define FINISH() __builtin_amdgcn_endpgm()
__global__ __launch_bounds__(512, 2) void fused_block(const u16*, const u16*, const u16*, const u16*, const u16*, const u16*,
                                                      const u16*, u16*, char*) {
  extern __shared__ __attribute__((aligned(16))) char smem[];
  LAS char* L = (LAS char*)smem;
  const int lb = (int)(unsigned)(size_t)L;
  const auto kp = __builtin_amdgcn_kernarg_segment_ptr();
  u32x16 ka; u32x2 kb;
  asm volatile("s_load_dwordx16 %0, %2, 0x0\n\ts_load_dwordx2 %1, %2, 0x40" : "=&s"(ka), "=&s"(kb) : "s"(kp));
  SB();
  const int w = __builtin_amdgcn_readfirstlane((int)threadIdx.x >> 6);
  const int bid = blockIdx.x, xcd = bid & 7, idz = bid >> 3;
  const int m = w & 3;
#define ARGP(i) ((const u16*)(((u64)ka[2 * (i) + 1] << 32) | ka[2 * (i)]))
#define ARGWAIT() asm volatile("s_waitcnt lgkmcnt(0)" : "+s"(ka), "+s"(kb) : "s"(kp) : "memory")
  u64 tag;
  const int un = xcd * 16 + idz, row0 = 32 * (un >> 1) + 8 * (un & 1);
  const bool isln = idz < 16;
  u32x4 hx[4];
  u64 e1;
  if (isln && w >= 4) {
    PRIO(3);
    int lv = sel(((int)threadIdx.x & 15) < 14, (((int)threadIdx.x >> 4) & 3) * (D_ * 2) + ((int)threadIdx.x & 15) * 16, OOB);
    int ls = (row0 + 4 * (m & 1) + 8 * (m & 2)) * (D_ * 2), z = 0;
    asm volatile("" : "+v"(lv), "+v"(z));
    asm volatile("" : "+s"(ls));
    ARGWAIT();
    ldx4(hx, __builtin_amdgcn_make_buffer_rsrc((void*)ARGP(0), 0, S_ * D_ * 2, RSRC_W3), lv, ls);
    e1 = peek(__builtin_amdgcn_make_buffer_rsrc((void*)(((u64)kb[1] << 32) | kb[0]), 0, WS_END, RSRC_W3), z);
    BAR();
  } else {
    u64 pc;
    unsigned co = sel(isln, 0, (((idz - 16) * 8 + w) * 8 + ((int)threadIdx.x & 7)) * 128);
    asm volatile(".Lfb_start:\n\ts_getpc_b64 %0\n\ts_mov_b32 m0, %2\n\tv_min_u32 %1, .Lfb_end-.Lfb_start-8, %1\n\t"
                 "global_load_lds_dword %1, %0\n\t.subsection 2\n.Lfb_end:\n\ts_endpgm\n\t.subsection 0" : "=&s"(pc), "+v"(co) : "s"(lb + L_DUMMY) : "memory");
    *((LAS unsigned*)(L + L_CNT) + ((int)threadIdx.x & 31)) = 0u;
  }
  SB();
  auto gettag = [&]() AI {
    const u32x2 tv = *(const LAS u32x2*)(L + L_CNT + 128);
    tag = ((u64)(unsigned)__builtin_amdgcn_readfirstlane((int)tv.y) << 32) | (unsigned)__builtin_amdgcn_readfirstlane((int)tv.x);
  };

  const int ng = 24 - 6 * (xcd & 1);
  const int k4 = idz + ng - 32, k5 = idz - 16 - 4 * (xcd & 1);
  const bool isg = k4 >= 0;
  const int lp = sel(k5 < 0, ((0x24 - 3 * ((8 >> xcd) & 1) + 0xc0) >> (k4 & 6)) & 3, k5 >> 2);
  int ft = 7 * (xcd >> 1) + 4 * (xcd & 1) + lp, dg = sel(k5 < 0, 4 + (idz & 1), idz & 3);
  int idx = sel(isg, 6 * lp + dg, ng + idz);
  asm volatile("" : "+s"(idx));
  int off1 = (0xda8520 >> (4 * dg)) & 15;
  const int q = 22 * (xcd >> 2) + 8 * (xcd & 1) + idx - ng;
  int hp = 2 * (xcd & 2) + (q & 3), rg = q >> 2;
  int rotq = RQ1 * (q & 3) + RQ2 * rg + 70;
  rotq -= 7 * ((rotq * 147) >> 10);
  constexpr int EPB = 56;
  auto k1 = [&](int j) AI { return (j + off1) & 15; };
  int reg = (w >> 2) * L_LATE + m * 4096;
  int fv, fw, fs, fs1, role = (isg ? 0 : 2) + (w >> 2);
  {
    const int l = (int)threadIdx.x & 63, rr = l >> 3, ch = l & 7;
    const int v1 = ((m * 512 + rr) * F_ + ft * 64) * 2 + (ch ^ (2 * ((rr >> 1) & 3))) * 16;
    const int cq = ch ^ (2 * ((rr >> 1) & 1)) ^ (4 * (m & 1));
    const int vq = sel(cq < 7, (8 * m + rr) * (N3_ * 2) + hp * 112 + cq * 16, OOB);
    const int va = (192 * rg + 16 + 8 * m + rr) * (D_ * 2) + (ch ^ (4 * (m & 1) + (rr >> 1))) * 16;
    fv = sel(isg, v1, vq);
    fw = va;
    fs = sel(isg, k1(0) * 32 * (F_ * 2), rotq * (64 * N3_ * 2));
    fs1 = sel(isg, k1(1) * 32 * (F_ * 2), rotq * 128);
  }
  asm volatile("" : "+v"(fv), "+v"(fw));
  asm volatile("" : "+s"(fs), "+s"(fs1), "+s"(ft), "+s"(dg), "+s"(reg), "+s"(off1), "+s"(rotq), "+s"(role), "+s"(hp), "+s"(rg));
  const int r0 = 192 * rg;
  ARGWAIT();
  const u16 *x = ARGP(0), *wqkv = ARGP(1), *wo = ARGP(2), *w1 = ARGP(3), *b1 = ARGP(4), *w2 = ARGP(5), *b2 = ARGP(6);
  u16* out = (u16*)ARGP(7);
  char* ws = (char*)(((u64)kb[1] << 32) | kb[0]);
  const auto rs_x = __builtin_amdgcn_make_buffer_rsrc((void*)x, 0, S_ * D_ * 2, RSRC_W3);
  const auto rs_ws = __builtin_amdgcn_make_buffer_rsrc((void*)ws, 0, WS_END, RSRC_W3);
  const auto rs_w1 = __builtin_amdgcn_make_buffer_rsrc((void*)w1, 0, S_ * F_ * 2, RSRC_W3);
  const auto rs_w2 = __builtin_amdgcn_make_buffer_rsrc((void*)w2, 0, F_ * S_ * 2, RSRC_W3);
  auto putflag = [&](int off, int lane) AI {
    __builtin_amdgcn_raw_buffer_store_b128(u32x4{(unsigned)tag, (unsigned)(tag >> 32), (unsigned)tag, (unsigned)(tag >> 32)}, rs_ws,
                                           sel(lane < 8, off + lane * 16, OOB), 0, AUX_SC1);
  };
  auto mktag = [&](u64 e0) AI {
    e0 = e0 * 0x9E3779B97F4A7C15ull + 0x632BE59BD9B4E019ull;
    *(LAS u64*)(L + L_CNT + 128) = e0;
    tag = ((u64)(unsigned)__builtin_amdgcn_readfirstlane((int)(e0 >> 32)) << 32) | (unsigned)__builtin_amdgcn_readfirstlane((int)e0);
  };
  const auto rs_wq = __builtin_amdgcn_make_buffer_rsrc((void*)wqkv, 0, D_ * N3_ * 2, RSRC_W3);
  auto piece = [&](bool rows, int fv, int sg, int so, int e) AI {
    if (rows) {
      if (e < 6 || m < 2) dma16(rs_x, e ? fv : sel(fv >= 32 * D_ * 2, fv - 32 * D_ * 2, OOB), so + (e ? e - 1 : 0) * 32 * (D_ * 2), lb + (sg ? A_X1 + (sg - 1) * A_XS : 0) + (m + 4 * e) * 1024);
    } else {
      dma16(rs_wq, fv, so + (e / 3) * 32 * (N3_ * 2) + (e % 3) * 896, lb + A_W + sg * A_WS + e * 4096 + m * 1024);
    }
  };
  if (role == 0) {
    if (isln) { BAR(); __builtin_amdgcn_s_sleep(GDL2); }
    __builtin_amdgcn_s_sleep(GDLY);
    dma4(rs_w1, fv, fs, 8 * F_ * 2, lb + reg);
    dma4(rs_w1, fv, fs1, 8 * F_ * 2, lb + reg + SS);
  }
  asm volatile("" : "+s"(role));
  if (!isln || role == 2) BAR();
  if (role == 2) {
#pragma unroll
    for (int e = 0; e < 13; ++e) {
      if (e == AS) await(L, 7, 4u);
      if (e < 6) piece(false, fv, 2, fs, e); else piece(true, fw, 2, fs1, e - 6);
    }
  }
  asm volatile("" : "+s"(role));
  if (!(role & 1)) {
  } else if (isln) {
    const int hbase = sel(isg, L_HB, A_W + A_WS);
    PRIO(3);
    LANEVARS;
    const bool act = c < 14;
    int hb = sel(act, hbase + (4 * m + g) * HB_PITCH + c * 16, L_DUMMY);
    int tp = hbase + (4 * (g & 1) + q4) * HB_PITCH + (16 * (g >> 1) + 4 * p4) * 2 + m * 64;
    int ho = WS_H + (un >> 1) * KS_ + (2 * m + (g >> 1)) * 1024 + (2 * (un & 1) + (g & 1)) * 256 + c * 16;
    int h3 = sel(m < 2, ho + 3 * 8192, sel((lane < 16) & (m == 3), WS_ST + (row0 + (c & 7) + 2 * (c & 8)) * 16, OOB));
    int sn = sel(lane == 0, lb + L_CNT, OOB), sw = lb + L_SA + (4 * m + g) * 4, sr = lb + L_SA + c * 4, cn = lb + L_CNT, h1 = ho + 8192, h2 = ho + 16384;
    unsigned one = 1u;
    hb += lb; tp += lb;
    asm volatile("" : "+v"(hb), "+v"(tp), "+v"(ho), "+v"(h3), "+v"(sn), "+v"(one), "+v"(sw), "+v"(sr), "+v"(cn), "+v"(h1), "+v"(h2));
    asm volatile("s_waitcnt vmcnt(1)" : "+v"(hx[0]), "+v"(hx[1]), "+v"(hx[2]), "+v"(hx[3])::"memory");
    const unsigned sa = ln2_rows(hx, act);
#pragma unroll
    for (int e = 0; e < 4; ++e) *(LAS u32x4*)(LA_(hb) + e * 224) = hx[e];
    *(LAS unsigned*)LA_(sw) = sa;
    SB();
    post<0>(sn, one);
    await_at(cn, 4u);
    u32x4 hs[4];
#pragma unroll
    for (int u = 0; u < 4; ++u) {
      const u32x2 a0 = tr_read(LA_(tp) + u * 256), a1 = tr_read(LA_(tp) + u * 256 + 8 * HB_PITCH);
      hs[u] = u32x4{a0.x, a0.y, a1.x, a1.y};
      SB();
    }
    const unsigned sb = *(const LAS unsigned*)LA_(sr);
    SB();
#pragma unroll
    for (int u = 0; u < 3; ++u) __builtin_amdgcn_raw_buffer_store_b128(hs[u], rs_ws, u == 0 ? ho : u == 1 ? h1 : h2, 0, AUX_SC1);
    SB();
    asm volatile("s_waitcnt vmcnt(3)" : "+v"(e1)::"memory");
    mktag(e1);
    if (m == 3) hs[3] = u32x4{sb, (unsigned)tag, sb, (unsigned)(tag >> 32)};
    __builtin_amdgcn_raw_buffer_store_b128(hs[3], rs_ws, h3, 0, AUX_SC1);
    SB();
    post<7>(sn, one);
    if (isg) {
      DRAIN();
      if (arrive(lb, 1) == 3u) putflag(WS_HF + un * FS, lane);
    }
  } else {
    u64 e0 = peek(rs_ws, WS_E);
    asm volatile("s_waitcnt vmcnt(0)" : "+v"(e0)::"memory");
    mktag(e0);
    sig(lb, 7);
  }
  asm volatile("" : "+s"(role));
  int idy = idx;
  asm volatile("" : "+s"(idy));
  const u64 tm = idy < 12 ? 0x59d28518a320820ull : idy < 24 ? 0xbdab49ca2f839acull : 0x18820ffbbcdeb38ull;
  const int tile = sel(xcd & 1, (int)(tm >> (5 * (idy - 12 * ((idy * 43) >> 9)))) & 31, idy);
  const int st = 4 * (xcd ^ 1) + (tile & 3), dt = tile >> 2;
  const int rotl = dt % 6;
  auto pos2 = [&](bool late, int p) AI { const int v = p + rotl; return 14 * m + (late ? 8 + (v >= 6 ? v - 6 : v) : (p + 2 * dt) & 7); };
  auto ldw2 = [&](u32x4 (&a)[4], int vr, int p) AI { ld4(a, rs_w2, vr, (pos2(false, p & 6) * 32 + 16 * (p & 1)) * (S_ * 2), S_ * 2); };
  auto w2lane = [&](int lane) AI { return ((32 * ((lane >> 3) & 1) + 4 * (lane >> 4)) * S_ + st * 64 + 8 * (lane & 7)) * 2; };
  auto gemm2 = [&](auto latec, auto nec, auto& f2, auto idc) AI {
    constexpr bool LATE = decltype(latec)::value;
    constexpr int NR2 = LATE ? 6 : 8, NK2 = LATE ? 10 : 8, N0 = decltype(nec)::value;
    {
      constexpr int NB2 = 3;
      u32x4 uv[NB2][4];
      f32x4 acc[4][4];
      {
        LANEVARS;
        PRIO(0);
        const int w2E = (rr * S_ + st * 64) * 2 + ((lane & 7) ^ (2 * ((rr >> 1) & 3))) * 16;
        int tb[4];
#pragma unroll
        for (int i = 0; i < 4; ++i) tb[i] = reg + trbase(i, g, q4, p4);
        const int v0 = lane * 16;
        const int ta = 4 * dt + 3 * ((lane >> 3) & 1), dgp = sel(ta < 20, (ta * 13) >> 6, (ta >> 2) - 1);
        const int fo = WS_UF + ((7 * m + min(lane & 7, 6)) * 6 + dgp) * FS;
        const int oo = WS_OF + (((st * 43) >> 7) * 8 + 2 * m + (lane & 1)) * FS;
        auto fill = [&](int p, int dst) AI { dma4(rs_w2, w2E, pos2(true, p) * 32 * (S_ * 2), 8 * S_ * 2, lb + dst); };
        constexpr int XS = L_HB - L_LATE;
        const auto rs_wo = __builtin_amdgcn_make_buffer_rsrc((void*)wo, 0, D_ * D_ * 2, RSRC_W3);
        const int woE = (rr * D_ + dt * 64) * 2 + ((lane & 7) ^ (2 * ((rr >> 1) & 3))) * 16;
        auto fillo = [&](int e) AI { dma4(rs_wo, woE, (4 * m + e) * 28 * (D_ * 2), 8 * D_ * 2, lb + L_LATE + m * 4096 + e * SS); };
        const int ub = WS_U + 4 * dt * 1024, ob = WS_O + (4 * st * 16 + 4 * m) * 1024;
        u64 o0 = 0;
        auto ldu = [&](auto pc, auto tc) AI {
          constexpr int p = decltype(pc)::value, t = decltype(tc)::value;
          if constexpr (LATE && p == 6 && t == 0) { asm volatile("s_waitcnt vmcnt(12)" : "+v"(o0)::"memory"); settle(rs_ws, oo, o0, tag); }
          if constexpr (LATE && p == 3 && t == 0) o0 = peek(rs_ws, oo);
          if constexpr (LATE && p >= 6) ld1_a<t * 1024>(uv[p % NB2][t], rs_ws, v0, ob + (p - 6) * 16384);
          else if constexpr (!LATE && p >= 4) ld1_a<(p - 4) * 1024>(uv[p % NB2][t], rs_ws, v0, ub + pos2(LATE, 4 + t) * KS_);
          else ld1_a<t * 1024>(uv[p % NB2][t], rs_ws, v0, ub + pos2(LATE, p) * KS_);
        };
        if constexpr (!LATE) {
          const int vr = w2lane(lane);
#pragma unroll
          for (int p = N0; p < 8; ++p) {
            ldw2(f2[p], vr, p);
            if (p == N0 + NSG - 1) { WAITVM(4 * NSG); sig(lb, 2); }
          }
          await(L, 3, 4u);
          await(L, 4, 4u);
          auto turn = [&](auto pc, auto nc) AI {
            constexpr int p = decltype(pc)::value, n = decltype(nc)::value;
            wait4<n>(f2[2 * p]);
            wait4<n>(f2[2 * p + 1]);
            turn_pair(f2[2 * p], f2[2 * p + 1]);
            SB();
          };
          fill(0, reg); fill(1, reg + SS);
          turn(IC(0), IC(32));
          await(L, 5, 4u);
          fill(2, reg + 2 * SS); fill(3, reg + 3 * SS);
          turn(IC(1), IC(32));
          fill(4, L_HB + m * 5120); fill(5, L_LATE + reg + 3 * SS);
          turn(IC(2), IC(32));
          fillo(0); fillo(1); fillo(2);
          WAITVM(12);
          sig(lb, 16 + m);
          u64 f0 = peek(rs_ws, fo);
          turn(IC(3), IC(13));
          asm volatile("s_waitcnt vmcnt(0)" : "+v"(f0)::"memory");
          settle(rs_ws, fo, f0, tag);
          sig(lb, 16 + m);
        } else {
          await(L, 16 + m, 1u);
          slot_frags(L, tb, 3 * SS, f2[5]);
          asm volatile("s_waitcnt lgkmcnt(0)" : "+v"(f2[5][0]), "+v"(f2[5][1]), "+v"(f2[5][2]), "+v"(f2[5][3])::"memory");
          fillo(3);
#pragma unroll
          for (int r = 0; r < 4; ++r) slot_frags(L, tb, r * SS - L_LATE, f2[r]);
          slot_frags(L, tb, XS + m * 1024, f2[4]);
          sig(lb, 20 + m);
          await(L, 16 + m, 2u);
        }
        PRIO(LATE ? 1 : 2);
        SFOR((NB2 - LATE) * 4, n, ldu(IC(n / 4), IC(n % 4)););
        SB();
        int wr[4], sa = sel(lane == 0, lb + L_CNT, OOB);
        unsigned one = 1u;
        if constexpr (LATE) { PINV(sa); PINV(one); }
#pragma unroll
        for (int e = 0; e < 4; ++e) wr[e] = reg + g * 256 + (LATE ? c ^ (2 * e + (g >> 1)) : (c + 8 * e + 4 * (g >> 1)) & 15) * 16;
        kloop<NK2, NR2, NB2, LATE ? 1 : -1>(L, tb, f2, uv, acc, ldu,
            [&](auto ic, auto tc) AI {
              constexpr int i = LATE ? decltype(tc)::value : decltype(ic)::value, t = LATE ? decltype(ic)::value : decltype(tc)::value;
              *(LAS f32x4*)(L + wr[(t & 1) + (LATE ? 2 * (i & 1) : 0)] + i * SS + t * 1024) = acc[i][t];
              if constexpr (LATE && (i & 1) && t == 3) { SB(); post<(i == 1 ? 31 : 10)>(sa, one); }
            }, [&]() AI { if constexpr (!LATE) await(L, 20 + m, 1u); });
      }
      if constexpr (!LATE) {
        PRIO(0);
        LANEVARS;
        const int rw = st * 64 + 8 * (rr & 3) + (rr & 4) + m, eo = (rw * D_ + dt * 64 + (lane & 7) * 8) * 2;
        u32x4 xr[2];
        float bs[2];
#pragma unroll
        for (int e = 0; e < 2; ++e) {
          xr[e] = __builtin_amdgcn_raw_buffer_load_b128(rs_x, eo + e * 32 * D_ * 2, 0, 0);
          bs[e] = lo_f(*(const GAS u16*)(b2 + rw + 32 * e));
        }
        int ra[2];
#pragma unroll
        for (int e = 0; e < 2; ++e) ra[e] = (m * 16 + ((lane & 7) >> 1)) * 1024 + (lane & 1) * 512 + ((4 * e + (rr & 3) + 2 * (rr & 4) + 4 * (lane & 3)) & 15) * 16;
        sig(lb, 9);
        await<2>(L, 9, 4u);
        f32x4 tm[4][4], base[4];
#pragma unroll
        for (int j = 0; j < 4; ++j)
#pragma unroll
          for (int e = 0; e < 4; ++e) tm[j][e] = *(const LAS f32x4*)(L + ra[e >> 1] + j * 4096 + (e & 1) * 256);
        SB();
#pragma unroll
        for (int e = 0; e < 4; ++e) {
          const unsigned a = xr[e >> 1][2 * (e & 1)], b = xr[e >> 1][2 * (e & 1) + 1];
          const float s = bs[e >> 1];
          base[e] = add4(add4(add4(f32x4{lo_f(a), hi_f(a), lo_f(b), hi_f(b)}, f32x4{s, s, s, s}), add4(tm[0][e], tm[1][e])), add4(tm[2][e], tm[3][e]));
          asm volatile("" : "+v"(base[e]));
        }
        int rl = L_LATE + ((rr >> 1) & 1) * SS + ((lane & 7) >> 1) * 1024 + (lane & 1) * 512 + ((8 * (rr & 1) + (rr & 4) + m) ^ ((lane & 3) + 2 * (rr & 2))) * 16;
        int e1 = eo + 32 * D_ * 2;
        asm volatile("" : "+v"(rl), "+v"(e1));
        const auto rs_out = __builtin_amdgcn_make_buffer_rsrc((void*)out, 0, S_ * D_ * 2, RSRC_W3);
        int ca = lb + L_CNT + 4 * 31, ev = eo, it = 0;
        f32x4 b0 = base[0], b1 = base[1];
        _Pragma("unroll 1") do {
          asm volatile("" : "+v"(ca), "+v"(ev), "+v"(rl), "+v"(b0), "+v"(b1));
          await_at(ca, 4u);
          SFOR(8, j, tm[j >> 1][j & 1] = *(const LAS f32x4*)(L + rl + (j >> 1) * 4096 + (j & 1) * 256););
          SB();
          const f32x4 s0 = (b0 + tm[0][0]) + tm[1][0] + (tm[2][0] + tm[3][0]), s1 = (b1 + tm[0][1]) + tm[1][1] + (tm[2][1] + tm[3][1]);
          __builtin_amdgcn_raw_buffer_store_b128(u32x4{pack2(s0[0], s0[1]), pack2(s0[2], s0[3]), pack2(s1[0], s1[1]), pack2(s1[2], s1[3])}, rs_out, ev, 0, AUX_SC1);
          SB();
          ca = lb + L_CNT + 4 * 10; ev = e1; rl += 2 * SS; b0 = base[2]; b1 = base[3];
          asm volatile("" : "+s"(it));
        } while (++it < 2);
      }
    }
    asm volatile("s_nop %0" ::"n"(decltype(idc)::value));
    FINISH();
  };
  auto utile = [&](auto latec) AI {
    constexpr bool LATE = decltype(latec)::value;
      auto gemm1 = [&](auto ntc) AI {
        constexpr int NT = decltype(ntc)::value, NL = 8 * NT, NB = NB1;
        const int t0 = NT == 5 ? 5 * dg : 4 * dg + 4;
        u32x4 fr[8][4], hv[NB], f2[LATE ? 6 : 8][4];
        LANEVARS;
        PRIO(LATE ? 0 : 1);
        const int v0 = lane * 16, v4 = v0 + 4096, wr = reg + v0;
        int ho[8];
        auto ld = [&](auto nc) AI {
          constexpr int n = decltype(nc)::value, t = n >> 3;
          ld1_a<(t & 3) * 1024>(hv[n % NB], rs_ws, t < 4 ? v0 : v4, ho[n & 7]);
        };
        auto start = [&]() AI {
          PRIO(LATE ? 2 : 1);
#pragma unroll
          for (int p = 0; p < 8; ++p) ho[p] = WS_H + (m * 16 + k1((LATE ? 8 : 0) + p)) * KS_ + t0 * 1024;
          SFOR(NB, n, ld(n_c););
          SB();
        };
        {
          const int w1E = ((m * 512 + rr) * F_ + ft * 64) * 2 + ((lane & 7) ^ (2 * ((rr >> 1) & 3))) * 16;
          auto fill = [&](int j) AI { dma4(rs_w1, w1E, k1(j) * 32 * (F_ * 2), 8 * F_ * 2, lb + reg + ((j >> 2) & 1) * L_LATE + (j & 3) * SS); };
          int tb[2][4];
#pragma unroll
          for (int i = 0; i < 8; ++i) tb[i >> 2][i & 3] = (i >> 2) * L_LATE + m * 4096 + trbase(i & 3, g, q4, p4);
          auto take = [&](int j) AI { slot_frags(L, tb[j >> 2], (j & 3) * SS, fr[j]); };
          auto taken = [&](int j) AI { asm volatile("s_waitcnt lgkmcnt(0)" : "+v"(fr[j][0]), "+v"(fr[j][1]), "+v"(fr[j][2]), "+v"(fr[j][3])::"memory"); };
          if constexpr (!LATE) {
            const int fo = WS_HF + (m * 32 + (lane & 31)) * FS;
            u64 f0;
            WAITVM(NT == 4 ? 0 : 8);
#pragma unroll
            for (int j = 2; j < 8; ++j) fill(j);
            await(L, 7, 4u);
            gettag();
            SFOR(8, j, WAITVM(28); take(j); taken(j); if constexpr (j == 7) f0 = peek(rs_ws, fo); fill(8 + j););
            WAITVM(17); sig(lb, 12 + m);
            asm volatile("s_waitcnt vmcnt(4)" : "+v"(f0)::"memory");
            settle(rs_ws, fo, f0, tag);
            sig(lb, 12 + m);
            start();
            WAITVM(NB); sig(lb, 12 + m);
          } else {
            if (m == 0) {
              const auto rs_b1 = __builtin_amdgcn_make_buffer_rsrc((void*)b1, 0, F_ * 2, RSRC_W3);
              await(L, 7, 4u);
              touch(rs_b1, sel(lane < 32, lane * 4, OOB), ft * 128, lb + L_B1);
            }
            await(L, 12 + m, 1u);
#pragma unroll
            for (int j = 0; j < 4; ++j) take(j);
            DRAIN();
            if (m == 0) sig(lb, 11);
            await(L, 12 + m, 2u);
            start();
            await(L, 12 + m, 3u);
#pragma unroll
            for (int j = 4; j < 8; ++j) take(j);
#pragma unroll
            for (int j = 0; j < 8; ++j) taken(j);
            sig(lb, 30);
          }
        }
        const int ra = (lane >> 5) * L_LATE + ((LATE ? 2 : 0) + (g & 1)) * 1024 + m * 256 + c * 16;
        const int ba = L_B1 + (16 * ((LATE ? 2 : 0) + (g & 1)) + 4 * m + 2 * (lane >> 5)) * 2;
        const int ub = WS_U + (2 * ft + (LATE ? 1 : 0)) * KS_ + t0 * 1024 + m * 256 + c * 16, uo = ub + g * 1024;
        const int vr = w2lane(lane);
        int sa = sel(lane == 0, lb + L_CNT, OOB);
        unsigned one = 1u;
        PINV(sa); PINV(one);
        f32x4 acc[2][4], pt[4];
        float bq[2], v[2], z[2];
        unsigned pr[6], cv, cr;
        auto rd = [&](int t) AI {
#pragma unroll
          for (int e = 0; e < 4; ++e) pt[e] = *(const LAS f32x4*)(L + ra + g1buf(NT, t) + e * 4096);
        };
        auto step = [&](auto sc, int t) AI {
          constexpr int s = decltype(sc)::value;
          if constexpr (s == 0) { pt[0] = add4(pt[0], pt[1]); PINV(pt[0]); }
          if constexpr (s == 1) { pt[2] = add4(pt[2], pt[3]); PINV(pt[2]); }
          if constexpr (s == 2) { pt[0] = add4(pt[0], pt[2]); PINV(pt[0]); }
          if constexpr (s == 3) {
#pragma unroll
            for (int e = 0; e < 2; ++e) { v[e] = vadd(fold32(pt[0][e], pt[0][2 + e]), bq[e]); PINV(v[e]); }
          }
          if constexpr (s == 4) {
#pragma unroll
            for (int e = 0; e < 2; ++e) {
              const float p = solo(__builtin_fmaf(vmul(v[e], v[e]), -0.1029432395800235f, -2.302208198144325f));
              z[e] = vmul(v[e], p); PINV(z[e]);
            }
          }
          if constexpr (s == 5) {
#pragma unroll
            for (int e = 0; e < 2; ++e) { z[e] = vadd(__builtin_amdgcn_exp2f(z[e]), 1.0f); PINV(z[e]); }
          }
          if constexpr (s == 6) {
            pr[t] = pack2(vmul(__builtin_amdgcn_rcpf(z[0]), v[0]), vmul(__builtin_amdgcn_rcpf(z[1]), v[1]));
            PINV(pr[t]);
          }
        };
        unsigned sw[4];
        auto fine = [&](auto sc, int t) AI {
          constexpr int s = decltype(sc)::value;
          if constexpr (s < 6) {
            constexpr int d = s < 2 || s >= 4 ? 0 : 2, o = s < 2 ? 1 : s < 4 ? 3 : 2, e = 2 * (s & 1);
            pt[d][e] = vadd(pt[d][e], pt[o][e]); pt[d][e + 1] = vadd(pt[d][e + 1], pt[o][e + 1]); PINV(pt[d]);
          }
          if constexpr (s == 6) {
#pragma unroll
            for (int e = 0; e < 2; ++e) {
              const auto r = __builtin_amdgcn_permlane32_swap(__float_as_uint(pt[0][e]), __float_as_uint(pt[0][2 + e]), false, false);
              sw[2 * e] = r[0]; sw[2 * e + 1] = r[1]; PINV(sw[2 * e]); PINV(sw[2 * e + 1]);
            }
          }
          auto two = [&](float (&d)[2], auto f) AI { d[0] = f(0); d[1] = f(1); PINV(d[0]); PINV(d[1]); };
          if constexpr (s == 7) two(v, [&](int e) AI { return vadd(__uint_as_float(sw[2 * e]), __uint_as_float(sw[2 * e + 1])); });
          if constexpr (s == 8) two(v, [&](int e) AI { return vadd(v[e], bq[e]); });
          if constexpr (s == 9) two(z, [&](int e) AI { return vmul(v[e], v[e]); });
          if constexpr (s == 10) two(z, [&](int e) AI { return solo(__builtin_fmaf(z[e], -0.1029432395800235f, -2.302208198144325f)); });
          if constexpr (s == 11) two(z, [&](int e) AI { return vmul(v[e], z[e]); });
          if constexpr (s == 12 || s == 13) { z[s - 12] = __builtin_amdgcn_exp2f(z[s - 12]); PINV(z[s - 12]); }
          if constexpr (s == 14) two(z, [&](int e) AI { return vadd(z[e], 1.0f); });
          if constexpr (s == 15 || s == 16) { z[s - 15] = __builtin_amdgcn_rcpf(z[s - 15]); PINV(z[s - 15]); }
          if constexpr (s == 17) two(z, [&](int e) AI { return vmul(z[e], v[e]); });
          if constexpr (s == 18) { pr[t] = pack2(z[0], z[1]); PINV(pr[t]); }
        };
        auto put = [&](unsigned a, unsigned b, unsigned cc, unsigned d, int off) AI {
          const auto s0 = __builtin_amdgcn_permlane32_swap(a, cc, false, false), s1 = __builtin_amdgcn_permlane32_swap(b, d, false, false);
          const auto r0 = __builtin_amdgcn_permlane16_swap(s0[0], s1[0], false, false), r1 = __builtin_amdgcn_permlane16_swap(s0[1], s1[1], false, false);
          __builtin_amdgcn_raw_buffer_store_b128(u32x4{r0[0], r1[0], r0[1], r1[1]}, rs_ws, off, 0, AUX_SC1);
          SB();
        };
        SFOR(NL, n,
          constexpr int t = n >> 3, p = n & 7, nw = !LATE && t == NT - 1 ? 4 * (p < NE ? p : NE) : 0;
          if constexpr (n == 9 && !LATE) check(L, cr, 30, 4u);
          if constexpr (n == 13) { check(L, cv, 11, 1u); const unsigned b = *(const LAS unsigned*)(L + ba); bq[0] = lo_f(b); bq[1] = hi_f(b); }
          if constexpr (t >= 2 && p == 2) { check(L, cv, 24 + t - 2, 8u); rd(t - 2); if constexpr (NT == 5 && t == 2) post<29>(sa, one); }
          if constexpr (t >= 2 && p == 1) cv = look(L, 24 + t - 2);
          if constexpr (n == 7 && !LATE) cr = look(L, 30);
          if constexpr (n == 11) cv = look(L, 11);
          if constexpr (n == NL - 2) { cv = look(L, 24 + NT - 2); if constexpr (NT == 5) cr = look(L, 29); }
          SB();
          if constexpr (!(n & 1)) wait2<(n + NB < NL ? n + NB : NL) - 2 - n + nw>(hv[n % NB], hv[(n + 1) % NB]);
          SFOR(4, i,
            constexpr int ci = 2 * (p & 1) + (i >> 1), cn = (n & ~1) + (i & 1);
            mfma_ip<(cn & 7) == 0>(acc[t & 1][ci], fr[cn & 7][ci], hv[cn % NB]);
            if constexpr (t >= 1 && (p == 1 || p == 2) && (i & 1)) *(LAS f32x4*)(L + wr + g1buf(NT, t - 1) + (2 * p - 2 + (i >> 1)) * 1024) = acc[(t - 1) & 1][2 * p - 2 + (i >> 1)];
            if constexpr (t >= 2 && 4 * p + i >= 13) fine(IC(4 * p + i - 13), t - 2);
            SB();
          );
          if constexpr (t >= 1 && p == 2) post<24 + t - 1>(sa, one);
          if constexpr ((n & 1) && n + NB < NL) { ld(IC(n + NB - 1)); ld(IC(n + NB)); }
          if constexpr (!LATE && t == NT - 1 && (p & 1) && p < NE) { ldw2(f2[p - 1], vr, p - 1); ldw2(f2[p], vr, p); }
          SB();
        );
        PRIO(LATE ? 0 : 3);
        if constexpr (NT == 5) check(L, cr, 29, 8u);
        asm volatile("s_nop 7" ::: "memory");
#pragma unroll
        for (int i = 0; i < 4; ++i) *(LAS f32x4*)(L + wr + g1buf(NT, NT - 1) + i * 1024) = acc[(NT - 1) & 1][i];
        post<24 + NT - 1>(sa, one);
        if constexpr (LATE) post<4>(sa, one);
        check(L, cv, 24 + NT - 2, 8u);
        rd(NT - 2);
        if constexpr (!LATE) { post<3>(sa, one); PRIO(3); }
        SFOR(7, s, step(s_c, NT - 2););
        if constexpr (NT == 5) put(pr[0], pr[1], pr[2], pr[3], uo);
        else if constexpr (!LATE) put(pr[0], pr[1], pr[2], pr[2], sel(lane < 48, uo, OOB));
        if constexpr (LATE) {
          const unsigned b2q = *(const LAS unsigned*)(L + ba - 64);
          await(L, 24 + NT - 1, 8u);
          PRIO(3);
          {
            f32x4 qa[8];
#pragma unroll
            for (int e = 0; e < 8; ++e) qa[e] = *(const LAS f32x4*)(L + ra - (e >> 2) * 2048 + g1buf(NT, NT - 1) + (e & 3) * 4096);
            post<5>(sa, one);
            const f32x4 s0 = (qa[0] + qa[1]) + (qa[2] + qa[3]), s1 = (qa[4] + qa[5]) + (qa[6] + qa[7]);
            f32x4 y[1] = {f32x4{fold32(s0[0], s0[2]), fold32(s0[1], s0[3]), fold32(s1[0], s1[2]), fold32(s1[1], s1[3])} + f32x4{bq[0], bq[1], lo_f(b2q), hi_f(b2q)}};
            unsigned o2[2];
            gelu_pack<1>(y, o2);
            pr[NT - 1] = o2[0]; pr[5] = o2[1];
          }
          if constexpr (NT == 4) put(pr[0], pr[1], pr[2], pr[3], uo);
          if constexpr (NT == 5) put(pr[4], pr[5], pr[4], pr[5], sel(lane < 32, ub + 4096 - g * KS_, OOB));
          else put(pr[5], pr[5], pr[5], pr[5], sel(lane < 16, ub + 3072 - KS_, OOB));
          DRAIN();
          if (arrive(lb, 6) == 3u) { await(L, 2, 4u); putflag(WS_UF + (ft * 6 + dg) * FS, lane); }
        }
        gemm2(latec, IC(LATE ? 0 : NE), f2, IC(2 * NT - 6 + LATE));
      };
      if (dg < 4) gemm1(IC(5));
      gemm1(IC(4));
  };
  if (role == 0) utile(std::false_type{});
  if (role == 1) utile(std::true_type{});
  if (role == 2) {
    u32x4 f2[8][4];
    {
      LANEVARS;
      const int vr = w2lane(lane);
        const int cq = (lane & 7) ^ (2 * ((rr >> 1) & 1)) ^ (4 * (m & 1));
        const int vq = sel(cq < 7, (8 * m + rr) * (N3_ * 2) + hp * 112 + cq * 16, OOB);
        const int va = (r0 + 16 + 8 * m + rr) * (D_ * 2) + ((lane & 7) ^ (4 * (m & 1) + (rr >> 1))) * 16;
        const int so_ = WS_ST + min(max(r0 - 16 + min(64 * m + lane, 207), 0), S_ - 1) * 16;
        auto pcs = [&](int pp, int from, int to) AI {
          const bool wr = rotq >= 7 - pp;
#pragma unroll
          for (int n = from; n < to; ++n) {
            if (n < 6) piece(false, vq, (pp + 2) % 3, fs + sel(wr, (pp - 7) * 64 * N3_ * 2, pp * 64 * N3_ * 2), n);
            else piece(true, va, (pp + 2) % 3, rotq * 128 + sel(wr, (pp - 7) * 128, pp * 128), n - 6);
          }
        };
        pcs(1, 0, LA);
        gettag();
        WAITVM(LA);
        BAR();
        auto warm = [&](int e) AI {
          const auto rs_wo = __builtin_amdgcn_make_buffer_rsrc((void*)wo, 0, D_ * D_ * 2, RSRC_W3);
          const int j = idx - 28;
          if (e < 3) touch(rs_w2, (lane >> 2) * (S_ * 2) + (lane & 3) * 128, (448 * m + 256 + 48 * j + 16 * e) * (S_ * 2) + (xcd ^ 1) * 512, lb + L_DUMMY);
          else touch(rs_wo, lane * 128, ((e - 3) * 16 + j * 4 + m) * 8192, lb + L_DUMMY);
        };
        u32x4 sg;
        SFOR(6, n0,
          constexpr int n = n0 + 1, open = n < 5 ? LA : n == 5 ? 12 : 4 * NA;
          if constexpr (n < 6) pcs(n, LA, 13);
          if constexpr (n == 3) asm volatile("buffer_load_dwordx4 %0, %1, %2, 0 offen sc1" : "=v"(sg) : "v"(so_), "s"(rs_ws) : "memory");
          if constexpr (n < 6) pcs(n + 1, 0, n == 5 ? 13 : LA);
          if constexpr (n == 4) {
            asm volatile("" : "+v"(sg));
            _Pragma("unroll 1")
            for (unsigned spins = 0; spins < SPIN_CAP; ++spins) {
              if (__all((sg.y == (unsigned)tag) & (sg.w == (unsigned)(tag >> 32)) & (sg.x == sg.z))) break;
              __builtin_amdgcn_s_sleep(4);
              asm volatile("buffer_load_dwordx4 %0, %1, %2, 0 offen sc1\n\ts_waitcnt vmcnt(0)" : "=v"(sg) : "v"(so_), "s"(rs_ws) : "memory");
            }
            *(LAS float*)(L + A_MEAN + (64 * m + lane) * 4) = -lo_f(sg.x);
            *(LAS float*)(L + A_RSTD + (64 * m + lane) * 4) = __builtin_amdgcn_rcpf(hi_f(sg.x));
            WAITLG();
          }
          if constexpr (n == 6) { SFOR(NA, e, ldw2(f2[e], vr, e);); }
          if (n >= 5 && idx >= 28) {
            warm(2 * n - 10); warm(2 * n - 9);
            WAITVM(open + 2);
          } else {
            WAITVM(open);
          }
          BAR();
        );
        sig(lb, 3);
        if (idx >= 28) {
          touch(rs_w2, lane * (S_ * 2), (448 * m + 64 * (idx - 28)) * (S_ * 2) + (xcd ^ 1) * 512 + 384, lb + L_DUMMY);
#pragma unroll
          for (int e = 4; e < 7; ++e) warm(e);
          DRAIN();
          SFOR(NA, p, wait4<0>(f2[p]););
        }
    }
    if (idx < 28) gemm2(std::false_type{}, IC(NA), f2, IC(0));
    FINISH();
  } else {
        auto att = [&](auto jc) AI {
          constexpr int J = decltype(jc)::value, NH = J ? 2 : 4, H0 = J ? 0 : 2;
          const int H = m & 1;
          LANEVARS;
          f32x4 qa[6][6], qh[NH];
          int wa[2], xa[2][2];
#pragma unroll
          for (int i = 0; i < 2; ++i) {
            const int by = sel((i == 1) & (p4 == 3), 112, 56 * H + 32 * i + 8 * p4);
            wa[i] = A_W + (8 * g + q4) * 128 + (((by >> 4) ^ (2 * ((q4 >> 1) | ((g & 1) << 1)))) << 4) + (by & 8);
          }
          int wb[2] = {wa[0] + A_WS + 12288, wa[1] + A_WS + 12288};
#pragma unroll
          for (int i = 0; i < 4; ++i) xa[i >> 1][i & 1] = (i >> 1) * A_X1 + c * 128 + (((4 * (i & 1) + g) ^ ((c >> 1) & 7)) << 4);
          asm volatile("" : "+v"(wa[0]), "+v"(wa[1]), "+v"(wb[0]), "+v"(wb[1]), "+v"(xa[0][0]), "+v"(xa[0][1]), "+v"(xa[1][0]), "+v"(xa[1][1]));
          u32x4 wf[6], xf[7];
          u32x2 lo;
          auto rdx = [&](int k, int t) AI {
            const int sg = ((k >> 1) + 2) % 3;
            xf[t] = *(const LAS u32x4*)(L + xa[sg ? 1 : 0][k & 1] + (sg == 2 ? A_XS : 0) + (t == 6 ? 0 : (1 + 6 * J + t) * 2048));
          };
          auto rdw = [&](int k, int i, int hf) AI {
            const int o = (((k >> 1) + 2) % 3) * 2 + (k & 1);
            const LAS char* p = L + (o < 3 ? wa[i & 1] + o * 12288 : wb[i & 1] + (o - 3) * 12288) + (i >> 1) * 4096;
            if (hf) { const u32x2 hi = tr_read(p + 512); wf[i] = u32x4{lo.x, lo.y, hi.x, hi.y}; }
            else lo = tr_read(p);
          };
          const bool c0 = c == 0;
          int exa = lb + A_X1 + H * 4096 + lane * 16;
          int ta = lb + A_MEAN + (96 * J + c) * 4, tk = lb + A_RSTD + (96 * J + 4 * g + (c & 1)) * 4, tc = lb + A_CS + (H * 96 + 4 * g) * 4, tv = lb + A_CS + (H * 96 + 64 + c) * 4;
          int sa = sel(lane == 0, lb + L_CNT, OOB);
          unsigned one = 1u;
          f32x2 mk[2][2];
          {
            const int d0 = 16 + (c & 14) - 4 * g, hi = min(r0 + c, 14);
#pragma unroll
            for (int e = 0; e < 4; ++e) {
              const int dk = d0 - 16 * (e >> 1) - 2 * (e & 1);
              mk[1][e >> 1][e & 1] = sel((unsigned)dk <= 14u, 0.f, -3.0e38f);
              mk[0][e >> 1][e & 1] = sel((unsigned)dk <= (unsigned)hi, 0.f, -3.0e38f);
            }
          }
          PINV(exa); PINV(ta); PINV(tk); PINV(tc); PINV(tv); PINV(sa); PINV(one); PINV(mk[1][0]); PINV(mk[1][1]);
          if constexpr (J) { mk[0][0] = mk[1][0]; mk[0][1] = mk[1][1]; } else { PINV(mk[0][0]); PINV(mk[0][1]); }
          float mc[7], rc[6];
          f32x2 rk[7];
          auto tab = [&](int e) AI {
            const int t = e / 3;
            if (e % 3 == 0) mc[t] = *(const LAS float*)(LA_(ta) + 64 * t);
            if (e % 3 == 1) { const LAS float* p = (const LAS float*)(LA_(tk) + 64 * t); rk[t] = f32x2{p[0], p[2]}; }
            if (e % 3 == 2) rc[t] = *(const LAS float*)(LA_(ta) + A_RSTD - A_MEAN + 64 * (t + 1));
          };
          DRAIN();
          if (arrive(lb, 1) == 3u) putflag(WS_HF + un * FS, lane);
          BAR();
          PRIO(2);
#pragma unroll
          for (int t = 0; t < 7; ++t) rdx(0, t);
#pragma unroll
          for (int i = 0; i < 10; ++i) rdw(0, i >> 1, i & 1);
          gettag();
          SB();
          SFOR(14, k,
            SFOR(6, ci,
              constexpr int i = (k == 13 && J == 0) ? (ci + 2) % 6 : ci;
              SFOR(7, t,
                if constexpr (t < 6) {
                  if constexpr (i < 4) mfma_ip<k == 0>(qa[t][i], wf[i], xf[t]); else mfma_ip<k == 0>(qa[t][i], xf[t], wf[i]);
                } else if constexpr (i >= H0 && i < H0 + NH) {
                  if constexpr (i < 4) mfma_ip<k == 0>(qh[i - H0], wf[i], xf[6]); else mfma_ip<k == 0>(qh[i - H0], xf[6], wf[i]);
                  if constexpr (k < 13 && i == H0 + NH - 1 && J == 0) rdx(k + 1, 6);
                }
                if constexpr (ci == 0 && t < 2) rdw(k, 5, t);
                if constexpr (ci == (k == 13 ? 0 : H0) && t == 2) { SFOR(4, j, xf[6][j] = sel(c0, 0x3f803f80u, xf[6][j]);); asm volatile("" : "+v"(xf[6])); }
                if constexpr ((k & 1) && k < 13 && ci == 1 && t == 1) { WAITLG(); BAR(); }
                if constexpr (k < 13 && ci >= 1 && (t == 2 || t == 3)) rdw(k + 1, ci - 1, t - 2);
                if constexpr (k < 13 && ci == 5 && t < 6) rdx(k + 1, t);
                if constexpr (k < 13 && J == 1 && ci == 2 && t == 0) rdx(k + 1, 6);
                if constexpr (k == 13 && t == 2 && ci >= 2 && ci < NH + 2) {
                  constexpr int ip = H0 + ci - 2;
                  if constexpr (ip < 4) *(LAS f32x4*)(LA_(sel(c0, tc, lb + L_DUMMY)) + 64 * ip) = qh[ci - 2];
                  else *(LAS float*)(LA_(sel(g == 0, tv, lb + L_DUMMY)) + 64 * (ip - 4)) = qh[ci - 2][0];
                }
                if constexpr (k == 13 && J == 0 && t == 4 && ci >= 2) *(LAS f32x4*)(LA_(exa) + (ci - 2) * 1024) = qa[5][ci];
                if constexpr (k == 13 && (J ? ci == 3 && t == 2 : ci == 5 && t == 4)) { SB(); post<4>(sa, one); }
                if constexpr (k == 13 && ci >= 2 && t < 5) tab(5 * (ci - 2) + t);
                SB();
              );
            );
          );
          await(L, 4, 4u);
          f32x4 c4[4], ex[4];
          float cv[2];
#pragma unroll
          for (int i = 0; i < 4; ++i) c4[(i + 2) & 3] = *(const LAS f32x4*)(LA_(tc) + 64 * ((i + 2) & 3));
#pragma unroll
          for (int i = 0; i < 4; ++i) { if constexpr (J) ex[i] = *(const LAS f32x4*)(LA_(exa) + i * 1024); else ex[i] = qh[i & (NH - 1)]; }
#pragma unroll
          for (int i = 0; i < 2; ++i) cv[i] = *(const LAS float*)(LA_(tv) + 64 * i);
          SB();
          post<5>(sa, one);
          const bool odd = ta & 4;
          f32x2 lg[6][2];
#pragma unroll
          for (int u = 0; u < 6; ++u) {
            const float sc = rc[u] * (0.18898223650461362f * 1.4426950408889634f);
            lg[u][0] = rk[u] * sc; lg[u][1] = rk[u + 1] * sc;
            PINV(lg[u][0]); PINV(lg[u][1]);
          }
          int tm = (tk & ~4) + A_MEAN - A_RSTD;
          int ov = exa - lb + WS_O - A_X1 + ((12 * rg + 6 * J) * 16 + 2 * hp - 3 * H) * 1024;
          unsigned p0 = sel(odd, 0x01000c0cu, 0x0c0c0100u), p1 = sel(odd, 0x03020c0cu, 0x0c0c0302u);
          PINV(tm); PINV(ov); PINV(p0); PINV(p1);
          SB();
          auto pk4 = [&](f32x4 v) AI { return u32x2{pack2(v[0], v[1]), pack2(v[2], v[3])}; };
          auto frag = [&](f32x4 a, f32x4 b, float mu, int i) AI {
            const u32x2 l = pk4(c4[i] * mu + a), h = pk4(c4[i + 1] * mu + b);
            return u32x4{l.x, l.y, h.x, h.y};
          };
          f32x4 s0[6], s1[6], m4[7];
          u32x4 kf[7], qf[6];
          kf[0] = frag(ex[0], ex[1], mc[0], 2);
          qf[0] = frag(qa[0][0], qa[0][1], mc[1], 0);
#pragma unroll
          for (int u = 0; u < 6; ++u) {
            kf[u + 1] = frag(qa[u][2], qa[u][3], mc[u + 1], 2);
            SB();
            s0[u] = mfma16(kf[u], qf[u], Z4);
            m4[u] = *(const LAS f32x4*)(LA_(tm) + 64 * u);
            SB();
            if (u < 5) qf[u + 1] = frag(qa[u + 1][0], qa[u + 1][1], mc[u + 2], 0); else m4[6] = *(const LAS f32x4*)(LA_(tm) + 64 * 6);
            SB();
            s1[u] = mfma16(kf[u + 1], qf[u], Z4);
            SB();
          }
          float mx[6], inv[6];
#pragma unroll
          for (int u = 0; u < 6; ++u) {
#pragma unroll
            for (int e = 0; e < 2; ++e) {
              const f32x4 s = e ? s1[u] : s0[u];
              lg[u][e] = __builtin_elementwise_fma(f32x2{sel(odd, s[1], s[0]), sel(odd, s[3], s[2])}, lg[u][e], mk[u ? 1 : 0][e]);
            }
            mx[u] = max2(max2(lg[u][0][0], lg[u][0][1]), max2(lg[u][1][0], lg[u][1][1]));
          }
          u32x16 vv[2];
          auto pkv = [&](int t) AI {
#pragma unroll
            for (int i = 0; i < 2; ++i) { u32x2 y = pk4(m4[t] * cv[i] + (t ? qa[t - 1][4 + i] : ex[2 + i])); PINV(y); vv[i][2 * t] = y.x; vv[i][2 * t + 1] = y.y; }
          };
          across6(mx, [](float a, float b) AI { return max2(a, b); }, [](float a) AI { return a; }, pkv);
          pkv(4); pkv(5); pkv(6);
          PINV(vv[0]); PINV(vv[1]);
          SB();
#pragma unroll
          for (int u = 0; u < 6; ++u) {
#pragma unroll
            for (int e = 0; e < 2; ++e) {
              lg[u][e] -= mx[u];
              lg[u][e] = f32x2{__builtin_amdgcn_exp2f(lg[u][e][0]), __builtin_amdgcn_exp2f(lg[u][e][1])};
            }
            const f32x2 t = lg[u][0] + lg[u][1];
            inv[u] = t[0] + t[1];
          }
          across6(inv, [](float a, float b) AI { return a + b; }, [](float a) AI { return __builtin_amdgcn_rcpf(a); },
                  [&](int q) AI { for (int e = 3 * q; e < 3 * q + 3; ++e) lg[e >> 1][e & 1] *= rk[(e >> 1) + (e & 1)]; });
          f32x4 o0[6], o1[6];
          auto sto = [&](int u) AI {
            const u32x2 l = pk4(o0[u]), h = pk4(o1[u]);
            __builtin_amdgcn_raw_buffer_store_b128(u32x4{l.x, l.y, h.x, h.y}, rs_ws, ov + u * 16384, 0, AUX_SC1);
          };
          PRIO(3);
          SFOR(6, u,
            const f32x2 a = lg[u][0] * inv[u], b = lg[u][1] * inv[u];
            const unsigned wa_ = pack2(a[0], a[1]), wb_ = pack2(b[0], b[1]);
            const u32x4 pp = {__builtin_amdgcn_perm(wa_, wa_, p0), __builtin_amdgcn_perm(wa_, wa_, p1), __builtin_amdgcn_perm(wb_, wb_, p0), __builtin_amdgcn_perm(wb_, wb_, p1)};
            if constexpr (u >= 2) sto(u - 2);
            SB();
            o0[u] = mfma16(__builtin_shufflevector(vv[0], vv[0], 2 * u, 2 * u + 1, 2 * u + 2, 2 * u + 3), pp, Z4);
            o1[u] = mfma16(__builtin_shufflevector(vv[1], vv[1], 2 * u, 2 * u + 1, 2 * u + 2, 2 * u + 3), pp, Z4);
            SB();
          );
          sto(4); sto(5);
          SB();
          };
        if (m < 2) att(std::integral_constant<int, 0>{}); else att(std::integral_constant<int, 1>{});
    LANEVARS;
    DRAIN();
    if (arrive(lb, 6) == 3u) putflag(WS_OF + (rg * 8 + hp) * FS, lane);
    if (idx < 28) { u32x4 f2[6][4]; gemm2(std::true_type{}, IC(0), f2, IC(1)); }
    if (bid == EPB) {
      __builtin_amdgcn_s_sleep(64);
      poll_flags<16>((const GAS u64*)(ws + WS_UF + (m * 64 + lane) * FS), tag);
      if (arrive(lb, 8) == 3u) putflag(WS_E, lane);
    }
  }
  FINISH();
}

extern "C" void kernel_launch(void* const* d_in, const int* in_sizes, int n_in,
                              void* d_out, int out_size, void* d_ws, size_t ws_size,
                              hipStream_t stream) {
  (void)in_sizes; (void)n_in; (void)out_size; (void)ws_size;
  (void)hipFuncSetAttribute((const void*)fused_block, hipFuncAttributeMaxDynamicSharedMemorySize, LDS_BYTES);
  fused_block<<<256, 512, LDS_BYTES, stream>>>((const u16*)d_in[0], (const u16*)d_in[1], (const u16*)d_in[2],
                                               (const u16*)d_in[4], (const u16*)d_in[5], (const u16*)d_in[6],
                                               (const u16*)d_in[7], (u16*)d_out, (char*)d_ws);
}
```

```cpp
#include <hip/hip_runtime.h>
#include <stdint.h>
#include <type_traits>
#include <utility>

#define __float_as_uint(x) __builtin_bit_cast(unsigned, (float)(x))
#define __uint_as_float(x) __builtin_bit_cast(float, (unsigned)(x))
#define __all(p) (__builtin_amdgcn_ballot_w64(!(p)) == 0)
typedef unsigned short u16;
typedef unsigned long long u64;
using bfv8 = __attribute__((ext_vector_type(8))) __bf16;
using bfv2 = __attribute__((ext_vector_type(2))) __bf16;
using f32x4 = __attribute__((ext_vector_type(4))) float;
using f32x2 = __attribute__((ext_vector_type(2))) float;
using u32x2 = __attribute__((ext_vector_type(2))) unsigned int;
using u32x4 = __attribute__((ext_vector_type(4))) unsigned int;
using u32x16 = __attribute__((ext_vector_type(16))) unsigned int;
using s16x4 = __attribute__((ext_vector_type(4))) short;

#define LAS __attribute__((address_space(3)))
#define GAS __attribute__((address_space(1)))
#define AI __attribute__((always_inline))
#define SB() __builtin_amdgcn_sched_barrier(0)
#define DRAIN() asm volatile("s_waitcnt vmcnt(0)" ::: "memory")
#define WAITVM(n) asm volatile("s_waitcnt vmcnt(%0)" ::"n"(n) : "memory")
#define WAITLG() asm volatile("s_waitcnt lgkmcnt(0)" ::: "memory")
#define RLX_AGENT __ATOMIC_RELAXED, __HIP_MEMORY_SCOPE_AGENT
#define PRIO(n) __builtin_amdgcn_s_setprio(n)

constexpr int RQ1 = 2, RQ2 = 5, LA = 6, AS = 4;
constexpr int ADLY = 3, GDLY = 9, GDL2 = 4, SLP = 4;
constexpr int NB1 = 8, NE = 4, NA = 2, NSG = 2;
constexpr int S_ = 2048, D_ = 448, F_ = 1792, N3_ = 1344;
constexpr int AUX_SC1 = 16;
constexpr int RSRC_W3 = 0x00020000;

constexpr int FS = 128;
constexpr int WS_E = 0, WS_HF = 4096, WS_UF = WS_HF + 128 * FS, WS_OF = WS_UF + 168 * FS;
constexpr int WS_ST = 65536;
constexpr int KS_ = 28 * 1024;
constexpr int WS_H = 131072;
constexpr int WS_U = WS_H + 64 * KS_;
constexpr int WS_O = WS_U + 56 * KS_;
constexpr int WS_END = WS_O + 132 * 16384;

constexpr int L_LATE = 65536, SS = 16384;
constexpr int L_HB = 131072;
constexpr int L_CNT = 162176;
constexpr int L_SA = 162432;
constexpr int L_DUMMY = 162560;
constexpr int LDS_BYTES = 163840;
constexpr int HB_PITCH = 928;

__device__ __forceinline__ unsigned pack2(float a, float b) {
  bfv2 v; v[0] = (__bf16)a; v[1] = (__bf16)b;
  return __builtin_bit_cast(unsigned, v);
}
__device__ __forceinline__ float bfr(float f) { return __uint_as_float(pack2(f, 0.f) << 16); }
__device__ __forceinline__ float lo_f(unsigned u) { return __uint_as_float(u << 16); }
__device__ __forceinline__ float hi_f(unsigned u) { return __uint_as_float(u & 0xffff0000u); }
__device__ __forceinline__ f32x4 mfma16(u32x4 a, u32x4 b, f32x4 c) {
  return __builtin_amdgcn_mfma_f32_16x16x32_bf16(__builtin_bit_cast(bfv8, a), __builtin_bit_cast(bfv8, b), c, 0, 0, 0);
}
__device__ __forceinline__ float max2(float a, float b) { return __builtin_elementwise_maximum(a, b); }
template <bool FIRST> __device__ __forceinline__ void mfma_ip(f32x4& d, u32x4 a, u32x4 b) {
  if constexpr (FIRST) asm volatile("v_mfma_f32_16x16x32_bf16 %0, %1, %2, 0" : "=&v"(d) : "v"(a), "v"(b));
  else asm volatile("v_mfma_f32_16x16x32_bf16 %0, %1, %2, %0" : "+v"(d) : "v"(a), "v"(b));
}
template <class OP, class FN, class FL> __device__ __forceinline__ void across6(float (&v)[6], OP op, FN fin, FL fill) {
  auto s32 = [](float a, float b) AI { const auto r = __builtin_amdgcn_permlane32_swap(__float_as_uint(a), __float_as_uint(b), false, false); return f32x2{__uint_as_float(r[0]), __uint_as_float(r[1])}; };
  auto s16 = [](float a, float b) AI { const auto r = __builtin_amdgcn_permlane16_swap(__float_as_uint(a), __float_as_uint(b), false, false); return f32x2{__uint_as_float(r[0]), __uint_as_float(r[1])}; };
  __builtin_amdgcn_sched_barrier(0);
  fill(0);
  __builtin_amdgcn_sched_barrier(0);
  const f32x2 a = s32(v[0], v[2]), b = s32(v[1], v[3]), d = s32(v[4], v[5]);
  float x = op(a[0], a[1]), y = op(b[0], b[1]), z = op(d[0], d[1]), z2 = z;
  asm volatile("" : "+v"(z2));
  __builtin_amdgcn_sched_barrier(0);
  fill(1);
  __builtin_amdgcn_sched_barrier(0);
  const f32x2 e = s16(x, y), f = s16(z, z2);
  float p = fin(op(e[0], e[1])), q = fin(op(f[0], f[1])), p2 = p, q2 = q;
  asm volatile("" : "+v"(p2), "+v"(q2));
  __builtin_amdgcn_sched_barrier(0);
  fill(2);
  __builtin_amdgcn_sched_barrier(0);
  const f32x2 h = s16(p, p2), k = s32(q, q2);
  float h0 = h[0], h1 = h[1];
  asm volatile("" : "+v"(h0), "+v"(h1));
  __builtin_amdgcn_sched_barrier(0);
  fill(3);
  __builtin_amdgcn_sched_barrier(0);
  const f32x2 r = s32(h[0], h0), t = s32(h[1], h1);
  v[0] = r[0]; v[2] = r[1]; v[1] = t[0]; v[3] = t[1]; v[4] = k[0]; v[5] = k[1];
}
template <int CTRL>
__device__ __forceinline__ float dpp_add(float v) {
  return v + __uint_as_float(__builtin_amdgcn_update_dpp(0u, __float_as_uint(v), CTRL, 0xf, 0xf, true));
}
__device__ __forceinline__ void dma16(__amdgpu_buffer_rsrc_t rs, int voff, int soff, int dst) {
  asm volatile("s_mov_b32 m0, %3\n\ts_nop 0\n\tbuffer_load_dwordx4 %0, %1, %2 offen lds" ::"v"(voff), "s"(rs), "s"(soff), "s"(dst) : "memory");
}
__device__ __forceinline__ void touch(__amdgpu_buffer_rsrc_t rs, int voff, int soff, int dst) {
  asm volatile("s_mov_b32 m0, %3\n\ts_nop 0\n\tbuffer_load_dword %0, %1, %2 offen lds" ::"v"(voff), "s"(rs), "s"(soff), "s"(dst) : "memory");
}
__device__ __forceinline__ void ldx4(u32x4 (&a)[4], __amdgpu_buffer_rsrc_t rs, int voff, int soff) {
  asm volatile("buffer_load_dwordx4 %0, %4, %5, %6 offen\n\tbuffer_load_dwordx4 %1, %4, %5, %6 offen offset:224\n\t"
               "buffer_load_dwordx4 %2, %4, %5, %6 offen offset:448\n\tbuffer_load_dwordx4 %3, %4, %5, %6 offen offset:672"
               : "=&v"(a[0]), "=&v"(a[1]), "=&v"(a[2]), "=&v"(a[3]) : "v"(voff), "s"(rs), "s"(soff) : "memory");
}
__device__ __forceinline__ void dma4(__amdgpu_buffer_rsrc_t rs, int v, int so, int step, int dst) {
  const int s1 = so + step - 1024, s2 = so + 2 * step - 2048, s3 = so + 3 * step - 3072;
  asm volatile("s_mov_b32 m0, %6\n\ts_nop 0\n\t"
               "buffer_load_dwordx4 %0, %1, %2 offen lds\n\t"
               "buffer_load_dwordx4 %0, %1, %3 offen offset:1024 lds\n\t"
               "buffer_load_dwordx4 %0, %1, %4 offen offset:2048 lds\n\t"
               "buffer_load_dwordx4 %0, %1, %5 offen offset:3072 lds"
               ::"v"(v), "s"(rs), "s"(so), "s"(s1), "s"(s2), "s"(s3), "s"(dst) : "memory");
}
__device__ __forceinline__ void ld4(u32x4 (&a)[4], __amdgpu_buffer_rsrc_t rs, int v, int so, int step) {
  asm volatile("buffer_load_dwordx4 %0, %4, %5, %6 offen\n\tbuffer_load_dwordx4 %1, %4, %5, %7 offen\n\t"
               "buffer_load_dwordx4 %2, %4, %5, %8 offen\n\tbuffer_load_dwordx4 %3, %4, %5, %9 offen"
               : "=&v"(a[0]), "=&v"(a[1]), "=&v"(a[2]), "=&v"(a[3])
               : "v"(v), "s"(rs), "s"(so), "s"(so + step), "s"(so + 2 * step), "s"(so + 3 * step) : "memory");
}
template <int N> __device__ __forceinline__ void wait4(u32x4 (&a)[4]) {
  asm volatile("s_waitcnt vmcnt(%4)" : "+v"(a[0]), "+v"(a[1]), "+v"(a[2]), "+v"(a[3]) : "n"(N) : "memory");
}
__device__ __forceinline__ void turn_pair(u32x4 (&a)[4], u32x4 (&b)[4]) {
  unsigned xa[8][2], xb[8][2];
#pragma unroll
  for (int r = 0; r < 8; ++r)
#pragma unroll
    for (int e = 0; e < 2; ++e) {
      const u32x4 v = r < 4 ? a[r] : b[r - 4];
      xa[r][e] = __builtin_amdgcn_update_dpp(v[e], v[2 + e], 0x128, 0xf, 0xc, false);
      xb[r][e] = __builtin_amdgcn_update_dpp(v[2 + e], v[e], 0x128, 0xf, 0x3, false);
    }
#pragma unroll
  for (int j = 0; j < 4; ++j)
#pragma unroll
    for (int q = 0; q < 4; ++q) {
      a[j][q] = __builtin_amdgcn_perm(xa[2 * q + 1][j >> 1], xa[2 * q][j >> 1], (j & 1) ? 0x07060302u : 0x05040100u);
      b[j][q] = __builtin_amdgcn_perm(xb[2 * q + 1][j >> 1], xb[2 * q][j >> 1], (j & 1) ? 0x07060302u : 0x05040100u);
    }
#pragma unroll
  for (int j = 0; j < 4; ++j) asm volatile("" : "+v"(a[j]), "+v"(b[j]));
}
__device__ __forceinline__ u32x2 tr_read(const LAS char* p) {
  return __builtin_bit_cast(u32x2, __builtin_amdgcn_ds_read_tr16_b64_v4i16((LAS s16x4*)p));
}
__device__ __forceinline__ float ln_sd(float var) { return bfr(__builtin_amdgcn_sqrtf(bfr(bfr(var) + 1e-5f))); }

__device__ __forceinline__ float row_sum(float v) {
  v = dpp_add<0xB1>(v);
  v = dpp_add<0x4E>(v);
  v = dpp_add<0x141>(v);
  return dpp_add<0x140>(v);
}
__device__ __forceinline__ unsigned ln2_rows(u32x4 (&f)[4], bool act) {
  f32x2 d[16];
#pragma unroll
  for (int e = 0; e < 16; ++e) d[e] = f32x2{lo_f(f[e >> 2][e & 3]), hi_f(f[e >> 2][e & 3])};
  f32x2 t[4];
#pragma unroll
  for (int e = 0; e < 4; ++e) t[e] = (d[e] + d[4 + e]) + (d[8 + e] + d[12 + e]);
  t[0] = (t[0] + t[1]) + (t[2] + t[3]);
  const float mean = row_sum(t[0][0] + t[0][1]) * (1.0f / D_);
  const float mr = bfr(mean);
  const float mb = act ? mr : 0.f;
#pragma unroll
  for (int e = 0; e < 16; ++e) d[e] -= f32x2{mb, mb};
#pragma unroll
  for (int e = 0; e < 4; ++e) t[e] = d[e] * d[e];
#pragma unroll
  for (int e = 4; e < 16; ++e) t[e & 3] = __builtin_elementwise_fma(d[e], d[e], t[e & 3]);
  t[0] = (t[0] + t[1]) + (t[2] + t[3]);
  const float dm = mean - mr;
  const float var = fmaxf(row_sum(t[0][0] + t[0][1]) * (1.0f / D_) - dm * dm, 0.f);
  const float s1 = ln_sd(var), r1 = __builtin_amdgcn_rcpf(s1);
  const float r2 = __builtin_amdgcn_rcpf(ln_sd(var * r1 * r1));
  const float a = r1 * r2, cc = -bfr(dm * r1) * r2;
#pragma unroll
  for (int e = 0; e < 16; ++e) {
    const f32x2 v = __builtin_elementwise_fma(d[e], f32x2{a, a}, f32x2{cc, cc});
    f[e >> 2][e & 3] = pack2(v[0], v[1]);
  }
  return (__float_as_uint(mr) >> 16) | __float_as_uint(s1);
}
__device__ __forceinline__ float solo(float v) { asm("" : "+v"(v)); return v; }
__device__ __forceinline__ float vadd(float a, float b) { return solo(a + b); }
__device__ __forceinline__ float vmul(float a, float b) { return solo(a * b); }
__device__ __forceinline__ f32x4 add4(f32x4 a, f32x4 b) { return f32x4{vadd(a[0], b[0]), vadd(a[1], b[1]), vadd(a[2], b[2]), vadd(a[3], b[3])}; }

constexpr unsigned SPIN_CAP = 1u << 18;
template <int SL>
__device__ __forceinline__ void poll_flags(const GAS u64* p, u64 tag) {
#pragma unroll 1
  for (unsigned spins = 0; spins < SPIN_CAP; ++spins) {
    const u64 v = __hip_atomic_load(p, RLX_AGENT);
    if (__all(v == tag)) break;
    __builtin_amdgcn_s_sleep(SL);
  }
  __builtin_amdgcn_fence(__ATOMIC_ACQUIRE, "wavefront");
}

__device__ __forceinline__ unsigned arrive(int lb, int i) {
  unsigned r;
  asm volatile("s_waitcnt lgkmcnt(0)\n\ts_mov_b64 exec, 1\n\tds_add_rtn_u32 %0, %1, %2\n\ts_mov_b64 exec, -1\n\ts_waitcnt lgkmcnt(0)"
               : "=&v"(r) : "v"(lb + L_CNT + 4 * i), "v"(1u) : "memory");
  return __builtin_amdgcn_readfirstlane(r);
}
__device__ __forceinline__ void sig(int lb, int i) {
  asm volatile("s_waitcnt lgkmcnt(0)\n\ts_mov_b64 exec, 1\n\tds_add_u32 %0, %1\n\ts_mov_b64 exec, -1" ::"v"(lb + L_CNT + 4 * i), "v"(1u) : "memory");
}
template <int I> __device__ __forceinline__ void post(int sa, unsigned one) {
  asm volatile("ds_add_u32 %0, %1 offset:%2" ::"v"(sa), "v"(one), "n"(4 * I) : "memory");
}
__device__ __forceinline__ unsigned look(LAS char* L, int i) {
  return __hip_atomic_load((LAS unsigned*)(L + L_CNT) + i, __ATOMIC_RELAXED, __HIP_MEMORY_SCOPE_WORKGROUP);
}
__device__ __forceinline__ void check(LAS char* L, unsigned v, int i, unsigned target) {
  unsigned t, n, a;
  asm volatile("s_nop 0\n\tv_readfirstlane_b32 %0, %3\n\ts_cmp_lt_u32 %0, %5\n\ts_cbranch_scc1 .Lcw%=\n.Lcb%=:\n\t.subsection 1\n"
               ".Lcw%=:\n\ts_mov_b32 %1, 0x400000\n\tv_mov_b32 %2, %4\n"
               ".Lcl%=:\n\tds_read_b32 %3, %2\n\ts_waitcnt lgkmcnt(0)\n\tv_readfirstlane_b32 %0, %3\n\ts_cmp_ge_u32 %0, %5\n\t"
               "s_cbranch_scc1 .Lcb%=\n\ts_sleep 0\n\ts_sub_u32 %1, %1, 1\n\ts_cbranch_scc0 .Lcl%=\n\ts_branch .Lcb%=\n\t.subsection 0"
               : "=&s"(t), "=&s"(n), "=&v"(a), "+v"(v) : "s"((int)(unsigned)(size_t)L + L_CNT + 4 * i), "n"(target) : "memory", "scc");
}
template <int SL = 0> __device__ __forceinline__ void await_at(int a, unsigned target) {
  unsigned t, n, v;
  asm volatile("s_mov_b32 %1, 0x400000\n.Lal%=:\n\tds_read_b32 %2, %3\n\ts_waitcnt lgkmcnt(0)\n\tv_readfirstlane_b32 %0, %2\n\ts_cmp_ge_u32 %0, %4\n\t"
               "s_cbranch_scc1 .Lab%=\n\ts_sleep %5\n\ts_sub_u32 %1, %1, 1\n\ts_cbranch_scc0 .Lal%=\n.Lab%=:"
               : "=&s"(t), "=&s"(n), "=&v"(v) : "v"(a), "n"(target), "n"(SL) : "memory", "scc");
}
template <int SL = 0> __device__ __forceinline__ void await(LAS char* L, int i, unsigned target) { await_at<SL>((int)(unsigned)(size_t)L + L_CNT + 4 * i, target); }

#define BAR() __builtin_amdgcn_s_barrier()
#define LANEVARS                                                                                         \
  int lane;                                                                                              \
  asm volatile("v_mbcnt_lo_u32_b32 %0, -1, 0\n\tv_mbcnt_hi_u32_b32 %0, -1, %0" : "=v"(lane));             \
  const int c = lane & 15, g = lane >> 4, q4 = c >> 2, p4 = c & 3, rr = lane >> 3;                       \
  (void)c; (void)g; (void)q4; (void)p4; (void)rr
constexpr f32x4 Z4 = {0.f, 0.f, 0.f, 0.f};
constexpr int OOB = 0x40000000;
template <class T> __device__ __forceinline__ T sel(bool c, T a, T b) { return __builtin_unpredictable(c) ? a : b; }
__device__ __forceinline__ int trbase(int i, int g, int q4, int p4) {
  return (4 * g + q4) * 128 + (((2 * i + (p4 >> 1)) ^ (2 * ((q4 >> 1) | ((g & 1) << 1)))) << 4) + 8 * (p4 & 1);
}
__device__ __forceinline__ void slot_frags(const LAS char* L, const int (&tb)[4], int so, u32x4 (&a)[4]) {
#pragma unroll
  for (int i = 0; i < 4; ++i) {
    const u32x2 lo = tr_read(L + tb[i] + so), hi = tr_read(L + tb[i] + so + 2048);
    a[i] = u32x4{lo.x, lo.y, hi.x, hi.y};
  }
}
__device__ __forceinline__ u64 peek(__amdgpu_buffer_rsrc_t rs, int off) {
  u64 v;
  asm volatile("buffer_load_dwordx2 %0, %1, %2, 0 offen sc1" : "=v"(v) : "v"(off), "s"(rs) : "memory");
  return v;
}
__device__ __forceinline__ void settle(__amdgpu_buffer_rsrc_t rs, int off, u64 first, u64 tag) {
  unsigned n;
  asm volatile("v_cmp_ne_u64 vcc, %4, %1\n\ts_cbranch_vccz .Lsb%=\n\ts_mov_b32 %0, 0x40000\n"
               ".Lsl%=:\n\tbuffer_load_dwordx2 %1, %2, %3, 0 offen sc1\n\ts_waitcnt vmcnt(0)\n\tv_cmp_ne_u64 vcc, %4, %1\n\t"
               "s_cbranch_vccz .Lsb%=\n\ts_sleep %5\n\ts_sub_u32 %0, %0, 1\n\ts_cbranch_scc0 .Lsl%=\n.Lsb%=:"
               : "=&s"(n), "+v"(first) : "v"(off), "s"(rs), "s"(tag), "n"(SLP) : "memory", "scc", "vcc");
}
template <class F, int... I> __device__ __forceinline__ void static_for(F f, std::integer_sequence<int, I...>) { (f(std::integral_constant<int, I>{}), ...); }
#define SFOR(n, var, ...) static_for([&](auto var##_c) AI { constexpr int var = decltype(var##_c)::value; __VA_ARGS__ }, std::make_integer_sequence<int, n>{})
template <int OFF> __device__ __forceinline__ void ld1_a(u32x4& v, __amdgpu_buffer_rsrc_t rs, int vo, int so) {
  asm volatile("buffer_load_dwordx4 %0, %1, %2, %3 offen offset:%4 sc1" : "=v"(v) : "v"(vo), "s"(rs), "s"(so), "n"(OFF) : "memory");
}
template <int N> __device__ __forceinline__ void wait1(u32x4& a) { asm volatile("s_waitcnt vmcnt(%1)" : "+v"(a) : "n"(N) : "memory"); }
template <int N> __device__ __forceinline__ void wait2(u32x4& a, u32x4& b) { asm volatile("s_waitcnt vmcnt(%2)" : "+v"(a), "+v"(b) : "n"(N) : "memory"); }

template <int NK, int NR, int NB, int GR, class LD, class PUT, class HK>
__device__ __forceinline__ void kloop(LAS char* L, const int (&tb)[4], u32x4 (&fr)[NR][4], u32x4 (&hv)[NB][4],
                                      f32x4 (&acc)[4][4], LD ld, PUT put, HK hook) {
  constexpr int KX = NK - 4;
  SFOR(KX, k,
    u32x2 lo;
    SFOR(4, t,
      constexpr int n = k * 4 + t, i0 = k < GR ? (NB - 1) * 4 + n : k == GR ? (NB - 1) * 4 + n + t : NB * 4 + n;
      wait1<i0 - n - 1>(hv[k % NB][t]);
      SFOR(4, i,
        acc[i][t] = mfma16(hv[k % NB][t], fr[k][i], k == 0 ? Z4 : acc[i][t]);
        asm volatile("" : "+v"(acc[i][t]));
        constexpr int e = t * 4 + i;
        if constexpr (k >= 1 && k - 1 + NR < NK && e < 8) {
          const LAS char* q = L + tb[e >> 1] + (k - 1) * SS;
          if constexpr (e & 1) { const u32x2 hi = tr_read(q + 2048); fr[k - 1][e >> 1] = u32x4{lo.x, lo.y, hi.x, hi.y}; }
          else lo = tr_read(q);
        }
        SB();
      );
      ld(std::integral_constant<int, i0 / 4>{}, std::integral_constant<int, i0 % 4>{});
      if constexpr (k == GR) ld(std::integral_constant<int, (i0 + 1) / 4>{}, std::integral_constant<int, (i0 + 1) % 4>{});
      SB();
    );
  );
  SFOR(4, t,
    constexpr int k = KX + t, is = (k + NB < NK ? k + NB : NK) - k - 1;
    wait4<4 * is>(hv[k % NB]);
    if constexpr (t == 0) hook();
    SFOR(16, e,
      constexpr int i = e >> 2, h = e & 3, d = 4 * t + i - 1;
      if constexpr (NK > NR) {
        acc[t][i] = mfma16(fr[(KX + h) % NR][i], hv[k % NB][h], acc[t][i]);
        asm volatile("" : "+v"(acc[t][i]));
      } else {
        acc[i][t] = mfma16(hv[k % NB][h], fr[KX + h][i], acc[i][t]);
        asm volatile("" : "+v"(acc[i][t]));
      }
      if constexpr (h == 3 && d >= 0) put(std::integral_constant<int, d & 3>{}, std::integral_constant<int, d / 4>{});
      SB();
    );
    if constexpr (k + NB < NK) { SFOR(4, h, ld(std::integral_constant<int, k + NB>{}, h_c);); }
    SB();
  );
  put(std::integral_constant<int, 3>{}, std::integral_constant<int, 3>{});
}
template <int N> __device__ __forceinline__ void gelu_pack(const f32x4 (&v)[N], unsigned (&pr)[2 * N]) {
  f32x4 z[N];
#pragma unroll
  for (int e = 0; e < N; ++e) z[e] = v[e] * (v[e] * v[e] * -0.1029432395800235f - 2.302208198144325f);
  SB();
#pragma unroll
  for (int e = 0; e < 4 * N; ++e) z[e >> 2][e & 3] = __builtin_amdgcn_exp2f(z[e >> 2][e & 3]);
  SB();
#pragma unroll
  for (int e = 0; e < N; ++e) z[e] += 1.0f;
#pragma unroll
  for (int e = 0; e < 4 * N; ++e) z[e >> 2][e & 3] = __builtin_amdgcn_rcpf(z[e >> 2][e & 3]);
  SB();
#pragma unroll
  for (int e = 0; e < N; ++e) { z[e] *= v[e]; pr[2 * e] = pack2(z[e][0], z[e][1]); pr[2 * e + 1] = pack2(z[e][2], z[e][3]); }
}
__device__ __forceinline__ float fold32(float a, float b) {
  const auto s = __builtin_amdgcn_permlane32_swap(__float_as_uint(a), __float_as_uint(b), false, false);
  return vadd(__uint_as_float(s[0]), __uint_as_float(s[1]));
}
#define LA_(a) ((LAS char*)(size_t)(unsigned)(a))
#define PINV(v) asm volatile("" : "+v"(v))
#define IC(n) std::integral_constant<int, (n)>{}
constexpr int g1buf(int nt, int t) { return ((t + 8 - nt) & 3) * SS; }
constexpr int L_B1 = 162816;
constexpr int A_W = 26624, A_WS = 24576, A_X1 = 100352, A_XS = 26624;
constexpr int A_MEAN = 153600, A_RSTD = A_MEAN + 1024, A_CS = A_RSTD + 1024;
#define FINISH() __builtin_amdgcn_endpgm()
__global__ __launch_bounds__(512, 2) void fused_block(const u16*, const u16*, const u16*, const u16*, const u16*, const u16*,
                                                      const u16*, u16*, char*) {
  extern __shared__ __attribute__((aligned(16))) char smem[];
  LAS char* L = (LAS char*)smem;
  const int lb = (int)(unsigned)(size_t)L;
  const auto kp = __builtin_amdgcn_kernarg_segment_ptr();
  u32x16 ka; u32x2 kb;
  asm volatile("s_load_dwordx16 %0, %2, 0x0\n\ts_load_dwordx2 %1, %2, 0x40" : "=&s"(ka), "=&s"(kb) : "s"(kp));
  SB();
  const int w = __builtin_amdgcn_readfirstlane((int)threadIdx.x >> 6);
  const int bid = blockIdx.x, xcd = bid & 7, idz = bid >> 3;
  const int m = w & 3;
#define ARGP(i) ((const u16*)(((u64)ka[2 * (i) + 1] << 32) | ka[2 * (i)]))
#define ARGWAIT() asm volatile("s_waitcnt lgkmcnt(0)" : "+s"(ka), "+s"(kb) : "s"(kp) : "memory")
  u64 tag;
  const int un = xcd * 16 + idz, row0 = 32 * (un >> 1) + 8 * (un & 1);
  const bool isln = idz < 16;
  u32x4 hx[4];
  u64 e1;
  if (isln && w >= 4) {
    PRIO(3);
    int lv = sel(((int)threadIdx.x & 15) < 14, (((int)threadIdx.x >> 4) & 3) * (D_ * 2) + ((int)threadIdx.x & 15) * 16, OOB);
    int ls = (row0 + 4 * (m & 1) + 8 * (m & 2)) * (D_ * 2), z = 0;
    asm volatile("" : "+v"(lv), "+v"(z));
    asm volatile("" : "+s"(ls));
    ARGWAIT();
    ldx4(hx, __builtin_amdgcn_make_buffer_rsrc((void*)ARGP(0), 0, S_ * D_ * 2, RSRC_W3), lv, ls);
    e1 = peek(__builtin_amdgcn_make_buffer_rsrc((void*)(((u64)kb[1] << 32) | kb[0]), 0, WS_END, RSRC_W3), z);
    BAR();
  } else {
    u64 pc;
    unsigned co = sel(isln, 0, (((idz - 16) * 8 + w) * 8 + ((int)threadIdx.x & 7)) * 128);
    if (!isln) asm volatile(".Lfb_start:\n\ts_getpc_b64 %0\n\ts_mov_b32 m0, %2\n\tv_min_u32 %1, .Lfb_end-.Lfb_start-8, %1\n\t"
                 "global_load_lds_dword %1, %0\n\t.subsection 2\n.Lfb_end:\n\ts_endpgm\n\t.subsection 0" : "=&s"(pc), "+v"(co) : "s"(lb + L_DUMMY) : "memory");
    *((LAS unsigned*)(L + L_CNT) + ((int)threadIdx.x & 31)) = 0u;
  }
  SB();
  auto gettag = [&]() AI {
    const u32x2 tv = *(const LAS u32x2*)(L + L_CNT + 128);
    tag = ((u64)(unsigned)__builtin_amdgcn_readfirstlane((int)tv.y) << 32) | (unsigned)__builtin_amdgcn_readfirstlane((int)tv.x);
  };

  const int ng = 24 - 6 * (xcd & 1);
  const int k4 = idz + ng - 32, k5 = idz - 16 - 4 * (xcd & 1);
  const bool isg = k4 >= 0;
  const int lp = sel(k5 < 0, ((0x24 - 3 * ((8 >> xcd) & 1) + 0xc0) >> (k4 & 6)) & 3, k5 >> 2);
  int ft = 7 * (xcd >> 1) + 4 * (xcd & 1) + lp, dg = sel(k5 < 0, 4 + (idz & 1), idz & 3);
  int idx = sel(isg, 6 * lp + dg, ng + idz);
  asm volatile("" : "+s"(idx));
  int off1 = (0xda8520 >> (4 * dg)) & 15;
  const int q = 22 * (xcd >> 2) + 8 * (xcd & 1) + idx - ng;
  int hp = 2 * (xcd & 2) + (q & 3), rg = q >> 2;
  int rotq = RQ1 * (q & 3) + RQ2 * rg + 70;
  rotq -= 7 * ((rotq * 147) >> 10);
  constexpr int EPB = 56;
  auto k1 = [&](int j) AI { return (j + off1) & 15; };
  int reg = (w >> 2) * L_LATE + m * 4096;
  int fv, fw, fs, fs1, role = (isg ? 0 : 2) + (w >> 2);
  {
    const int l = (int)threadIdx.x & 63, rr = l >> 3, ch = l & 7;
    const int v1 = ((m * 512 + rr) * F_ + ft * 64) * 2 + (ch ^ (2 * ((rr >> 1) & 3))) * 16;
    const int cq = ch ^ (2 * ((rr >> 1) & 1)) ^ (4 * (m & 1));
    const int vq = sel(cq < 7, (8 * m + rr) * (N3_ * 2) + hp * 112 + cq * 16, OOB);
    const int va = (192 * rg + 16 + 8 * m + rr) * (D_ * 2) + (ch ^ (4 * (m & 1) + (rr >> 1))) * 16;
    fv = sel(isg, v1, vq);
    fw = va;
    fs = sel(isg, k1(0) * 32 * (F_ * 2), rotq * (64 * N3_ * 2));
    fs1 = sel(isg, k1(1) * 32 * (F_ * 2), rotq * 128);
  }
  asm volatile("" : "+v"(fv), "+v"(fw));
  asm volatile("" : "+s"(fs), "+s"(fs1), "+s"(ft), "+s"(dg), "+s"(reg), "+s"(off1), "+s"(rotq), "+s"(role), "+s"(hp), "+s"(rg));
  const int r0 = 192 * rg;
  ARGWAIT();
  const u16 *x = ARGP(0), *wqkv = ARGP(1), *wo = ARGP(2), *w1 = ARGP(3), *b1 = ARGP(4), *w2 = ARGP(5), *b2 = ARGP(6);
  u16* out = (u16*)ARGP(7);
  char* ws = (char*)(((u64)kb[1] << 32) | kb[0]);
  const auto rs_x = __builtin_amdgcn_make_buffer_rsrc((void*)x, 0, S_ * D_ * 2, RSRC_W3);
  const auto rs_ws = __builtin_amdgcn_make_buffer_rsrc((void*)ws, 0, WS_END, RSRC_W3);
  const auto rs_w1 = __builtin_amdgcn_make_buffer_rsrc((void*)w1, 0, S_ * F_ * 2, RSRC_W3);
  const auto rs_w2 = __builtin_amdgcn_make_buffer_rsrc((void*)w2, 0, F_ * S_ * 2, RSRC_W3);
  auto putflag = [&](int off, int lane) AI {
    __builtin_amdgcn_raw_buffer_store_b128(u32x4{(unsigned)tag, (unsigned)(tag >> 32), (unsigned)tag, (unsigned)(tag >> 32)}, rs_ws,
                                           sel(lane < 8, off + lane * 16, OOB), 0, AUX_SC1);
  };
  auto mktag = [&](u64 e0) AI {
    e0 = e0 * 0x9E3779B97F4A7C15ull + 0x632BE59BD9B4E019ull;
    *(LAS u64*)(L + L_CNT + 128) = e0;
    tag = ((u64)(unsigned)__builtin_amdgcn_readfirstlane((int)(e0 >> 32)) << 32) | (unsigned)__builtin_amdgcn_readfirstlane((int)e0);
  };
  const auto rs_wq = __builtin_amdgcn_make_buffer_rsrc((void*)wqkv, 0, D_ * N3_ * 2, RSRC_W3);
  auto piece = [&](bool rows, int fv, int sg, int so, int e) AI {
    if (rows) {
      if (e < 6 || m < 2) dma16(rs_x, e ? fv : sel(fv >= 32 * D_ * 2, fv - 32 * D_ * 2, OOB), so + (e ? e - 1 : 0) * 32 * (D_ * 2), lb + (sg ? A_X1 + (sg - 1) * A_XS : 0) + (m + 4 * e) * 1024);
    } else {
      dma16(rs_wq, fv, so + (e / 3) * 32 * (N3_ * 2) + (e % 3) * 896, lb + A_W + sg * A_WS + e * 4096 + m * 1024);
    }
  };
  if (role == 0) {
    if (isln) { BAR(); __builtin_amdgcn_s_sleep(GDL2); }
    __builtin_amdgcn_s_sleep(GDLY);
    dma4(rs_w1, fv, fs, 8 * F_ * 2, lb + reg);
    dma4(rs_w1, fv, fs1, 8 * F_ * 2, lb + reg + SS);
  }
  asm volatile("" : "+s"(role));
  if (!isln || role == 2) BAR();
  if (role == 2) {
    __builtin_amdgcn_s_sleep(ADLY);
#pragma unroll
    for (int e = 0; e < 13; ++e) {
      if (e == AS) await(L, 7, 4u);
      if (e < 6) piece(false, fv, 2, fs, e); else piece(true, fw, 2, fs1, e - 6);
    }
  }
  asm volatile("" : "+s"(role));
  if (!(role & 1)) {
  } else if (isln) {
    const int hbase = sel(isg, L_HB, A_W + A_WS);
    PRIO(3);
    LANEVARS;
    const bool act = c < 14;
    int hb = sel(act, hbase + (4 * m + g) * HB_PITCH + c * 16, L_DUMMY);
    int tp = hbase + (4 * (g & 1) + q4) * HB_PITCH + (16 * (g >> 1) + 4 * p4) * 2 + m * 64;
    int ho = WS_H + (un >> 1) * KS_ + (2 * m + (g >> 1)) * 1024 + (2 * (un & 1) + (g & 1)) * 256 + c * 16;
    int h3 = sel(m < 2, ho + 3 * 8192, sel((lane < 16) & (m == 3), WS_ST + (row0 + (c & 7) + 2 * (c & 8)) * 16, OOB));
    int sn = sel(lane == 0, lb + L_CNT, OOB), sw = lb + L_SA + (4 * m + g) * 4, sr = lb + L_SA + c * 4, cn = lb + L_CNT, h1 = ho + 8192, h2 = ho + 16384;
    unsigned one = 1u;
    hb += lb; tp += lb;
    asm volatile("" : "+v"(hb), "+v"(tp), "+v"(ho), "+v"(h3), "+v"(sn), "+v"(one), "+v"(sw), "+v"(sr), "+v"(cn), "+v"(h1), "+v"(h2));
    asm volatile("s_waitcnt vmcnt(1)" : "+v"(hx[0]), "+v"(hx[1]), "+v"(hx[2]), "+v"(hx[3])::"memory");
    const unsigned sa = ln2_rows(hx, act);
#pragma unroll
    for (int e = 0; e < 4; ++e) *(LAS u32x4*)(LA_(hb) + e * 224) = hx[e];
    *(LAS unsigned*)LA_(sw) = sa;
    SB();
    post<0>(sn, one);
    await_at(cn, 4u);
    u32x4 hs[4];
#pragma unroll
    for (int u = 0; u < 4; ++u) {
      const u32x2 a0 = tr_read(LA_(tp) + u * 256), a1 = tr_read(LA_(tp) + u * 256 + 8 * HB_PITCH);
      hs[u] = u32x4{a0.x, a0.y, a1.x, a1.y};
      SB();
    }
    const unsigned sb = *(const LAS unsigned*)LA_(sr);
    SB();
#pragma unroll
    for (int u = 0; u < 3; ++u) __builtin_amdgcn_raw_buffer_store_b128(hs[u], rs_ws, u == 0 ? ho : u == 1 ? h1 : h2, 0, AUX_SC1);
    SB();
    asm volatile("s_waitcnt vmcnt(3)" : "+v"(e1)::"memory");
    mktag(e1);
    if (m == 3) hs[3] = u32x4{sb, (unsigned)tag, sb, (unsigned)(tag >> 32)};
    __builtin_amdgcn_raw_buffer_store_b128(hs[3], rs_ws, h3, 0, AUX_SC1);
    SB();
    post<7>(sn, one);
    DRAIN();
    if (arrive(lb, 1) == 3u) putflag(WS_HF + un * FS, lane);
  } else {
    u64 e0 = peek(rs_ws, WS_E);
    asm volatile("s_waitcnt vmcnt(0)" : "+v"(e0)::"memory");
    mktag(e0);
    sig(lb, 7);
  }
  asm volatile("" : "+s"(role));
  int idy = idx;
  asm volatile("" : "+s"(idy));
  const u64 tm = idy < 12 ? 0x59d28518a320820ull : idy < 24 ? 0xbdab49ca2f839acull : 0x18820ffbbcdeb38ull;
  const int tile = sel(xcd & 1, (int)(tm >> (5 * (idy - 12 * ((idy * 43) >> 9)))) & 31, idy);
  const int st = 4 * (xcd ^ 1) + (tile & 3), dt = tile >> 2;
  const int rotl = dt % 6;
  auto pos2 = [&](bool late, int p) AI { const int v = p + rotl; return 14 * m + (late ? 8 + (v >= 6 ? v - 6 : v) : (p + 2 * dt) & 7); };
  auto ldw2 = [&](u32x4 (&a)[4], int vr, int p) AI { ld4(a, rs_w2, vr, (pos2(false, p & 6) * 32 + 16 * (p & 1)) * (S_ * 2), S_ * 2); };
  auto w2lane = [&](int lane) AI { return ((32 * ((lane >> 3) & 1) + 4 * (lane >> 4)) * S_ + st * 64 + 8 * (lane & 7)) * 2; };
  auto gemm2 = [&](auto latec, auto nec, auto& f2, auto idc) AI {
    constexpr bool LATE = decltype(latec)::value;
    constexpr int NR2 = LATE ? 6 : 8, NK2 = LATE ? 10 : 8, N0 = decltype(nec)::value;
    {
      constexpr int NB2 = 3;
      u32x4 uv[NB2][4];
      f32x4 acc[4][4];
      {
        LANEVARS;
        PRIO(0);
        const int w2E = (rr * S_ + st * 64) * 2 + ((lane & 7) ^ (2 * ((rr >> 1) & 3))) * 16;
        int tb[4];
#pragma unroll
        for (int i = 0; i < 4; ++i) tb[i] = reg + trbase(i, g, q4, p4);
        const int v0 = lane * 16;
        const int ta = 4 * dt + 3 * ((lane >> 3) & 1), dgp = sel(ta < 20, (ta * 13) >> 6, (ta >> 2) - 1);
        const int fo = WS_UF + ((7 * m + min(lane & 7, 6)) * 6 + dgp) * FS;
        const int oo = WS_OF + (((st * 43) >> 7) * 8 + 2 * m + (lane & 1)) * FS;
        auto fill = [&](int p, int dst) AI { dma4(rs_w2, w2E, pos2(true, p) * 32 * (S_ * 2), 8 * S_ * 2, lb + dst); };
        constexpr int XS = L_HB - L_LATE;
        const auto rs_wo = __builtin_amdgcn_make_buffer_rsrc((void*)wo, 0, D_ * D_ * 2, RSRC_W3);
        const int woE = (rr * D_ + dt * 64) * 2 + ((lane & 7) ^ (2 * ((rr >> 1) & 3))) * 16;
        auto fillo = [&](int e) AI { dma4(rs_wo, woE, (4 * m + e) * 28 * (D_ * 2), 8 * D_ * 2, lb + L_LATE + m * 4096 + e * SS); };
        const int ub = WS_U + 4 * dt * 1024, ob = WS_O + (4 * st * 16 + 4 * m) * 1024;
        u64 o0 = 0;
        auto ldu = [&](auto pc, auto tc) AI {
          constexpr int p = decltype(pc)::value, t = decltype(tc)::value;
          if constexpr (LATE && p == 6 && t == 0) { asm volatile("s_waitcnt vmcnt(12)" : "+v"(o0)::"memory"); settle(rs_ws, oo, o0, tag); }
          if constexpr (LATE && p == 3 && t == 0) o0 = peek(rs_ws, oo);
          if constexpr (LATE && p >= 6) ld1_a<t * 1024>(uv[p % NB2][t], rs_ws, v0, ob + (p - 6) * 16384);
          else if constexpr (!LATE && p >= 4) ld1_a<(p - 4) * 1024>(uv[p % NB2][t], rs_ws, v0, ub + pos2(LATE, 4 + t) * KS_);
          else ld1_a<t * 1024>(uv[p % NB2][t], rs_ws, v0, ub + pos2(LATE, p) * KS_);
        };
        if constexpr (!LATE) {
          const int vr = w2lane(lane);
#pragma unroll
          for (int p = N0; p < 8; ++p) {
            ldw2(f2[p], vr, p);
            if (p == N0 + NSG - 1) { WAITVM(4 * NSG); sig(lb, 2); }
          }
          await(L, 3, 4u);
          await(L, 4, 4u);
          auto turn = [&](auto pc, auto nc) AI {
            constexpr int p = decltype(pc)::value, n = decltype(nc)::value;
            wait4<n>(f2[2 * p]);
            wait4<n>(f2[2 * p + 1]);
            turn_pair(f2[2 * p], f2[2 * p + 1]);
            SB();
          };
          fill(0, reg); fill(1, reg + SS);
          turn(IC(0), IC(32));
          await(L, 5, 4u);
          fill(2, reg + 2 * SS); fill(3, reg + 3 * SS);
          turn(IC(1), IC(32));
          fill(4, L_HB + m * 5120); fill(5, L_LATE + reg + 3 * SS);
          turn(IC(2), IC(32));
          fillo(0); fillo(1); fillo(2);
          WAITVM(12);
          sig(lb, 16 + m);
          u64 f0 = peek(rs_ws, fo);
          turn(IC(3), IC(13));
          asm volatile("s_waitcnt vmcnt(0)" : "+v"(f0)::"memory");
          settle(rs_ws, fo, f0, tag);
          sig(lb, 16 + m);
        } else {
          await(L, 16 + m, 1u);
          slot_frags(L, tb, 3 * SS, f2[5]);
          asm volatile("s_waitcnt lgkmcnt(0)" : "+v"(f2[5][0]), "+v"(f2[5][1]), "+v"(f2[5][2]), "+v"(f2[5][3])::"memory");
          fillo(3);
#pragma unroll
          for (int r = 0; r < 4; ++r) slot_frags(L, tb, r * SS - L_LATE, f2[r]);
          slot_frags(L, tb, XS + m * 1024, f2[4]);
          sig(lb, 20 + m);
          await(L, 16 + m, 2u);
        }
        PRIO(LATE ? 1 : 2);
        SFOR((NB2 - LATE) * 4, n, ldu(IC(n / 4), IC(n % 4)););
        SB();
        int wr[4], sa = sel(lane == 0, lb + L_CNT, OOB);
        unsigned one = 1u;
        if constexpr (LATE) { PINV(sa); PINV(one); }
#pragma unroll
        for (int e = 0; e < 4; ++e) wr[e] = reg + g * 256 + (LATE ? c ^ (2 * e + (g >> 1)) : (c + 8 * e + 4 * (g >> 1)) & 15) * 16;
        kloop<NK2, NR2, NB2, LATE ? 1 : -1>(L, tb, f2, uv, acc, ldu,
            [&](auto ic, auto tc) AI {
              constexpr int i = LATE ? decltype(tc)::value : decltype(ic)::value, t = LATE ? decltype(ic)::value : decltype(tc)::value;
              *(LAS f32x4*)(L + wr[(t & 1) + (LATE ? 2 * (i & 1) : 0)] + i * SS + t * 1024) = acc[i][t];
              if constexpr (LATE && (i & 1) && t == 3) { SB(); post<(i == 1 ? 31 : 10)>(sa, one); }
            }, [&]() AI { if constexpr (!LATE) await(L, 20 + m, 1u); });
      }
      if constexpr (!LATE) {
        PRIO(0);
        LANEVARS;
        const int rw = st * 64 + 8 * (rr & 3) + (rr & 4) + m, eo = (rw * D_ + dt * 64 + (lane & 7) * 8) * 2;
        u32x4 xr[2];
        float bs[2];
#pragma unroll
        for (int e = 0; e < 2; ++e) {
          xr[e] = __builtin_amdgcn_raw_buffer_load_b128(rs_x, eo + e * 32 * D_ * 2, 0, 0);
          bs[e] = lo_f(*(const GAS u16*)(b2 + rw + 32 * e));
        }
        int ra[2];
#pragma unroll
        for (int e = 0; e < 2; ++e) ra[e] = (m * 16 + ((lane & 7) >> 1)) * 1024 + (lane & 1) * 512 + ((4 * e + (rr & 3) + 2 * (rr & 4) + 4 * (lane & 3)) & 15) * 16;
        sig(lb, 9);
        await<2>(L, 9, 4u);
        f32x4 tm[4][4], base[4];
#pragma unroll
        for (int j = 0; j < 4; ++j)
#pragma unroll
          for (int e = 0; e < 4; ++e) tm[j][e] = *(const LAS f32x4*)(L + ra[e >> 1] + j * 4096 + (e & 1) * 256);
        SB();
#pragma unroll
        for (int e = 0; e < 4; ++e) {
          const unsigned a = xr[e >> 1][2 * (e & 1)], b = xr[e >> 1][2 * (e & 1) + 1];
          const float s = bs[e >> 1];
          base[e] = add4(add4(add4(f32x4{lo_f(a), hi_f(a), lo_f(b), hi_f(b)}, f32x4{s, s, s, s}), add4(tm[0][e], tm[1][e])), add4(tm[2][e], tm[3][e]));
          asm volatile("" : "+v"(base[e]));
        }
        int rl = L_LATE + ((rr >> 1) & 1) * SS + ((lane & 7) >> 1) * 1024 + (lane & 1) * 512 + ((8 * (rr & 1) + (rr & 4) + m) ^ ((lane & 3) + 2 * (rr & 2))) * 16;
        int e1 = eo + 32 * D_ * 2;
        asm volatile("" : "+v"(rl), "+v"(e1));
        const auto rs_out = __builtin_amdgcn_make_buffer_rsrc((void*)out, 0, S_ * D_ * 2, RSRC_W3);
        int ca = lb + L_CNT + 4 * 31, ev = eo, it = 0;
        f32x4 b0 = base[0], b1 = base[1];
        _Pragma("unroll 1") do {
          asm volatile("" : "+v"(ca), "+v"(ev), "+v"(rl), "+v"(b0), "+v"(b1));
          await_at(ca, 4u);
          SFOR(8, j, tm[j >> 1][j & 1] = *(const LAS f32x4*)(L + rl + (j >> 1) * 4096 + (j & 1) * 256););
          SB();
          const f32x4 s0 = (b0 + tm[0][0]) + tm[1][0] + (tm[2][0] + tm[3][0]), s1 = (b1 + tm[0][1]) + tm[1][1] + (tm[2][1] + tm[3][1]);
          __builtin_amdgcn_raw_buffer_store_b128(u32x4{pack2(s0[0], s0[1]), pack2(s0[2], s0[3]), pack2(s1[0], s1[1]), pack2(s1[2], s1[3])}, rs_out, ev, 0, AUX_SC1);
          SB();
          ca = lb + L_CNT + 4 * 10; ev = e1; rl += 2 * SS; b0 = base[2]; b1 = base[3];
          asm volatile("" : "+s"(it));
        } while (++it < 2);
      }
    }
    asm volatile("s_nop %0" ::"n"(decltype(idc)::value));
    FINISH();
  };
  auto utile = [&](auto latec) AI {
    constexpr bool LATE = decltype(latec)::value;
      auto gemm1 = [&](auto ntc) AI {
        constexpr int NT = decltype(ntc)::value, NL = 8 * NT, NB = NB1;
        const int t0 = NT == 5 ? 5 * dg : 4 * dg + 4;
        u32x4 fr[8][4], hv[NB], f2[LATE ? 6 : 8][4];
        LANEVARS;
        PRIO(LATE ? 0 : 1);
        const int v0 = lane * 16, v4 = v0 + 4096, wr = reg + v0;
        int ho[8];
        auto ld = [&](auto nc) AI {
          constexpr int n = decltype(nc)::value, t = n >> 3;
          ld1_a<(t & 3) * 1024>(hv[n % NB], rs_ws, t < 4 ? v0 : v4, ho[n & 7]);
        };
        auto start = [&]() AI {
          PRIO(LATE ? 2 : 1);
#pragma unroll
          for (int p = 0; p < 8; ++p) ho[p] = WS_H + (m * 16 + k1((LATE ? 8 : 0) + p)) * KS_ + t0 * 1024;
          SFOR(NB, n, ld(n_c););
          SB();
        };
        {
          const int w1E = ((m * 512 + rr) * F_ + ft * 64) * 2 + ((lane & 7) ^ (2 * ((rr >> 1) & 3))) * 16;
          auto fill = [&](int j) AI { dma4(rs_w1, w1E, k1(j) * 32 * (F_ * 2), 8 * F_ * 2, lb + reg + ((j >> 2) & 1) * L_LATE + (j & 3) * SS); };
          int tb[2][4];
#pragma unroll
          for (int i = 0; i < 8; ++i) tb[i >> 2][i & 3] = (i >> 2) * L_LATE + m * 4096 + trbase(i & 3, g, q4, p4);
          auto take = [&](int j) AI { slot_frags(L, tb[j >> 2], (j & 3) * SS, fr[j]); };
          auto taken = [&](int j) AI { asm volatile("s_waitcnt lgkmcnt(0)" : "+v"(fr[j][0]), "+v"(fr[j][1]), "+v"(fr[j][2]), "+v"(fr[j][3])::"memory"); };
          if constexpr (!LATE) {
            const int fo = WS_HF + (m * 32 + (lane & 31)) * FS;
            u64 f0;
            WAITVM(NT == 4 ? 0 : 8);
#pragma unroll
            for (int j = 2; j < 8; ++j) fill(j);
            await(L, 7, 4u);
            gettag();
            SFOR(8, j, WAITVM(28); take(j); taken(j); if constexpr (j == 7) f0 = peek(rs_ws, fo); fill(8 + j););
            WAITVM(17); sig(lb, 12 + m);
            asm volatile("s_waitcnt vmcnt(4)" : "+v"(f0)::"memory");
            settle(rs_ws, fo, f0, tag);
            sig(lb, 12 + m);
            start();
            WAITVM(NB); sig(lb, 12 + m);
          } else {
            if (m == 0) {
              const auto rs_b1 = __builtin_amdgcn_make_buffer_rsrc((void*)b1, 0, F_ * 2, RSRC_W3);
              await(L, 7, 4u);
              touch(rs_b1, sel(lane < 32, lane * 4, OOB), ft * 128, lb + L_B1);
            }
            await(L, 12 + m, 1u);
#pragma unroll
            for (int j = 0; j < 4; ++j) take(j);
            DRAIN();
            if (m == 0) sig(lb, 11);
            await(L, 12 + m, 2u);
            start();
            await(L, 12 + m, 3u);
#pragma unroll
            for (int j = 4; j < 8; ++j) take(j);
#pragma unroll
            for (int j = 0; j < 8; ++j) taken(j);
            sig(lb, 30);
          }
        }
        const int ra = (lane >> 5) * L_LATE + ((LATE ? 2 : 0) + (g & 1)) * 1024 + m * 256 + c * 16;
        const int ba = L_B1 + (16 * ((LATE ? 2 : 0) + (g & 1)) + 4 * m + 2 * (lane >> 5)) * 2;
        const int ub = WS_U + (2 * ft + (LATE ? 1 : 0)) * KS_ + t0 * 1024 + m * 256 + c * 16, uo = ub + g * 1024;
        const int vr = w2lane(lane);
        int sa = sel(lane == 0, lb + L_CNT, OOB);
        unsigned one = 1u;
        PINV(sa); PINV(one);
        f32x4 acc[2][4], pt[4];
        float bq[2], v[2], z[2];
        unsigned pr[6], cv, cr;
        auto rd = [&](int t) AI {
#pragma unroll
          for (int e = 0; e < 4; ++e) pt[e] = *(const LAS f32x4*)(L + ra + g1buf(NT, t) + e * 4096);
        };
        auto step = [&](auto sc, int t) AI {
          constexpr int s = decltype(sc)::value;
          if constexpr (s == 0) { pt[0] = add4(pt[0], pt[1]); PINV(pt[0]); }
          if constexpr (s == 1) { pt[2] = add4(pt[2], pt[3]); PINV(pt[2]); }
          if constexpr (s == 2) { pt[0] = add4(pt[0], pt[2]); PINV(pt[0]); }
          if constexpr (s == 3) {
#pragma unroll
            for (int e = 0; e < 2; ++e) { v[e] = vadd(fold32(pt[0][e], pt[0][2 + e]), bq[e]); PINV(v[e]); }
          }
          if constexpr (s == 4) {
#pragma unroll
            for (int e = 0; e < 2; ++e) {
              const float p = solo(__builtin_fmaf(vmul(v[e], v[e]), -0.1029432395800235f, -2.302208198144325f));
              z[e] = vmul(v[e], p); PINV(z[e]);
            }
          }
          if constexpr (s == 5) {
#pragma unroll
            for (int e = 0; e < 2; ++e) { z[e] = vadd(__builtin_amdgcn_exp2f(z[e]), 1.0f); PINV(z[e]); }
          }
          if constexpr (s == 6) {
            pr[t] = pack2(vmul(__builtin_amdgcn_rcpf(z[0]), v[0]), vmul(__builtin_amdgcn_rcpf(z[1]), v[1]));
            PINV(pr[t]);
          }
        };
        unsigned sw[4];
        auto fine = [&](auto sc, int t) AI {
          constexpr int s = decltype(sc)::value;
          if constexpr (s < 6) {
            constexpr int d = s < 2 || s >= 4 ? 0 : 2, o = s < 2 ? 1 : s < 4 ? 3 : 2, e = 2 * (s & 1);
            pt[d][e] = vadd(pt[d][e], pt[o][e]); pt[d][e + 1] = vadd(pt[d][e + 1], pt[o][e + 1]); PINV(pt[d]);
          }
          if constexpr (s == 6) {
#pragma unroll
            for (int e = 0; e < 2; ++e) {
              const auto r = __builtin_amdgcn_permlane32_swap(__float_as_uint(pt[0][e]), __float_as_uint(pt[0][2 + e]), false, false);
              sw[2 * e] = r[0]; sw[2 * e + 1] = r[1]; PINV(sw[2 * e]); PINV(sw[2 * e + 1]);
            }
          }
          auto two = [&](float (&d)[2], auto f) AI { d[0] = f(0); d[1] = f(1); PINV(d[0]); PINV(d[1]); };
          if constexpr (s == 7) two(v, [&](int e) AI { return vadd(__uint_as_float(sw[2 * e]), __uint_as_float(sw[2 * e + 1])); });
          if constexpr (s == 8) two(v, [&](int e) AI { return vadd(v[e], bq[e]); });
          if constexpr (s == 9) two(z, [&](int e) AI { return vmul(v[e], v[e]); });
          if constexpr (s == 10) two(z, [&](int e) AI { return solo(__builtin_fmaf(z[e], -0.1029432395800235f, -2.302208198144325f)); });
          if constexpr (s == 11) two(z, [&](int e) AI { return vmul(v[e], z[e]); });
          if constexpr (s == 12 || s == 13) { z[s - 12] = __builtin_amdgcn_exp2f(z[s - 12]); PINV(z[s - 12]); }
          if constexpr (s == 14) two(z, [&](int e) AI { return vadd(z[e], 1.0f); });
          if constexpr (s == 15 || s == 16) { z[s - 15] = __builtin_amdgcn_rcpf(z[s - 15]); PINV(z[s - 15]); }
          if constexpr (s == 17) two(z, [&](int e) AI { return vmul(z[e], v[e]); });
          if constexpr (s == 18) { pr[t] = pack2(z[0], z[1]); PINV(pr[t]); }
        };
        auto put = [&](unsigned a, unsigned b, unsigned cc, unsigned d, int off) AI {
          const auto s0 = __builtin_amdgcn_permlane32_swap(a, cc, false, false), s1 = __builtin_amdgcn_permlane32_swap(b, d, false, false);
          const auto r0 = __builtin_amdgcn_permlane16_swap(s0[0], s1[0], false, false), r1 = __builtin_amdgcn_permlane16_swap(s0[1], s1[1], false, false);
          __builtin_amdgcn_raw_buffer_store_b128(u32x4{r0[0], r1[0], r0[1], r1[1]}, rs_ws, off, 0, AUX_SC1);
          SB();
        };
        SFOR(NL, n,
          constexpr int t = n >> 3, p = n & 7, nw = !LATE && t == NT - 1 ? 4 * (p < NE ? p : NE) : 0;
          if constexpr (n == 9 && !LATE) check(L, cr, 30, 4u);
          if constexpr (n == 13) { check(L, cv, 11, 1u); const unsigned b = *(const LAS unsigned*)(L + ba); bq[0] = lo_f(b); bq[1] = hi_f(b); }
          if constexpr (t >= 2 && p == 2) { check(L, cv, 24 + t - 2, 8u); rd(t - 2); if constexpr (NT == 5 && t == 2) post<29>(sa, one); }
          if constexpr (t >= 2 && p == 1) cv = look(L, 24 + t - 2);
          if constexpr (n == 7 && !LATE) cr = look(L, 30);
          if constexpr (n == 11) cv = look(L, 11);
          if constexpr (n == NL - 2) { cv = look(L, 24 + NT - 2); if constexpr (NT == 5) cr = look(L, 29); }
          SB();
          if constexpr (!(n & 1)) wait2<(n + NB < NL ? n + NB : NL) - 2 - n + nw>(hv[n % NB], hv[(n + 1) % NB]);
          SFOR(4, i,
            constexpr int ci = 2 * (p & 1) + (i >> 1), cn = (n & ~1) + (i & 1);
            mfma_ip<(cn & 7) == 0>(acc[t & 1][ci], fr[cn & 7][ci], hv[cn % NB]);
            if constexpr (t >= 1 && (p == 1 || p == 2) && (i & 1)) *(LAS f32x4*)(L + wr + g1buf(NT, t - 1) + (2 * p - 2 + (i >> 1)) * 1024) = acc[(t - 1) & 1][2 * p - 2 + (i >> 1)];
            if constexpr (t >= 2 && 4 * p + i >= 13) fine(IC(4 * p + i - 13), t - 2);
            SB();
          );
          if constexpr (t >= 1 && p == 2) post<24 + t - 1>(sa, one);
          if constexpr ((n & 1) && n + NB < NL) { ld(IC(n + NB - 1)); ld(IC(n + NB)); }
          if constexpr (!LATE && t == NT - 1 && (p & 1) && p < NE) { ldw2(f2[p - 1], vr, p - 1); ldw2(f2[p], vr, p); }
          SB();
        );
        PRIO(LATE ? 0 : 3);
        if constexpr (NT == 5) check(L, cr, 29, 8u);
        asm volatile("s_nop 7" ::: "memory");
#pragma unroll
        for (int i = 0; i < 4; ++i) *(LAS f32x4*)(L + wr + g1buf(NT, NT - 1) + i * 1024) = acc[(NT - 1) & 1][i];
        post<24 + NT - 1>(sa, one);
        if constexpr (LATE) post<4>(sa, one);
        check(L, cv, 24 + NT - 2, 8u);
        rd(NT - 2);
        if constexpr (!LATE) { post<3>(sa, one); PRIO(3); }
        SFOR(7, s, step(s_c, NT - 2););
        if constexpr (NT == 5) put(pr[0], pr[1], pr[2], pr[3], uo);
        else if constexpr (!LATE) put(pr[0], pr[1], pr[2], pr[2], sel(lane < 48, uo, OOB));
        if constexpr (LATE) {
          const unsigned b2q = *(const LAS unsigned*)(L + ba - 64);
          await(L, 24 + NT - 1, 8u);
          PRIO(3);
          {
            f32x4 qa[8];
#pragma unroll
            for (int e = 0; e < 8; ++e) qa[e] = *(const LAS f32x4*)(L + ra - (e >> 2) * 2048 + g1buf(NT, NT - 1) + (e & 3) * 4096);
            post<5>(sa, one);
            const f32x4 s0 = (qa[0] + qa[1]) + (qa[2] + qa[3]), s1 = (qa[4] + qa[5]) + (qa[6] + qa[7]);
            f32x4 y[1] = {f32x4{fold32(s0[0], s0[2]), fold32(s0[1], s0[3]), fold32(s1[0], s1[2]), fold32(s1[1], s1[3])} + f32x4{bq[0], bq[1], lo_f(b2q), hi_f(b2q)}};
            unsigned o2[2];
            gelu_pack<1>(y, o2);
            pr[NT - 1] = o2[0]; pr[5] = o2[1];
          }
          if constexpr (NT == 4) put(pr[0], pr[1], pr[2], pr[3], uo);
          if constexpr (NT == 5) put(pr[4], pr[5], pr[4], pr[5], sel(lane < 32, ub + 4096 - g * KS_, OOB));
          else put(pr[5], pr[5], pr[5], pr[5], sel(lane < 16, ub + 3072 - KS_, OOB));
          DRAIN();
          if (arrive(lb, 6) == 3u) { await(L, 2, 4u); putflag(WS_UF + (ft * 6 + dg) * FS, lane); }
        }
        gemm2(latec, IC(LATE ? 0 : NE), f2, IC(2 * NT - 6 + LATE));
      };
      if (dg < 4) gemm1(IC(5));
      gemm1(IC(4));
  };
  if (role == 0) utile(std::false_type{});
  if (role == 1) utile(std::true_type{});
  if (role == 2) {
    u32x4 f2[8][4];
    {
      LANEVARS;
      const int vr = w2lane(lane);
        const int cq = (lane & 7) ^ (2 * ((rr >> 1) & 1)) ^ (4 * (m & 1));
        const int vq = sel(cq < 7, (8 * m + rr) * (N3_ * 2) + hp * 112 + cq * 16, OOB);
        const int va = (r0 + 16 + 8 * m + rr) * (D_ * 2) + ((lane & 7) ^ (4 * (m & 1) + (rr >> 1))) * 16;
        const int so_ = WS_ST + min(max(r0 - 16 + min(64 * m + lane, 207), 0), S_ - 1) * 16;
        auto pcs = [&](int pp, int from, int to) AI {
          const bool wr = rotq >= 7 - pp;
#pragma unroll
          for (int n = from; n < to; ++n) {
            if (n < 6) piece(false, vq, (pp + 2) % 3, fs + sel(wr, (pp - 7) * 64 * N3_ * 2, pp * 64 * N3_ * 2), n);
            else piece(true, va, (pp + 2) % 3, rotq * 128 + sel(wr, (pp - 7) * 128, pp * 128), n - 6);
          }
        };
        pcs(1, 0, LA);
        gettag();
        WAITVM(LA);
        BAR();
        auto warm = [&](int e) AI {
          const auto rs_wo = __builtin_amdgcn_make_buffer_rsrc((void*)wo, 0, D_ * D_ * 2, RSRC_W3);
          const int j = idx - 28;
          if (e < 3) touch(rs_w2, (lane >> 2) * (S_ * 2) + (lane & 3) * 128, (448 * m + 256 + 48 * j + 16 * e) * (S_ * 2) + (xcd ^ 1) * 512, lb + L_DUMMY);
          else touch(rs_wo, lane * 128, ((e - 3) * 16 + j * 4 + m) * 8192, lb + L_DUMMY);
        };
        u32x4 sg;
        SFOR(6, n0,
          constexpr int n = n0 + 1, open = n < 5 ? LA : n == 5 ? 12 : 4 * NA;
          if constexpr (n < 6) pcs(n, LA, 13);
          if constexpr (n == 3) asm volatile("buffer_load_dwordx4 %0, %1, %2, 0 offen sc1" : "=v"(sg) : "v"(so_), "s"(rs_ws) : "memory");
          if constexpr (n < 6) pcs(n + 1, 0, n == 5 ? 13 : LA);
          if constexpr (n == 4) {
            asm volatile("" : "+v"(sg));
            _Pragma("unroll 1")
            for (unsigned spins = 0; spins < SPIN_CAP; ++spins) {
              if (__all((sg.y == (unsigned)tag) & (sg.w == (unsigned)(tag >> 32)) & (sg.x == sg.z))) break;
              __builtin_amdgcn_s_sleep(4);
              asm volatile("buffer_load_dwordx4 %0, %1, %2, 0 offen sc1\n\ts_waitcnt vmcnt(0)" : "=v"(sg) : "v"(so_), "s"(rs_ws) : "memory");
            }
            *(LAS float*)(L + A_MEAN + (64 * m + lane) * 4) = -lo_f(sg.x);
            *(LAS float*)(L + A_RSTD + (64 * m + lane) * 4) = __builtin_amdgcn_rcpf(hi_f(sg.x));
            WAITLG();
          }
          if constexpr (n == 6) { SFOR(NA, e, ldw2(f2[e], vr, e);); }
          if (n >= 5 && idx >= 28) {
            warm(2 * n - 10); warm(2 * n - 9);
            WAITVM(open + 2);
          } else {
            WAITVM(open);
          }
          BAR();
        );
        sig(lb, 3);
        if (idx >= 28) {
          touch(rs_w2, lane * (S_ * 2), (448 * m + 64 * (idx - 28)) * (S_ * 2) + (xcd ^ 1) * 512 + 384, lb + L_DUMMY);
#pragma unroll
          for (int e = 4; e < 7; ++e) warm(e);
          DRAIN();
          SFOR(NA, p, wait4<0>(f2[p]););
        }
    }
    if (idx < 28) gemm2(std::false_type{}, IC(NA), f2, IC(0));
    FINISH();
  } else {
        auto att = [&](auto jc) AI {
          constexpr int J = decltype(jc)::value, NH = J ? 2 : 4, H0 = J ? 0 : 2;
          const int H = m & 1;
          LANEVARS;
          f32x4 qa[6][6], qh[NH];
          int wa[2], xa[2][2];
#pragma unroll
          for (int i = 0; i < 2; ++i) {
            const int by = sel((i == 1) & (p4 == 3), 112, 56 * H + 32 * i + 8 * p4);
            wa[i] = A_W + (8 * g + q4) * 128 + (((by >> 4) ^ (2 * ((q4 >> 1) | ((g & 1) << 1)))) << 4) + (by & 8);
          }
          int wb[2] = {wa[0] + A_WS + 12288, wa[1] + A_WS + 12288};
#pragma unroll
          for (int i = 0; i < 4; ++i) xa[i >> 1][i & 1] = (i >> 1) * A_X1 + c * 128 + (((4 * (i & 1) + g) ^ ((c >> 1) & 7)) << 4);
          asm volatile("" : "+v"(wa[0]), "+v"(wa[1]), "+v"(wb[0]), "+v"(wb[1]), "+v"(xa[0][0]), "+v"(xa[0][1]), "+v"(xa[1][0]), "+v"(xa[1][1]));
          u32x4 wf[6], xf[7];
          u32x2 lo;
          auto rdx = [&](int k, int t) AI {
            const int sg = ((k >> 1) + 2) % 3;
            xf[t] = *(const LAS u32x4*)(L + xa[sg ? 1 : 0][k & 1] + (sg == 2 ? A_XS : 0) + (t == 6 ? 0 : (1 + 6 * J + t) * 2048));
          };
          auto rdw = [&](int k, int i, int hf) AI {
            const int o = (((k >> 1) + 2) % 3) * 2 + (k & 1);
            const LAS char* p = L + (o < 3 ? wa[i & 1] + o * 12288 : wb[i & 1] + (o - 3) * 12288) + (i >> 1) * 4096;
            if (hf) { const u32x2 hi = tr_read(p + 512); wf[i] = u32x4{lo.x, lo.y, hi.x, hi.y}; }
            else lo = tr_read(p);
          };
          const bool c0 = c == 0;
          int exa = lb + A_X1 + H * 4096 + lane * 16;
          int ta = lb + A_MEAN + (96 * J + c) * 4, tk = lb + A_RSTD + (96 * J + 4 * g + (c & 1)) * 4, tc = lb + A_CS + (H * 96 + 4 * g) * 4, tv = lb + A_CS + (H * 96 + 64 + c) * 4;
          int sa = sel(lane == 0, lb + L_CNT, OOB);
          unsigned one = 1u;
          f32x2 mk[2][2];
          {
            const int d0 = 16 + (c & 14) - 4 * g, hi = min(r0 + c, 14);
#pragma unroll
            for (int e = 0; e < 4; ++e) {
              const int dk = d0 - 16 * (e >> 1) - 2 * (e & 1);
              mk[1][e >> 1][e & 1] = sel((unsigned)dk <= 14u, 0.f, -3.0e38f);
              mk[0][e >> 1][e & 1] = sel((unsigned)dk <= (unsigned)hi, 0.f, -3.0e38f);
            }
          }
          PINV(exa); PINV(ta); PINV(tk); PINV(tc); PINV(tv); PINV(sa); PINV(one); PINV(mk[1][0]); PINV(mk[1][1]);
          if constexpr (J) { mk[0][0] = mk[1][0]; mk[0][1] = mk[1][1]; } else { PINV(mk[0][0]); PINV(mk[0][1]); }
          float mc[7], rc[6];
          f32x2 rk[7];
          auto tab = [&](int e) AI {
            const int t = e / 3;
            if (e % 3 == 0) mc[t] = *(const LAS float*)(LA_(ta) + 64 * t);
            if (e % 3 == 1) { const LAS float* p = (const LAS float*)(LA_(tk) + 64 * t); rk[t] = f32x2{p[0], p[2]}; }
            if (e % 3 == 2) rc[t] = *(const LAS float*)(LA_(ta) + A_RSTD - A_MEAN + 64 * (t + 1));
          };
          BAR();
          PRIO(2);
#pragma unroll
          for (int t = 0; t < 7; ++t) rdx(0, t);
#pragma unroll
          for (int i = 0; i < 10; ++i) rdw(0, i >> 1, i & 1);
          gettag();
          SB();
          SFOR(14, k,
            SFOR(6, ci,
              constexpr int i = (k == 13 && J == 0) ? (ci + 2) % 6 : ci;
              SFOR(7, t,
                if constexpr (t < 6) {
                  if constexpr (i < 4) mfma_ip<k == 0>(qa[t][i], wf[i], xf[t]); else mfma_ip<k == 0>(qa[t][i], xf[t], wf[i]);
                } else if constexpr (i >= H0 && i < H0 + NH) {
                  if constexpr (i < 4) mfma_ip<k == 0>(qh[i - H0], wf[i], xf[6]); else mfma_ip<k == 0>(qh[i - H0], xf[6], wf[i]);
                  if constexpr (k < 13 && i == H0 + NH - 1 && J == 0) rdx(k + 1, 6);
                }
                if constexpr (ci == 0 && t < 2) rdw(k, 5, t);
                if constexpr (ci == (k == 13 ? 0 : H0) && t == 2) { SFOR(4, j, xf[6][j] = sel(c0, 0x3f803f80u, xf[6][j]);); asm volatile("" : "+v"(xf[6])); }
                if constexpr ((k & 1) && k < 13 && ci == 1 && t == 1) { WAITLG(); BAR(); }
                if constexpr (k < 13 && ci >= 1 && (t == 2 || t == 3)) rdw(k + 1, ci - 1, t - 2);
                if constexpr (k < 13 && ci == 5 && t < 6) rdx(k + 1, t);
                if constexpr (k < 13 && J == 1 && ci == 2 && t == 0) rdx(k + 1, 6);
                if constexpr (k == 13 && t == 2 && ci >= 2 && ci < NH + 2) {
                  constexpr int ip = H0 + ci - 2;
                  if constexpr (ip < 4) *(LAS f32x4*)(LA_(sel(c0, tc, lb + L_DUMMY)) + 64 * ip) = qh[ci - 2];
                  else *(LAS float*)(LA_(sel(g == 0, tv, lb + L_DUMMY)) + 64 * (ip - 4)) = qh[ci - 2][0];
                }
                if constexpr (k == 13 && J == 0 && t == 4 && ci >= 2) *(LAS f32x4*)(LA_(exa) + (ci - 2) * 1024) = qa[5][ci];
                if constexpr (k == 13 && (J ? ci == 3 && t == 2 : ci == 5 && t == 4)) { SB(); post<4>(sa, one); }
                if constexpr (k == 13 && ci >= 2 && t < 5) tab(5 * (ci - 2) + t);
                SB();
              );
            );
          );
          await(L, 4, 4u);
          f32x4 c4[4], ex[4];
          float cv[2];
#pragma unroll
          for (int i = 0; i < 4; ++i) c4[(i + 2) & 3] = *(const LAS f32x4*)(LA_(tc) + 64 * ((i + 2) & 3));
#pragma unroll
          for (int i = 0; i < 4; ++i) { if constexpr (J) ex[i] = *(const LAS f32x4*)(LA_(exa) + i * 1024); else ex[i] = qh[i & (NH - 1)]; }
#pragma unroll
          for (int i = 0; i < 2; ++i) cv[i] = *(const LAS float*)(LA_(tv) + 64 * i);
          SB();
          post<5>(sa, one);
          const bool odd = ta & 4;
          f32x2 lg[6][2];
#pragma unroll
          for (int u = 0; u < 6; ++u) {
            const float sc = rc[u] * (0.18898223650461362f * 1.4426950408889634f);
            lg[u][0] = rk[u] * sc; lg[u][1] = rk[u + 1] * sc;
            PINV(lg[u][0]); PINV(lg[u][1]);
          }
          int tm = (tk & ~4) + A_MEAN - A_RSTD;
          int ov = exa - lb + WS_O - A_X1 + ((12 * rg + 6 * J) * 16 + 2 * hp - 3 * H) * 1024;
          unsigned p0 = sel(odd, 0x01000c0cu, 0x0c0c0100u), p1 = sel(odd, 0x03020c0cu, 0x0c0c0302u);
          PINV(tm); PINV(ov); PINV(p0); PINV(p1);
          SB();
          auto pk4 = [&](f32x4 v) AI { return u32x2{pack2(v[0], v[1]), pack2(v[2], v[3])}; };
          auto frag = [&](f32x4 a, f32x4 b, float mu, int i) AI {
            const u32x2 l = pk4(c4[i] * mu + a), h = pk4(c4[i + 1] * mu + b);
            return u32x4{l.x, l.y, h.x, h.y};
          };
          f32x4 s0[6], s1[6], m4[7];
          u32x4 kf[7], qf[6];
          kf[0] = frag(ex[0], ex[1], mc[0], 2);
          qf[0] = frag(qa[0][0], qa[0][1], mc[1], 0);
#pragma unroll
          for (int u = 0; u < 6; ++u) {
            kf[u + 1] = frag(qa[u][2], qa[u][3], mc[u + 1], 2);
            SB();
            s0[u] = mfma16(kf[u], qf[u], Z4);
            m4[u] = *(const LAS f32x4*)(LA_(tm) + 64 * u);
            SB();
            if (u < 5) qf[u + 1] = frag(qa[u + 1][0], qa[u + 1][1], mc[u + 2], 0); else m4[6] = *(const LAS f32x4*)(LA_(tm) + 64 * 6);
            SB();
            s1[u] = mfma16(kf[u + 1], qf[u], Z4);
            SB();
          }
          float mx[6], inv[6];
#pragma unroll
          for (int u = 0; u < 6; ++u) {
#pragma unroll
            for (int e = 0; e < 2; ++e) {
              const f32x4 s = e ? s1[u] : s0[u];
              lg[u][e] = __builtin_elementwise_fma(f32x2{sel(odd, s[1], s[0]), sel(odd, s[3], s[2])}, lg[u][e], mk[u ? 1 : 0][e]);
            }
            mx[u] = max2(max2(lg[u][0][0], lg[u][0][1]), max2(lg[u][1][0], lg[u][1][1]));
          }
          u32x16 vv[2];
          auto pkv = [&](int t) AI {
#pragma unroll
            for (int i = 0; i < 2; ++i) { u32x2 y = pk4(m4[t] * cv[i] + (t ? qa[t - 1][4 + i] : ex[2 + i])); PINV(y); vv[i][2 * t] = y.x; vv[i][2 * t + 1] = y.y; }
          };
          across6(mx, [](float a, float b) AI { return max2(a, b); }, [](float a) AI { return a; }, pkv);
          pkv(4); pkv(5); pkv(6);
          PINV(vv[0]); PINV(vv[1]);
          SB();
#pragma unroll
          for (int u = 0; u < 6; ++u) {
#pragma unroll
            for (int e = 0; e < 2; ++e) {
              lg[u][e] -= mx[u];
              lg[u][e] = f32x2{__builtin_amdgcn_exp2f(lg[u][e][0]), __builtin_amdgcn_exp2f(lg[u][e][1])};
            }
            const f32x2 t = lg[u][0] + lg[u][1];
            inv[u] = t[0] + t[1];
          }
          across6(inv, [](float a, float b) AI { return a + b; }, [](float a) AI { return __builtin_amdgcn_rcpf(a); },
                  [&](int q) AI { for (int e = 3 * q; e < 3 * q + 3; ++e) lg[e >> 1][e & 1] *= rk[(e >> 1) + (e & 1)]; });
          f32x4 o0[6], o1[6];
          auto sto = [&](int u) AI {
            const u32x2 l = pk4(o0[u]), h = pk4(o1[u]);
            __builtin_amdgcn_raw_buffer_store_b128(u32x4{l.x, l.y, h.x, h.y}, rs_ws, ov + u * 16384, 0, AUX_SC1);
          };
          PRIO(3);
          SFOR(6, u,
            const f32x2 a = lg[u][0] * inv[u], b = lg[u][1] * inv[u];
            const unsigned wa_ = pack2(a[0], a[1]), wb_ = pack2(b[0], b[1]);
            const u32x4 pp = {__builtin_amdgcn_perm(wa_, wa_, p0), __builtin_amdgcn_perm(wa_, wa_, p1), __builtin_amdgcn_perm(wb_, wb_, p0), __builtin_amdgcn_perm(wb_, wb_, p1)};
            if constexpr (u >= 2) sto(u - 2);
            SB();
            o0[u] = mfma16(__builtin_shufflevector(vv[0], vv[0], 2 * u, 2 * u + 1, 2 * u + 2, 2 * u + 3), pp, Z4);
            o1[u] = mfma16(__builtin_shufflevector(vv[1], vv[1], 2 * u, 2 * u + 1, 2 * u + 2, 2 * u + 3), pp, Z4);
            SB();
          );
          sto(4); sto(5);
          SB();
          };
        if (m < 2) att(std::integral_constant<int, 0>{}); else att(std::integral_constant<int, 1>{});
    LANEVARS;
    DRAIN();
    if (arrive(lb, 6) == 3u) putflag(WS_OF + (rg * 8 + hp) * FS, lane);
    if (idx < 28) { u32x4 f2[6][4]; gemm2(std::true_type{}, IC(0), f2, IC(1)); }
    if (bid == EPB) {
      __builtin_amdgcn_s_sleep(64);
      poll_flags<16>((const GAS u64*)(ws + WS_UF + (m * 64 + lane) * FS), tag);
      if (arrive(lb, 8) == 3u) putflag(WS_E, lane);
    }
  }
  FINISH();
}

extern "C" void kernel_launch(void* const* d_in, const int* in_sizes, int n_in,
                              void* d_out, int out_size, void* d_ws, size_t ws_size,
                              hipStream_t stream) {
  (void)in_sizes; (void)n_in; (void)out_size; (void)ws_size;
  (void)hipFuncSetAttribute((const void*)fused_block, hipFuncAttributeMaxDynamicSharedMemorySize, LDS_BYTES);
  fused_block<<<256, 512, LDS_BYTES, stream>>>((const u16*)d_in[0], (const u16*)d_in[1], (const u16*)d_in[2],
                                               (const u16*)d_in[4], (const u16*)d_in[5], (const u16*)d_in[6],
                                               (const u16*)d_in[7], (u16*)d_out, (char*)d_ws);
}
```
